```python
import torch

def _softmax(x, axis=-1):
    return torch.softmax(x, dim=axis)
def _gelu(x):
    return torch.nn.functional.gelu(x, approximate="tanh")
def _silu(x):
    return torch.nn.functional.silu(x)
def _sigmoid(x):
    return torch.sigmoid(x)
def _layernorm(x, axis=-1, eps=1e-5):
    m = x.mean(dim=axis, keepdim=True)
    v = x.var(dim=axis, keepdim=True, unbiased=False)
    return (x - m) / torch.sqrt(v + eps)
def _scatter_add(tgt, idx, src, axis=0):
    return tgt.clone().index_add_(axis, idx, src)
def _segment_sum(src, seg, n):
    out = torch.zeros((n,) + src.shape[1:], dtype=src.dtype)
    return out.index_add_(0, seg, src)
def _pad(x, pw):
    flat = tuple(v for pair in reversed(pw) for v in pair)
    return torch.nn.functional.pad(x, flat)

kernel_name = 'standalone__gqa_block_cached__near_ridge'

def setup_inputs():

    _w = (lambda a: (lambda t: t.to(torch.bfloat16) if t.is_floating_point() else t)(torch.as_tensor(a)))
    B, Sq, D = 16, 192, 512
    Hq, Hkv, Sk, Dh = 32, 32, 512, 128
    assert Hq % Hkv == 0, (Hq, Hkv)
    return {
        "x": _w((lambda *s: torch.randn(*s, dtype=torch.bfloat16) * 0.8)(B, Sq, D)),
        "k_cache": _w((lambda *s: torch.randn(*s, dtype=torch.bfloat16) * 0.8)(B, Hkv, Sk, Dh)),
        "v_cache": _w((lambda *s: torch.randn(*s, dtype=torch.bfloat16) * 0.05)(B, Hkv, Sk, Dh)),
        "wq": _w((lambda *s: torch.randn(*s, dtype=torch.bfloat16) * 0.8)(D, Hq * Dh) * (D ** -0.5)),
        "wo": _w((lambda *s: torch.randn(*s, dtype=torch.bfloat16) * 0.05)(Hq * Dh, D) * ((Hq * Dh) ** -0.5)),
    }

def reference(x, k_cache, v_cache, wq, wo):
    B, Sq, D = x.shape
    Hq, Dh = 32, 128
    Hkv = k_cache.shape[1]
    rep = Hq // Hkv
    q = (lambda x, *d: x.permute(*d))((x @ wq).reshape(B, Sq, Hq, Dh), 0, 2, 1, 3)
    k = (lambda x, n, axis: torch.repeat_interleave(x, n, dim=axis))(k_cache, rep, axis=1)
    v = (lambda x, n, axis: torch.repeat_interleave(x, n, dim=axis))(v_cache, rep, axis=1)
    scores = torch.einsum("bhqd,bhkd->bhqk", q, k) * (Dh ** -0.5)
    p = _softmax(scores, axis=-1)
    o = torch.einsum("bhqk,bhkd->bhqd", p, v)
    return (lambda x, *d: x.permute(*d))(o, 0, 2, 1, 3).reshape(B, Sq, Hq * Dh) @ wo
```

```cpp
#include <hip/hip_runtime.h>
#include <hip/hip_bf16.h>
typedef unsigned short u16;
__device__ __forceinline__ float bf2f(u16 v){ return __uint_as_float(((unsigned)v)<<16); }
__device__ __forceinline__ u16 f2bf(float f){ unsigned u=__float_as_uint(f); u += 0x7FFFu + ((u>>16)&1u); return (u16)(u>>16); }
constexpr int B=16,SQ=192,D=512,H=32,SK=512,DH=128;
__global__ void gemm_naive(const u16* A,const u16* W,u16* C,int M,int N,int K){
  __shared__ float As[16][17], Ws[16][17];
  int tx=threadIdx.x&15, ty=threadIdx.x>>4;
  int row=blockIdx.y*16+ty, col=blockIdx.x*16+tx; float acc=0;
  for(int k0=0;k0<K;k0+=16){
    As[ty][tx]=bf2f(A[(long)row*K+k0+tx]); Ws[ty][tx]=bf2f(W[(long)(k0+ty)*N+col]);
    __syncthreads();
    for(int k=0;k<16;k++) acc+=As[ty][k]*Ws[k][tx];
    __syncthreads();
  }
  C[(long)row*N+col]=f2bf(acc);
}
__global__ void attn_naive(const u16* Q,const u16* Kc,const u16* Vc,u16* O){
  __shared__ float q[DH], p[SK], red[128];
  int qi=blockIdx.x, h=blockIdx.y, b=blockIdx.z, t=threadIdx.x;
  q[t]=bf2f(Q[((long)(b*SQ+qi))*(H*DH)+h*DH+t]); __syncthreads();
  const u16* Kb=Kc+((long)(b*H+h))*SK*DH; const u16* Vb=Vc+((long)(b*H+h))*SK*DH;
  float mx=-1e30f;
  for(int k=t;k<SK;k+=128){ float s=0; for(int d=0;d<DH;d++) s+=q[d]*bf2f(Kb[k*DH+d]); s*=0.08838834764831845f; p[k]=s; mx=fmaxf(mx,s);}
  red[t]=mx; __syncthreads(); for(int o=64;o>0;o>>=1){ if(t<o) red[t]=fmaxf(red[t],red[t+o]); __syncthreads(); } mx=red[0]; __syncthreads();
  float sm=0; for(int k=t;k<SK;k+=128){ float e=__expf(p[k]-mx); p[k]=e; sm+=e; }
  red[t]=sm; __syncthreads(); for(int o=64;o>0;o>>=1){ if(t<o) red[t]+=red[t+o]; __syncthreads(); } sm=red[0];
  float acc=0; for(int k=0;k<SK;k++) acc+=p[k]*bf2f(Vb[k*DH+t]);
  O[((long)(b*SQ+qi))*(H*DH)+h*DH+t]=f2bf(acc/sm);
}
extern "C" void kernel_launch(void* const* d_in, const int* in_sizes, int n_in, void* d_out, int out_size, void* d_ws, size_t ws_size, hipStream_t stream){
  const u16* x=(const u16*)d_in[0]; const u16* kc=(const u16*)d_in[1]; const u16* vc=(const u16*)d_in[2]; const u16* wq=(const u16*)d_in[3]; const u16* wo=(const u16*)d_in[4];
  u16* Q=(u16*)d_ws; u16* O=Q+(long)B*SQ*H*DH;
  gemm_naive<<<dim3(H*DH/16,B*SQ/16),256,0,stream>>>(x,wq,Q,B*SQ,H*DH,D);
  attn_naive<<<dim3(SQ,H,B),128,0,stream>>>(Q,kc,vc,O);
  gemm_naive<<<dim3(D/16,B*SQ/16),256,0,stream>>>(O,wo,(u16*)d_out,B*SQ,D,H*DH);
}
```

```cpp
#include <hip/hip_runtime.h>
__global__ __launch_bounds__(256, 1) void gqa_block_kernel(const void* x, const void* k, const void* v, const void* wq, const void* wo, void* ws, void* out) {
  const unsigned tid = threadIdx.x, bid = blockIdx.x;
  asm volatile(
"v_mov_b32 v251, %[tid]\n"
"s_load_dwordx8 s[4:11], %[ka], 0x0\n"
"s_load_dwordx4 s[12:15], %[ka], 0x20\n"
"s_load_dwordx2 s[16:17], %[ka], 0x30\n"
"s_mov_b32 s20, %[bid]\n"
"s_bfe_u32 s21, s20, 0x10002\n"
"s_lshr_b32 s23, s20, 5\n"
"s_lshl3_add_u32 s21, s21, s23\n"
"s_bfe_u32 s24, s20, 0x20003\n"
"s_and_b32 s22, s20, 3\n"
"s_lshl2_add_u32 s22, s22, s24\n"
"s_lshl1_add_u32 s23, s23, s24\n"
"s_and_b32 s23, s23, 7\n"
"s_lshl_b32 s88, s23, 19\n"
"s_lshl_b32 s89, s23, 7\n"
"v_lshrrev_b32 v226, 6, v251\n"
"v_and_b32 v230, 63, v251\n"
"v_readfirstlane_b32 s76, v226\n"
"s_nop 3\n"
"s_lshl_b32 s78, s76, 10\n"
"s_mul_i32 s87, s76, 0x3000\n"
"s_lshr_b32 s26, s76, 1\n"
"s_and_b32 s27, s76, 1\n"
"v_bfe_u32 v226, v230, 2, 3\n"
"v_lshrrev_b32 v227, 5, v230\n"
"v_lshlrev_b32 v227, 6, v227\n"
"v_and_b32 v228, 3, v230\n"
"v_bfe_u32 v229, v230, 4, 1\n"
"v_lshlrev_b32 v229, 1, v229\n"
"v_xor_b32 v231, v228, v229\n"
"s_mul_i32 s28, s76, 48\n"
"v_add_u32 v229, s28, v226\n"
"v_lshl_add_u32 v222, v231, 4, v227\n"
"v_lshl_add_u32 v222, v229, 10, v222\n"
"s_lshl_b32 s28, s27, 7\n"
"v_add_u32 v227, s28, v227\n"
"s_lshl_b32 s28, s26, 3\n"
"v_add_u32 v229, s28, v226\n"
"v_lshl_add_u32 v221, v229, 13, v227\n"
"v_lshl_add_u32 v221, v228, 4, v221\n"
"s_waitcnt lgkmcnt(0)\n"
"s_mul_i32 s24, s21, 0x30000\n"
"s_mov_b64 s[36:37], s[4:5]\n"
"s_add_u32 s36, s36, s24\n"
"s_addc_u32 s37, s37, 0\n"
"s_and_b32 s37, s37, 0xffff\n"
"s_mov_b32 s38, 0x30000\n"
"s_mov_b32 s39, 0x20000\n"
"s_lshl_b32 s24, s22, 8\n"
"s_sub_u32 s25, 0x400000, s24\n"
"s_mov_b64 s[40:41], s[10:11]\n"
"s_add_u32 s40, s40, s24\n"
"s_addc_u32 s41, s41, 0\n"
"s_and_b32 s41, s41, 0xffff\n"
"s_mov_b32 s42, s25\n"
"s_mov_b32 s43, 0x20000\n"
"s_add_u32 m0, s87, 0x1c000\n"
"s_xor_b32 s81, s89, 0x0\n"
"buffer_load_dwordx4 v222, s[36:39], s81 offen lds\n"
"s_add_u32 m0, s87, 0x1c400\n"
"s_xor_b32 s81, s89, 0x2000\n"
"buffer_load_dwordx4 v222, s[36:39], s81 offen lds\n"
"s_add_u32 m0, s87, 0x1c800\n"
"s_xor_b32 s81, s89, 0x4000\n"
"buffer_load_dwordx4 v222, s[36:39], s81 offen lds\n"
"s_add_u32 m0, s87, 0x1cc00\n"
"s_xor_b32 s81, s89, 0x6000\n"
"buffer_load_dwordx4 v222, s[36:39], s81 offen lds\n"
"s_add_u32 m0, s87, 0x1d000\n"
"s_xor_b32 s81, s89, 0x8000\n"
"buffer_load_dwordx4 v222, s[36:39], s81 offen lds\n"
"s_add_u32 m0, s87, 0x1d400\n"
"s_xor_b32 s81, s89, 0xa000\n"
"buffer_load_dwordx4 v222, s[36:39], s81 offen lds\n"
"s_add_u32 m0, s78, 0x0\n"
"s_xor_b32 s80, s88, 0x0\n"
"buffer_load_dwordx4 v221, s[40:43], s80 offen lds\n"
"s_add_u32 m0, s78, 0x1000\n"
"s_xor_b32 s80, s88, 0x20000\n"
"buffer_load_dwordx4 v221, s[40:43], s80 offen lds\n"
"s_add_u32 m0, s78, 0x2000\n"
"s_xor_b32 s80, s88, 0x40000\n"
"buffer_load_dwordx4 v221, s[40:43], s80 offen lds\n"
"s_add_u32 m0, s78, 0x3000\n"
"s_xor_b32 s80, s88, 0x60000\n"
"buffer_load_dwordx4 v221, s[40:43], s80 offen lds\n"
"s_add_u32 m0, s87, 0x1d800\n"
"s_xor_b32 s81, s89, 0x80\n"
"buffer_load_dwordx4 v222, s[36:39], s81 offen lds\n"
"s_add_u32 m0, s87, 0x1dc00\n"
"s_xor_b32 s81, s89, 0x2080\n"
"buffer_load_dwordx4 v222, s[36:39], s81 offen lds\n"
"s_add_u32 m0, s87, 0x1e000\n"
"s_xor_b32 s81, s89, 0x4080\n"
"buffer_load_dwordx4 v222, s[36:39], s81 offen lds\n"
"s_add_u32 m0, s87, 0x1e400\n"
"s_xor_b32 s81, s89, 0x6080\n"
"buffer_load_dwordx4 v222, s[36:39], s81 offen lds\n"
"s_add_u32 m0, s87, 0x1e800\n"
"s_xor_b32 s81, s89, 0x8080\n"
"buffer_load_dwordx4 v222, s[36:39], s81 offen lds\n"
"s_add_u32 m0, s87, 0x1ec00\n"
"s_xor_b32 s81, s89, 0xa080\n"
"buffer_load_dwordx4 v222, s[36:39], s81 offen lds\n"
"s_add_u32 m0, s78, 0x4000\n"
"s_xor_b32 s80, s88, 0x80000\n"
"buffer_load_dwordx4 v221, s[40:43], s80 offen lds\n"
"s_add_u32 m0, s78, 0x5000\n"
"s_xor_b32 s80, s88, 0xa0000\n"
"buffer_load_dwordx4 v221, s[40:43], s80 offen lds\n"
"s_add_u32 m0, s78, 0x6000\n"
"s_xor_b32 s80, s88, 0xc0000\n"
"buffer_load_dwordx4 v221, s[40:43], s80 offen lds\n"
"s_add_u32 m0, s78, 0x7000\n"
"s_xor_b32 s80, s88, 0xe0000\n"
"buffer_load_dwordx4 v221, s[40:43], s80 offen lds\n"
"s_lshr_b32 s28, s20, 3\n"
"s_lshl2_add_u32 s28, s28, s76\n"
"s_and_b32 s28, s28, 7\n"
"s_lshl_b32 s28, s28, 13\n"
"v_lshlrev_b32 v234, 7, v230\n"
"v_add_u32 v234, s28, v234\n"
"v_min_u32 v234, Lend_%=-Lpc_%=-4, v234\n"
"v_and_b32 v234, -4, v234\n"
"s_getpc_b64 s[24:25]\n"
"Lpc_%=:\n"
"global_load_dword v253, v234, s[24:25]\n"
"s_lshl_b32 s24, s21, 5\n"
"s_add_u32 s24, s24, s22\n"
"s_lshl_b32 s24, s24, 17\n"
"s_mov_b64 s[44:45], s[6:7]\n"
"s_add_u32 s44, s44, s24\n"
"s_addc_u32 s45, s45, 0\n"
"s_and_b32 s45, s45, 0xffff\n"
"s_mov_b32 s46, 0x220000\n"
"s_mov_b32 s47, 0x20000\n"
"s_mov_b64 s[48:49], s[8:9]\n"
"s_add_u32 s48, s48, s24\n"
"s_addc_u32 s49, s49, 0\n"
"s_and_b32 s49, s49, 0xffff\n"
"s_mov_b32 s50, 0x220000\n"
"s_mov_b32 s51, 0x20000\n"
"s_bfe_u32 s100, s20, 0x40003\n"
"s_lshl_b32 s100, s100, 13\n"
"s_lshl_b32 s77, s26, 12\n"
"s_lshl_b32 s28, s27, 10\n"
"s_add_u32 s77, s77, s28\n"
"v_lshl_add_u32 v228, v231, 4, v227\n"
"s_lshl_b32 s28, s26, 4\n"
"v_add_u32 v229, s28, v226\n"
"v_lshl_add_u32 v220, v229, 8, v228\n"
"v_lshl_add_u32 v224, v229, 10, v228\n"
"s_mov_b64 s[68:69], s[14:15]\n"
"s_add_u32 s68, s68, 0x3400000\n"
"s_addc_u32 s69, s69, 0\n"
"s_and_b32 s69, s69, 0xffff\n"
"s_mov_b32 s70, 0x3000\n"
"s_mov_b32 s71, 0x20000\n"
"s_cmp_eq_u32 s76, 0\n"
"s_cselect_b32 s70, 0x3000, 0\n"
"s_cselect_b32 s25, 8, 0\n"
"s_cselect_b64 s[94:95], -1, 0\n"
"v_mov_b32 v242, 0\n"
"buffer_load_dwordx2 v[246:247], v242, s[68:71], 0 offen sc1\n"
"s_lshr_b32 s30, s22, 2\n"
"s_and_b32 s31, s22, 3\n"
"s_lshl4_add_u32 s24, s21, s22\n"
"s_lshl_b32 s24, s24, 3\n"
"s_and_b32 s92, s24, 0xffffffe0\n"
"s_add_u32 s92, s92, 0x800\n"
"s_and_b32 s93, s24, 0xffffff9f\n"
"s_add_u32 s93, s93, 0x1800\n"
"s_add_u32 s24, s24, 0x3400800\n"
"s_mov_b64 s[64:65], s[14:15]\n"
"s_add_u32 s64, s64, s24\n"
"s_addc_u32 s65, s65, 0\n"
"s_and_b32 s65, s65, 0xffff\n"
"s_mov_b32 s66, s25\n"
"s_mov_b32 s67, 0x20000\n"
"s_lshl_b32 s24, s31, 8\n"
"s_sub_u32 s29, 0x400000, s24\n"
"s_mov_b64 s[52:53], s[12:13]\n"
"s_add_u32 s52, s52, s24\n"
"s_addc_u32 s53, s53, 0\n"
"s_and_b32 s53, s53, 0xffff\n"
"s_mov_b32 s54, s29\n"
"s_mov_b32 s55, 0x20000\n"
"s_mov_b64 s[56:57], s[14:15]\n"
"s_and_b32 s57, s57, 0xffff\n"
"s_mov_b32 s58, 0x2000000\n"
"s_mov_b32 s59, 0x20000\n"
"s_mov_b64 s[60:61], s[14:15]\n"
"s_add_u32 s60, s60, 0x2000000\n"
"s_addc_u32 s61, s61, 0\n"
"s_and_b32 s61, s61, 0xffff\n"
"s_mov_b32 s62, 0x1200000\n"
"s_mov_b32 s63, 0x20000\n"
"s_lshl_b32 s24, s21, 5\n"
"s_add_u32 s24, s24, s22\n"
"s_lshl_b32 s24, s24, 16\n"
"s_mul_i32 s28, s76, 0xc00\n"
"s_add_u32 s82, s24, s28\n"
"s_lshl2_add_u32 s24, s21, s30\n"
"s_mul_i32 s24, s24, 0xc000\n"
"s_lshl_b32 s28, s31, 8\n"
"s_add_u32 s24, s24, s28\n"
"s_sub_u32 s28, 0xc000, s28\n"
"s_mov_b64 s[72:73], s[16:17]\n"
"s_add_u32 s72, s72, s24\n"
"s_addc_u32 s73, s73, 0\n"
"s_and_b32 s73, s73, 0xffff\n"
"s_mov_b32 s74, s28\n"
"s_mov_b32 s75, 0x20000\n"
"s_mov_b32 s84, s21\n"
"s_mov_b32 s85, 0x3e0293ee\n"
"s_mov_b32 s86, 0x4e800000\n"
"v_lshlrev_b32 v223, 4, v230\n"
"v_lshrrev_b32 v226, 4, v230\n"
"v_bfe_u32 v227, v230, 2, 2\n"
"v_and_b32 v228, 3, v230\n"
"v_lshlrev_b32 v217, 11, v226\n"
"v_lshl_add_u32 v217, v227, 6, v217\n"
"v_lshl_add_u32 v217, v228, 4, v217\n"
"v_lshrrev_b32 v229, 1, v226\n"
"v_lshlrev_b32 v211, 11, v229\n"
"v_and_b32 v229, 1, v226\n"
"v_mul_u32_u24 v229, 0x120, v229\n"
"v_add_u32 v211, v211, v229\n"
"v_lshl_add_u32 v211, v227, 6, v211\n"
"v_lshl_add_u32 v211, v228, 3, v211\n"
"v_add_u32 v211, 0x2000, v211\n"
"v_xor_b32 v214, 32, v211\n"
"v_and_b32 v229, 7, v230\n"
"v_lshlrev_b32 v208, 6, v229\n"
"v_bfe_u32 v229, v230, 2, 1\n"
"v_lshlrev_b32 v229, 1, v229\n"
"v_xor_b32 v229, v229, v226\n"
"v_lshl_add_u32 v208, v229, 4, v208\n"
"v_bfe_u32 v229, v230, 3, 1\n"
"v_lshl_add_u32 v225, v229, 10, v208\n"
"v_lshl_add_u32 v208, v229, 11, v208\n"
"v_add_u32 v225, s87, v225\n"
"v_add_u32 v225, 0x1c000, v225\n"
"v_add_u32 v209, 0x10000, v208\n"
"v_add_u32 v210, 0x20000, v208\n"
"v_add_u32 v212, 0x10000, v211\n"
"v_add_u32 v213, 0x20000, v211\n"
"v_add_u32 v215, 0x10000, v214\n"
"v_add_u32 v216, 0x20000, v214\n"
"v_add_u32 v218, 0x10000, v217\n"
"v_add_u32 v219, 0x20000, v217\n"
"v_mov_b32 v204, 0x3f803f80\n"
"v_mov_b32 v205, 0x3f803f80\n"
"v_mov_b32 v206, 0x3f803f80\n"
"v_mov_b32 v207, 0x3f803f80\n"
"s_lshl_b32 s24, s26, 13\n"
"s_lshl_b32 s28, s27, 10\n"
"s_add_u32 s24, s24, s28\n"
"s_waitcnt vmcnt(12)\n"
"s_barrier\n"
"ds_read_b128 a[0:3], v225 offset:0\n"
"ds_read_b64_tr_b16 a[192:193], v217 offset:0\n"
"ds_read_b64_tr_b16 a[194:195], v217 offset:256\n"
"ds_read_b128 a[4:7], v225 offset:2048\n"
"ds_read_b128 a[8:11], v225 offset:4096\n"
"ds_read_b64_tr_b16 a[196:197], v217 offset:8\n"
"ds_read_b64_tr_b16 a[198:199], v217 offset:264\n"
"ds_read_b128 a[12:15], v225 offset:512\n"
"ds_read_b128 a[16:19], v225 offset:2560\n"
"ds_read_b128 a[20:23], v225 offset:4608\n"
"ds_read_b64_tr_b16 a[200:201], v217 offset:512\n"
"ds_read_b64_tr_b16 a[202:203], v217 offset:768\n"
"s_waitcnt lgkmcnt(9)\n"
"v_mfma_f32_16x16x32_bf16 v[0:3], a[192:195], a[0:3], 0\n"
"s_xor_b32 s80, s88, 0x100000\n"
"ds_read_b64_tr_b16 a[204:205], v217 offset:520\n"
"ds_read_b64_tr_b16 a[206:207], v217 offset:776\n"
"s_waitcnt lgkmcnt(10)\n"
"v_mfma_f32_16x16x32_bf16 v[4:7], a[192:195], a[4:7], 0\n"
"s_add_u32 s83, s82, 0x0\n"
"ds_read_b64_tr_b16 a[208:209], v217 offset:1024\n"
"s_waitcnt lgkmcnt(10)\n"
"v_mfma_f32_16x16x32_bf16 v[8:11], a[192:195], a[8:11], 0\n"
"ds_read_b64_tr_b16 a[210:211], v217 offset:1280\n"
"s_xor_b32 s79, s100, 0x0\n"
"s_add_u32 m0, s78, 0x8000\n"
"s_waitcnt lgkmcnt(9)\n"
"v_mfma_f32_16x16x32_bf16 v[12:15], a[196:199], a[0:3], 0\n"
"buffer_load_dwordx4 v221, s[40:43], s80 offen lds\n"
"s_xor_b32 s80, s88, 0x120000\n"
"s_waitcnt lgkmcnt(6)\n"
"v_mfma_f32_16x16x32_bf16 v[16:19], a[196:199], a[4:7], 0\n"
"s_xor_b32 s81, s89, 0x100\n"
"ds_read_b64_tr_b16 a[212:213], v217 offset:1032\n"
"v_mfma_f32_16x16x32_bf16 v[20:23], a[196:199], a[8:11], 0\n"
"ds_read_b64_tr_b16 a[214:215], v217 offset:1288\n"
"ds_read_b64_tr_b16 a[216:217], v217 offset:1536\n"
"s_add_u32 m0, s87, 0x1c000\n"
"s_waitcnt lgkmcnt(4)\n"
"v_mfma_f32_16x16x32_bf16 v[24:27], a[200:203], a[0:3], 0\n"
"buffer_load_dwordx4 v222, s[36:39], s81 offen lds\n"
"s_add_u32 m0, s78, 0x9000\n"
"v_mfma_f32_16x16x32_bf16 v[28:31], a[200:203], a[4:7], 0\n"
"buffer_load_dwordx4 v221, s[40:43], s80 offen lds\n"
"s_xor_b32 s80, s88, 0x140000\n"
"v_mfma_f32_16x16x32_bf16 v[32:35], a[200:203], a[8:11], 0\n"
"ds_read_b64_tr_b16 a[218:219], v217 offset:1792\n"
"ds_read_b64_tr_b16 a[220:221], v217 offset:1544\n"
"ds_read_b64_tr_b16 a[222:223], v217 offset:1800\n"
"ds_read_b64_tr_b16 a[224:225], v217 offset:8192\n"
"ds_read_b64_tr_b16 a[226:227], v217 offset:8448\n"
"ds_read_b64_tr_b16 a[228:229], v217 offset:8200\n"
"ds_read_b64_tr_b16 a[230:231], v217 offset:8456\n"
"ds_read_b64_tr_b16 a[232:233], v217 offset:8704\n"
"ds_read_b64_tr_b16 a[234:235], v217 offset:8960\n"
"ds_read_b64_tr_b16 a[236:237], v217 offset:8712\n"
"ds_read_b64_tr_b16 a[238:239], v217 offset:8968\n"
"ds_read_b64_tr_b16 a[240:241], v217 offset:9216\n"
"ds_read_b64_tr_b16 a[242:243], v217 offset:9472\n"
"ds_read_b64_tr_b16 a[244:245], v217 offset:9224\n"
"ds_read_b64_tr_b16 a[246:247], v217 offset:9480\n"
"ds_read_b64_tr_b16 a[248:249], v217 offset:9728\n"
"ds_read_b64_tr_b16 a[250:251], v217 offset:9984\n"
"ds_read_b64_tr_b16 a[252:253], v217 offset:9736\n"
"ds_read_b64_tr_b16 a[254:255], v217 offset:9992\n"
"s_waitcnt vmcnt(5) lgkmcnt(0)\n"
"s_barrier\n"
"v_mfma_f32_16x16x32_bf16 v[36:39], a[204:207], a[0:3], 0\n"
"s_xor_b32 s81, s89, 0x2100\n"
"s_add_u32 m0, s87, 0x1c400\n"
"v_mfma_f32_16x16x32_bf16 v[40:43], a[204:207], a[4:7], 0\n"
"buffer_load_dwordx4 v222, s[36:39], s81 offen lds\n"
"s_add_u32 m0, s78, 0xa000\n"
"v_mfma_f32_16x16x32_bf16 v[44:47], a[204:207], a[8:11], 0\n"
"buffer_load_dwordx4 v221, s[40:43], s80 offen lds\n"
"s_xor_b32 s80, s88, 0x160000\n"
"v_mfma_f32_16x16x32_bf16 v[48:51], a[208:211], a[0:3], 0\n"
"ds_read_b128 a[24:27], v225 offset:6144\n"
"ds_read_b128 a[28:31], v225 offset:8192\n"
"ds_read_b128 a[32:35], v225 offset:10240\n"
"v_mfma_f32_16x16x32_bf16 v[52:55], a[208:211], a[4:7], 0\n"
"ds_read_b128 a[36:39], v225 offset:6656\n"
"ds_read_b128 a[40:43], v225 offset:8704\n"
"ds_read_b128 a[44:47], v225 offset:10752\n"
"v_mfma_f32_16x16x32_bf16 v[56:59], a[208:211], a[8:11], 0\n"
"s_xor_b32 s81, s89, 0x4100\n"
"ds_read_b64_tr_b16 a[192:193], v217 offset:16384\n"
"s_add_u32 m0, s78, 0xb000\n"
"v_mfma_f32_16x16x32_bf16 v[60:63], a[212:215], a[0:3], 0\n"
"buffer_load_dwordx4 v221, s[40:43], s80 offen lds\n"
"s_add_u32 m0, s87, 0x1c800\n"
"v_mfma_f32_16x16x32_bf16 v[64:67], a[212:215], a[4:7], 0\n"
"buffer_load_dwordx4 v222, s[36:39], s81 offen lds\n"
"v_mfma_f32_16x16x32_bf16 v[68:71], a[212:215], a[8:11], 0\n"
"s_xor_b32 s81, s89, 0x6100\n"
"s_xor_b32 s80, s88, 0x180000\n"
"v_mfma_f32_16x16x32_bf16 v[72:75], a[216:219], a[0:3], 0\n"
"ds_read_b64_tr_b16 a[194:195], v217 offset:16640\n"
"ds_read_b64_tr_b16 a[196:197], v217 offset:16392\n"
"v_mfma_f32_16x16x32_bf16 v[76:79], a[216:219], a[4:7], 0\n"
"s_waitcnt lgkmcnt(2)\n"
"ds_read_b64_tr_b16 a[198:199], v217 offset:16648\n"
"s_add_u32 m0, s87, 0x1cc00\n"
"v_mfma_f32_16x16x32_bf16 v[80:83], a[216:219], a[8:11], 0\n"
"buffer_load_dwordx4 v222, s[36:39], s81 offen lds\n"
"v_mfma_f32_16x16x32_bf16 v[84:87], a[220:223], a[0:3], 0\n"
"s_xor_b32 s81, s89, 0x8100\n"
"ds_read_b64_tr_b16 a[200:201], v217 offset:16896\n"
"s_add_u32 m0, s78, 0xc000\n"
"v_mfma_f32_16x16x32_bf16 v[88:91], a[220:223], a[4:7], 0\n"
"buffer_load_dwordx4 v221, s[40:43], s80 offen lds\n"
"v_mfma_f32_16x16x32_bf16 v[92:95], a[220:223], a[8:11], 0\n"
"s_xor_b32 s80, s88, 0x1a0000\n"
"ds_read_b64_tr_b16 a[202:203], v217 offset:17152\n"
"s_add_u32 m0, s87, 0x1d000\n"
"v_mfma_f32_16x16x32_bf16 v[0:3], a[224:227], a[12:15], v[0:3]\n"
"buffer_load_dwordx4 v222, s[36:39], s81 offen lds\n"
"v_mfma_f32_16x16x32_bf16 v[4:7], a[224:227], a[16:19], v[4:7]\n"
"s_xor_b32 s81, s89, 0xa100\n"
"ds_read_b64_tr_b16 a[204:205], v217 offset:16904\n"
"s_add_u32 m0, s78, 0xd000\n"
"v_mfma_f32_16x16x32_bf16 v[8:11], a[224:227], a[20:23], v[8:11]\n"
"buffer_load_dwordx4 v221, s[40:43], s80 offen lds\n"
"v_mfma_f32_16x16x32_bf16 v[12:15], a[228:231], a[12:15], v[12:15]\n"
"s_xor_b32 s80, s88, 0x1c0000\n"
"ds_read_b64_tr_b16 a[206:207], v217 offset:17160\n"
"s_add_u32 m0, s87, 0x1d400\n"
"v_mfma_f32_16x16x32_bf16 v[16:19], a[228:231], a[16:19], v[16:19]\n"
"buffer_load_dwordx4 v222, s[36:39], s81 offen lds\n"
"v_mfma_f32_16x16x32_bf16 v[20:23], a[228:231], a[20:23], v[20:23]\n"
"s_xor_b32 s81, s89, 0x180\n"
"ds_read_b64_tr_b16 a[208:209], v217 offset:17408\n"
"s_add_u32 m0, s78, 0xe000\n"
"v_mfma_f32_16x16x32_bf16 v[24:27], a[232:235], a[12:15], v[24:27]\n"
"buffer_load_dwordx4 v221, s[40:43], s80 offen lds\n"
"v_mfma_f32_16x16x32_bf16 v[28:31], a[232:235], a[16:19], v[28:31]\n"
"s_xor_b32 s80, s88, 0x1e0000\n"
"ds_read_b64_tr_b16 a[210:211], v217 offset:17664\n"
"s_add_u32 m0, s87, 0x1d800\n"
"v_mfma_f32_16x16x32_bf16 v[32:35], a[232:235], a[20:23], v[32:35]\n"
"buffer_load_dwordx4 v222, s[36:39], s81 offen lds\n"
"v_mfma_f32_16x16x32_bf16 v[36:39], a[236:239], a[12:15], v[36:39]\n"
"s_xor_b32 s81, s89, 0x2180\n"
"ds_read_b64_tr_b16 a[212:213], v217 offset:17416\n"
"s_add_u32 m0, s78, 0xf000\n"
"v_mfma_f32_16x16x32_bf16 v[40:43], a[236:239], a[16:19], v[40:43]\n"
"buffer_load_dwordx4 v221, s[40:43], s80 offen lds\n"
"v_mfma_f32_16x16x32_bf16 v[44:47], a[236:239], a[20:23], v[44:47]\n"
"s_xor_b32 s80, s88, 0x200000\n"
"ds_read_b64_tr_b16 a[214:215], v217 offset:17672\n"
"s_add_u32 m0, s87, 0x1dc00\n"
"v_mfma_f32_16x16x32_bf16 v[48:51], a[240:243], a[12:15], v[48:51]\n"
"buffer_load_dwordx4 v222, s[36:39], s81 offen lds\n"
"v_mfma_f32_16x16x32_bf16 v[52:55], a[240:243], a[16:19], v[52:55]\n"
"s_xor_b32 s81, s89, 0x4180\n"
"ds_read_b64_tr_b16 a[216:217], v217 offset:17920\n"
"s_add_u32 m0, s78, 0x10000\n"
"v_mfma_f32_16x16x32_bf16 v[56:59], a[240:243], a[20:23], v[56:59]\n"
"buffer_load_dwordx4 v221, s[40:43], s80 offen lds\n"
"v_mfma_f32_16x16x32_bf16 v[60:63], a[244:247], a[12:15], v[60:63]\n"
"s_xor_b32 s80, s88, 0x220000\n"
"ds_read_b64_tr_b16 a[218:219], v217 offset:18176\n"
"s_add_u32 m0, s87, 0x1e000\n"
"v_mfma_f32_16x16x32_bf16 v[64:67], a[244:247], a[16:19], v[64:67]\n"
"buffer_load_dwordx4 v222, s[36:39], s81 offen lds\n"
"v_mfma_f32_16x16x32_bf16 v[68:71], a[244:247], a[20:23], v[68:71]\n"
"s_xor_b32 s81, s89, 0x6180\n"
"ds_read_b64_tr_b16 a[220:221], v217 offset:17928\n"
"s_add_u32 m0, s78, 0x11000\n"
"v_mfma_f32_16x16x32_bf16 v[72:75], a[248:251], a[12:15], v[72:75]\n"
"buffer_load_dwordx4 v221, s[40:43], s80 offen lds\n"
"v_mfma_f32_16x16x32_bf16 v[76:79], a[248:251], a[16:19], v[76:79]\n"
"s_xor_b32 s80, s88, 0x240000\n"
"ds_read_b64_tr_b16 a[222:223], v217 offset:18184\n"
"s_add_u32 m0, s87, 0x1e400\n"
"v_mfma_f32_16x16x32_bf16 v[80:83], a[248:251], a[20:23], v[80:83]\n"
"buffer_load_dwordx4 v222, s[36:39], s81 offen lds\n"
"v_mfma_f32_16x16x32_bf16 v[84:87], a[252:255], a[12:15], v[84:87]\n"
"s_xor_b32 s81, s89, 0x8180\n"
"ds_read_b64_tr_b16 a[224:225], v217 offset:24576\n"
"v_mfma_f32_16x16x32_bf16 v[88:91], a[252:255], a[16:19], v[88:91]\n"
"ds_read_b64_tr_b16 a[226:227], v217 offset:24832\n"
"ds_read_b64_tr_b16 a[228:229], v217 offset:24584\n"
"v_mfma_f32_16x16x32_bf16 v[92:95], a[252:255], a[20:23], v[92:95]\n"
"ds_read_b64_tr_b16 a[230:231], v217 offset:24840\n"
"ds_read_b64_tr_b16 a[232:233], v217 offset:25088\n"
"s_waitcnt lgkmcnt(5)\n"
"v_mfma_f32_16x16x32_bf16 v[0:3], a[192:195], a[24:27], v[0:3]\n"
"ds_read_b64_tr_b16 a[234:235], v217 offset:25344\n"
"ds_read_b64_tr_b16 a[236:237], v217 offset:25096\n"
"v_mfma_f32_16x16x32_bf16 v[4:7], a[192:195], a[28:31], v[4:7]\n"
"ds_read_b64_tr_b16 a[238:239], v217 offset:25352\n"
"ds_read_b64_tr_b16 a[240:241], v217 offset:25600\n"
"v_mfma_f32_16x16x32_bf16 v[8:11], a[192:195], a[32:35], v[8:11]\n"
"ds_read_b64_tr_b16 a[242:243], v217 offset:25856\n"
"ds_read_b64_tr_b16 a[244:245], v217 offset:25608\n"
"v_mfma_f32_16x16x32_bf16 v[12:15], a[196:199], a[24:27], v[12:15]\n"
"ds_read_b64_tr_b16 a[246:247], v217 offset:25864\n"
"ds_read_b64_tr_b16 a[248:249], v217 offset:26112\n"
"v_mfma_f32_16x16x32_bf16 v[16:19], a[196:199], a[28:31], v[16:19]\n"
"ds_read_b64_tr_b16 a[250:251], v217 offset:26368\n"
"ds_read_b64_tr_b16 a[252:253], v217 offset:26120\n"
"v_mfma_f32_16x16x32_bf16 v[20:23], a[196:199], a[32:35], v[20:23]\n"
"ds_read_b64_tr_b16 a[254:255], v217 offset:26376\n"
"s_mov_b32 s23, 0x3f800000\n"
"s_add_u32 m0, s78, 0x12000\n"
"v_mfma_f32_16x16x32_bf16 v[24:27], a[200:203], a[24:27], v[24:27]\n"
"buffer_load_dwordx4 v221, s[40:43], s80 offen lds\n"
"v_mfma_f32_16x16x32_bf16 v[28:31], a[200:203], a[28:31], v[28:31]\n"
"s_xor_b32 s80, s88, 0x260000\n"
"v_mfma_f32_16x16x32_bf16 v[32:35], a[200:203], a[32:35], v[32:35]\n"
"s_waitcnt vmcnt(15) lgkmcnt(0)\n"
"s_barrier\n"
"s_add_u32 m0, s87, 0x1e800\n"
"v_mfma_f32_16x16x32_bf16 v[36:39], a[204:207], a[24:27], v[36:39]\n"
"buffer_load_dwordx4 v222, s[36:39], s81 offen lds\n"
"v_mfma_f32_16x16x32_bf16 v[40:43], a[204:207], a[28:31], v[40:43]\n"
"s_xor_b32 s81, s89, 0xa180\n"
"ds_read_b64_tr_b16 a[192:193], v217 offset:32768\n"
"s_add_u32 m0, s78, 0x13000\n"
"v_mfma_f32_16x16x32_bf16 v[44:47], a[204:207], a[32:35], v[44:47]\n"
"buffer_load_dwordx4 v221, s[40:43], s80 offen lds\n"
"v_mfma_f32_16x16x32_bf16 v[48:51], a[208:211], a[24:27], v[48:51]\n"
"s_xor_b32 s80, s88, 0x280000\n"
"ds_read_b64_tr_b16 a[194:195], v217 offset:33024\n"
"s_add_u32 m0, s87, 0x1ec00\n"
"v_mfma_f32_16x16x32_bf16 v[52:55], a[208:211], a[28:31], v[52:55]\n"
"buffer_load_dwordx4 v222, s[36:39], s81 offen lds\n"
"v_mfma_f32_16x16x32_bf16 v[56:59], a[208:211], a[32:35], v[56:59]\n"
"ds_read_b64_tr_b16 a[196:197], v217 offset:32776\n"
"ds_read_b64_tr_b16 a[198:199], v217 offset:33032\n"
"s_add_u32 m0, s78, 0x0\n"
"v_mfma_f32_16x16x32_bf16 v[60:63], a[212:215], a[24:27], v[60:63]\n"
"buffer_load_dwordx4 v221, s[40:43], s80 offen lds\n"
"v_mfma_f32_16x16x32_bf16 v[64:67], a[212:215], a[28:31], v[64:67]\n"
"s_xor_b32 s80, s88, 0x2a0000\n"
"ds_read_b64_tr_b16 a[200:201], v217 offset:33280\n"
"v_mfma_f32_16x16x32_bf16 v[68:71], a[212:215], a[32:35], v[68:71]\n"
"ds_read_b64_tr_b16 a[202:203], v217 offset:33536\n"
"ds_read_b64_tr_b16 a[204:205], v217 offset:33288\n"
"v_mfma_f32_16x16x32_bf16 v[72:75], a[216:219], a[24:27], v[72:75]\n"
"ds_read_b64_tr_b16 a[206:207], v217 offset:33544\n"
"ds_read_b64_tr_b16 a[208:209], v217 offset:33792\n"
"s_add_u32 m0, s78, 0x1000\n"
"v_mfma_f32_16x16x32_bf16 v[76:79], a[216:219], a[28:31], v[76:79]\n"
"buffer_load_dwordx4 v221, s[40:43], s80 offen lds\n"
"v_mfma_f32_16x16x32_bf16 v[80:83], a[216:219], a[32:35], v[80:83]\n"
"s_xor_b32 s80, s88, 0x2c0000\n"
"ds_read_b64_tr_b16 a[210:211], v217 offset:34048\n"
"v_mfma_f32_16x16x32_bf16 v[84:87], a[220:223], a[24:27], v[84:87]\n"
"ds_read_b64_tr_b16 a[212:213], v217 offset:33800\n"
"ds_read_b64_tr_b16 a[214:215], v217 offset:34056\n"
"v_mfma_f32_16x16x32_bf16 v[88:91], a[220:223], a[28:31], v[88:91]\n"
"ds_read_b64_tr_b16 a[216:217], v217 offset:34304\n"
"ds_read_b64_tr_b16 a[218:219], v217 offset:34560\n"
"s_add_u32 m0, s78, 0x2000\n"
"v_mfma_f32_16x16x32_bf16 v[92:95], a[220:223], a[32:35], v[92:95]\n"
"buffer_load_dwordx4 v221, s[40:43], s80 offen lds\n"
"v_mfma_f32_16x16x32_bf16 v[0:3], a[224:227], a[36:39], v[0:3]\n"
"s_xor_b32 s80, s88, 0x2e0000\n"
"ds_read_b64_tr_b16 a[220:221], v217 offset:34312\n"
"v_mfma_f32_16x16x32_bf16 v[4:7], a[224:227], a[40:43], v[4:7]\n"
"ds_read_b64_tr_b16 a[222:223], v217 offset:34568\n"
"v_mfma_f32_16x16x32_bf16 v[8:11], a[224:227], a[44:47], v[8:11]\n"
"ds_read_b64_tr_b16 a[224:225], v217 offset:40960\n"
"ds_read_b64_tr_b16 a[226:227], v217 offset:41216\n"
"s_add_u32 m0, s78, 0x3000\n"
"v_mfma_f32_16x16x32_bf16 v[12:15], a[228:231], a[36:39], v[12:15]\n"
"buffer_load_dwordx4 v221, s[40:43], s80 offen lds\n"
"v_mfma_f32_16x16x32_bf16 v[16:19], a[228:231], a[40:43], v[16:19]\n"
"v_mfma_f32_16x16x32_bf16 v[20:23], a[228:231], a[44:47], v[20:23]\n"
"ds_read_b64_tr_b16 a[228:229], v217 offset:40968\n"
"ds_read_b64_tr_b16 a[230:231], v217 offset:41224\n"
"v_mfma_f32_16x16x32_bf16 v[24:27], a[232:235], a[36:39], v[24:27]\n"
"v_mfma_f32_16x16x32_bf16 v[28:31], a[232:235], a[40:43], v[28:31]\n"
"v_mfma_f32_16x16x32_bf16 v[32:35], a[232:235], a[44:47], v[32:35]\n"
"ds_read_b64_tr_b16 a[232:233], v217 offset:41472\n"
"ds_read_b64_tr_b16 a[234:235], v217 offset:41728\n"
"v_mfma_f32_16x16x32_bf16 v[36:39], a[236:239], a[36:39], v[36:39]\n"
"v_mfma_f32_16x16x32_bf16 v[40:43], a[236:239], a[40:43], v[40:43]\n"
"v_mfma_f32_16x16x32_bf16 v[44:47], a[236:239], a[44:47], v[44:47]\n"
"ds_read_b64_tr_b16 a[236:237], v217 offset:41480\n"
"ds_read_b64_tr_b16 a[238:239], v217 offset:41736\n"
"v_mfma_f32_16x16x32_bf16 v[48:51], a[240:243], a[36:39], v[48:51]\n"
"v_mfma_f32_16x16x32_bf16 v[52:55], a[240:243], a[40:43], v[52:55]\n"
"v_mfma_f32_16x16x32_bf16 v[56:59], a[240:243], a[44:47], v[56:59]\n"
"ds_read_b64_tr_b16 a[240:241], v217 offset:41984\n"
"ds_read_b64_tr_b16 a[242:243], v217 offset:42240\n"
"v_mfma_f32_16x16x32_bf16 v[60:63], a[244:247], a[36:39], v[60:63]\n"
"v_mfma_f32_16x16x32_bf16 v[64:67], a[244:247], a[40:43], v[64:67]\n"
"s_waitcnt vmcnt(16) lgkmcnt(2)\n"
"ds_read_b128 a[48:51], v225 offset:0\n"
"ds_read_b128 a[52:55], v225 offset:2048\n"
"v_mfma_f32_16x16x32_bf16 v[68:71], a[244:247], a[44:47], v[68:71]\n"
"ds_read_b128 a[56:59], v225 offset:4096\n"
"ds_read_b128 a[60:63], v225 offset:512\n"
"s_add_u32 m0, s77, 0x14000\n"
"v_mfma_f32_16x16x32_bf16 v[72:75], a[248:251], a[36:39], v[72:75]\n"
"buffer_load_dwordx4 v220, s[44:47], s79 offen nt lds\n"
"ds_read_b128 a[64:67], v225 offset:2560\n"
"v_mfma_f32_16x16x32_bf16 v[76:79], a[248:251], a[40:43], v[76:79]\n"
"ds_read_b128 a[68:71], v225 offset:4608\n"
"ds_read_b64_tr_b16 a[244:245], v217 offset:41992\n"
"v_mfma_f32_16x16x32_bf16 v[80:83], a[248:251], a[44:47], v[80:83]\n"
"ds_read_b64_tr_b16 a[246:247], v217 offset:42248\n"
"ds_read_b64_tr_b16 a[248:249], v217 offset:42496\n"
"v_mfma_f32_16x16x32_bf16 v[84:87], a[252:255], a[36:39], v[84:87]\n"
"ds_read_b64_tr_b16 a[250:251], v217 offset:42752\n"
"v_mfma_f32_16x16x32_bf16 v[88:91], a[252:255], a[40:43], v[88:91]\n"
"v_mfma_f32_16x16x32_bf16 v[92:95], a[252:255], a[44:47], v[92:95]\n"
"ds_read_b64_tr_b16 a[252:253], v217 offset:42504\n"
"ds_read_b64_tr_b16 a[254:255], v217 offset:42760\n"
"s_waitcnt lgkmcnt(5)\n"
"v_mfma_f32_16x16x32_bf16 v[0:3], a[192:195], a[48:51], v[0:3]\n"
"v_mfma_f32_16x16x32_bf16 v[4:7], a[192:195], a[52:55], v[4:7]\n"
"s_waitcnt lgkmcnt(3)\n"
"s_xor_b32 s81, s89, 0x200\n"
"s_add_u32 m0, s87, 0x1c000\n"
"v_mfma_f32_16x16x32_bf16 v[8:11], a[192:195], a[56:59], v[8:11]\n"
"buffer_load_dwordx4 v222, s[36:39], s81 offen lds\n"
"v_mfma_f32_16x16x32_bf16 v[12:15], a[196:199], a[48:51], v[12:15]\n"
"s_xor_b32 s81, s89, 0x2200\n"
"v_mfma_f32_16x16x32_bf16 v[16:19], a[196:199], a[52:55], v[16:19]\n"
"v_readfirstlane_b32 s90, v246\n"
"v_readfirstlane_b32 s91, v247\n"
"v_mfma_f32_16x16x32_bf16 v[20:23], a[196:199], a[56:59], v[20:23]\n"
"s_nop 1\n"
"s_mul_i32 s90, s90, 0x9e3779b1\n"
"s_add_u32 s90, s90, 0x7f4a7c15\n"
"s_add_u32 m0, s87, 0x1c400\n"
"v_mfma_f32_16x16x32_bf16 v[24:27], a[200:203], a[48:51], v[24:27]\n"
"buffer_load_dwordx4 v222, s[36:39], s81 offen lds\n"
"v_mfma_f32_16x16x32_bf16 v[28:31], a[200:203], a[52:55], v[28:31]\n"
"s_xor_b32 s81, s89, 0x4200\n"
"s_mul_i32 s91, s91, 0x85ebca6b\n"
"s_add_u32 m0, s77, 0x14000\n"
"v_mfma_f32_16x16x32_bf16 v[32:35], a[200:203], a[56:59], v[32:35]\n"
"buffer_load_dwordx4 v220, s[44:47], s79 offen offset:2048 nt lds\n"
"s_waitcnt vmcnt(17) lgkmcnt(0)\n"
"s_barrier\n"
"v_mfma_f32_16x16x32_bf16 v[36:39], a[204:207], a[48:51], v[36:39]\n"
"s_xor_b32 s80, s88, 0x300000\n"
"ds_read_b64_tr_b16 a[192:193], v217 offset:49152\n"
"s_add_u32 m0, s87, 0x1c800\n"
"v_mfma_f32_16x16x32_bf16 v[40:43], a[204:207], a[52:55], v[40:43]\n"
"buffer_load_dwordx4 v222, s[36:39], s81 offen lds\n"
"v_mfma_f32_16x16x32_bf16 v[44:47], a[204:207], a[56:59], v[44:47]\n"
"s_xor_b32 s81, s89, 0x6200\n"
"ds_read_b64_tr_b16 a[194:195], v217 offset:49408\n"
"s_add_u32 m0, s78, 0x8000\n"
"v_mfma_f32_16x16x32_bf16 v[48:51], a[208:211], a[48:51], v[48:51]\n"
"buffer_load_dwordx4 v221, s[40:43], s80 offen lds\n"
"v_mfma_f32_16x16x32_bf16 v[52:55], a[208:211], a[52:55], v[52:55]\n"
"s_xor_b32 s80, s88, 0x320000\n"
"ds_read_b64_tr_b16 a[196:197], v217 offset:49160\n"
"s_add_u32 m0, s87, 0x1cc00\n"
"v_mfma_f32_16x16x32_bf16 v[56:59], a[208:211], a[56:59], v[56:59]\n"
"buffer_load_dwordx4 v222, s[36:39], s81 offen lds\n"
"v_mfma_f32_16x16x32_bf16 v[60:63], a[212:215], a[48:51], v[60:63]\n"
"s_xor_b32 s81, s89, 0x8200\n"
"ds_read_b64_tr_b16 a[198:199], v217 offset:49416\n"
"s_add_u32 m0, s78, 0x9000\n"
"v_mfma_f32_16x16x32_bf16 v[64:67], a[212:215], a[52:55], v[64:67]\n"
"buffer_load_dwordx4 v221, s[40:43], s80 offen lds\n"
"v_mfma_f32_16x16x32_bf16 v[68:71], a[212:215], a[56:59], v[68:71]\n"
"s_xor_b32 s80, s88, 0x340000\n"
"ds_read_b64_tr_b16 a[200:201], v217 offset:49664\n"
"s_add_u32 m0, s87, 0x1d000\n"
"v_mfma_f32_16x16x32_bf16 v[72:75], a[216:219], a[48:51], v[72:75]\n"
"buffer_load_dwordx4 v222, s[36:39], s81 offen lds\n"
"v_mfma_f32_16x16x32_bf16 v[76:79], a[216:219], a[52:55], v[76:79]\n"
"s_xor_b32 s81, s89, 0xa200\n"
"ds_read_b64_tr_b16 a[202:203], v217 offset:49920\n"
"s_add_u32 m0, s78, 0xa000\n"
"v_mfma_f32_16x16x32_bf16 v[80:83], a[216:219], a[56:59], v[80:83]\n"
"buffer_load_dwordx4 v221, s[40:43], s80 offen lds\n"
"v_mfma_f32_16x16x32_bf16 v[84:87], a[220:223], a[48:51], v[84:87]\n"
"s_xor_b32 s80, s88, 0x360000\n"
"ds_read_b64_tr_b16 a[204:205], v217 offset:49672\n"
"s_add_u32 m0, s77, 0x16000\n"
"v_mfma_f32_16x16x32_bf16 v[88:91], a[220:223], a[52:55], v[88:91]\n"
"buffer_load_dwordx4 v220, s[48:51], s79 offen nt lds\n"
"s_add_u32 m0, s87, 0x1d400\n"
"v_mfma_f32_16x16x32_bf16 v[92:95], a[220:223], a[56:59], v[92:95]\n"
"buffer_load_dwordx4 v222, s[36:39], s81 offen lds\n"
"s_add_u32 m0, s78, 0xb000\n"
"v_mfma_f32_16x16x32_bf16 v[0:3], a[224:227], a[60:63], v[0:3]\n"
"buffer_load_dwordx4 v221, s[40:43], s80 offen lds\n"
"v_mfma_f32_16x16x32_bf16 v[4:7], a[224:227], a[64:67], v[4:7]\n"
"s_waitcnt vmcnt(17) lgkmcnt(1)\n"
"ds_read_b128 a[72:75], v225 offset:6144\n"
"v_mfma_f32_16x16x32_bf16 v[8:11], a[224:227], a[68:71], v[8:11]\n"
"ds_read_b128 a[76:79], v225 offset:8192\n"
"ds_read_b128 a[80:83], v225 offset:10240\n"
"v_mfma_f32_16x16x32_bf16 v[12:15], a[228:231], a[60:63], v[12:15]\n"
"ds_read_b128 a[84:87], v225 offset:6656\n"
"ds_read_b128 a[88:91], v225 offset:8704\n"
"ds_read_b128 a[92:95], v225 offset:10752\n"
"v_mfma_f32_16x16x32_bf16 v[16:19], a[228:231], a[64:67], v[16:19]\n"
"ds_read_b64_tr_b16 a[206:207], v217 offset:49928\n"
"ds_read_b64_tr_b16 a[208:209], v217 offset:50176\n"
"v_mfma_f32_16x16x32_bf16 v[20:23], a[228:231], a[68:71], v[20:23]\n"
"ds_read_b64_tr_b16 a[210:211], v217 offset:50432\n"
"ds_read_b64_tr_b16 a[212:213], v217 offset:50184\n"
"v_mfma_f32_16x16x32_bf16 v[24:27], a[232:235], a[60:63], v[24:27]\n"
"ds_read_b64_tr_b16 a[214:215], v217 offset:50440\n"
"ds_read_b64_tr_b16 a[216:217], v217 offset:50688\n"
"v_mfma_f32_16x16x32_bf16 v[28:31], a[232:235], a[64:67], v[28:31]\n"
"ds_read_b64_tr_b16 a[218:219], v217 offset:50944\n"
"ds_read_b64_tr_b16 a[220:221], v217 offset:50696\n"
"v_mfma_f32_16x16x32_bf16 v[32:35], a[232:235], a[68:71], v[32:35]\n"
"ds_read_b64_tr_b16 a[222:223], v217 offset:50952\n"
"ds_read_b64_tr_b16 a[224:225], v217 offset:57344\n"
"v_mfma_f32_16x16x32_bf16 v[36:39], a[236:239], a[60:63], v[36:39]\n"
"s_waitcnt lgkmcnt(8)\n"
"s_xor_b32 s81, s89, 0x280\n"
"s_add_u32 m0, s87, 0x1d800\n"
"v_mfma_f32_16x16x32_bf16 v[40:43], a[236:239], a[64:67], v[40:43]\n"
"buffer_load_dwordx4 v222, s[36:39], s81 offen lds\n"
"v_mfma_f32_16x16x32_bf16 v[44:47], a[236:239], a[68:71], v[44:47]\n"
"s_xor_b32 s81, s89, 0x2280\n"
"ds_read_b64_tr_b16 a[226:227], v217 offset:57600\n"
"s_add_u32 m0, s77, 0x16000\n"
"v_mfma_f32_16x16x32_bf16 v[48:51], a[240:243], a[60:63], v[48:51]\n"
"buffer_load_dwordx4 v220, s[48:51], s79 offen offset:2048 nt lds\n"
"s_xor_b32 s79, s100, 0x2000\n"
"v_mfma_f32_16x16x32_bf16 v[52:55], a[240:243], a[64:67], v[52:55]\n"
"ds_read_b64_tr_b16 a[228:229], v217 offset:57352\n"
"ds_read_b64_tr_b16 a[230:231], v217 offset:57608\n"
"s_add_u32 m0, s87, 0x1dc00\n"
"v_mfma_f32_16x16x32_bf16 v[56:59], a[240:243], a[68:71], v[56:59]\n"
"buffer_load_dwordx4 v222, s[36:39], s81 offen lds\n"
"v_mfma_f32_16x16x32_bf16 v[60:63], a[244:247], a[60:63], v[60:63]\n"
"s_xor_b32 s81, s89, 0x4280\n"
"ds_read_b64_tr_b16 a[232:233], v217 offset:57856\n"
"v_mfma_f32_16x16x32_bf16 v[64:67], a[244:247], a[64:67], v[64:67]\n"
"ds_read_b64_tr_b16 a[234:235], v217 offset:58112\n"
"ds_read_b64_tr_b16 a[236:237], v217 offset:57864\n"
"v_mfma_f32_16x16x32_bf16 v[68:71], a[244:247], a[68:71], v[68:71]\n"
"ds_read_b64_tr_b16 a[238:239], v217 offset:58120\n"
"ds_read_b64_tr_b16 a[240:241], v217 offset:58368\n"
"s_add_u32 m0, s87, 0x1e000\n"
"v_mfma_f32_16x16x32_bf16 v[72:75], a[248:251], a[60:63], v[72:75]\n"
"buffer_load_dwordx4 v222, s[36:39], s81 offen lds\n"
"v_mfma_f32_16x16x32_bf16 v[76:79], a[248:251], a[64:67], v[76:79]\n"
"s_xor_b32 s81, s89, 0x6280\n"
"ds_read_b64_tr_b16 a[242:243], v217 offset:58624\n"
"v_mfma_f32_16x16x32_bf16 v[80:83], a[248:251], a[68:71], v[80:83]\n"
"ds_read_b64_tr_b16 a[244:245], v217 offset:58376\n"
"ds_read_b64_tr_b16 a[246:247], v217 offset:58632\n"
"v_mfma_f32_16x16x32_bf16 v[84:87], a[252:255], a[60:63], v[84:87]\n"
"ds_read_b64_tr_b16 a[248:249], v217 offset:58880\n"
"ds_read_b64_tr_b16 a[250:251], v217 offset:59136\n"
"s_add_u32 m0, s87, 0x1e400\n"
"v_mfma_f32_16x16x32_bf16 v[88:91], a[252:255], a[64:67], v[88:91]\n"
"buffer_load_dwordx4 v222, s[36:39], s81 offen lds\n"
"v_mfma_f32_16x16x32_bf16 v[92:95], a[252:255], a[68:71], v[92:95]\n"
"s_xor_b32 s81, s89, 0x8280\n"
"ds_read_b64_tr_b16 a[252:253], v217 offset:58888\n"
"v_mfma_f32_16x16x32_bf16 v[0:3], a[192:195], a[72:75], v[0:3]\n"
"ds_read_b64_tr_b16 a[254:255], v217 offset:59144\n"
"s_add_u32 s91, s91, 0x1b873593\n"
"v_mfma_f32_16x16x32_bf16 v[4:7], a[192:195], a[76:79], v[4:7]\n"
"s_add_u32 m0, s77, 0x18000\n"
"v_mfma_f32_16x16x32_bf16 v[8:11], a[192:195], a[80:83], v[8:11]\n"
"buffer_load_dwordx4 v220, s[44:47], s79 offen nt lds\n"
"s_add_u32 m0, s87, 0x1e800\n"
"v_mfma_f32_16x16x32_bf16 v[12:15], a[196:199], a[72:75], v[12:15]\n"
"buffer_load_dwordx4 v222, s[36:39], s81 offen lds\n"
"v_mfma_f32_16x16x32_bf16 v[16:19], a[196:199], a[76:79], v[16:19]\n"
"s_xor_b32 s81, s89, 0xa280\n"
"v_mfma_f32_16x16x32_bf16 v[20:23], a[196:199], a[80:83], v[20:23]\n"
"v_mfma_f32_16x16x32_bf16 v[24:27], a[200:203], a[72:75], v[24:27]\n"
"s_add_u32 m0, s87, 0x1ec00\n"
"v_mfma_f32_16x16x32_bf16 v[28:31], a[200:203], a[76:79], v[28:31]\n"
"buffer_load_dwordx4 v222, s[36:39], s81 offen lds\n"
"v_mfma_f32_16x16x32_bf16 v[32:35], a[200:203], a[80:83], v[32:35]\n"
"s_waitcnt lgkmcnt(0)\n"
"s_barrier\n"
"v_mfma_f32_16x16x32_bf16 v[36:39], a[204:207], a[72:75], v[36:39]\n"
"s_xor_b32 s80, s88, 0x380000\n"
"ds_read_b64_tr_b16 a[192:193], v218 offset:0\n"
"s_add_u32 m0, s78, 0xc000\n"
"v_mfma_f32_16x16x32_bf16 v[40:43], a[204:207], a[76:79], v[40:43]\n"
"buffer_load_dwordx4 v221, s[40:43], s80 offen lds\n"
"v_mfma_f32_16x16x32_bf16 v[44:47], a[204:207], a[80:83], v[44:47]\n"
"s_xor_b32 s80, s88, 0x3a0000\n"
"ds_read_b64_tr_b16 a[194:195], v218 offset:256\n"
"v_mfma_f32_16x16x32_bf16 v[48:51], a[208:211], a[72:75], v[48:51]\n"
"ds_read_b64_tr_b16 a[196:197], v218 offset:8\n"
"ds_read_b64_tr_b16 a[198:199], v218 offset:264\n"
"v_mfma_f32_16x16x32_bf16 v[52:55], a[208:211], a[76:79], v[52:55]\n"
"ds_read_b64_tr_b16 a[200:201], v218 offset:512\n"
"ds_read_b64_tr_b16 a[202:203], v218 offset:768\n"
"s_add_u32 m0, s78, 0xd000\n"
"v_mfma_f32_16x16x32_bf16 v[56:59], a[208:211], a[80:83], v[56:59]\n"
"buffer_load_dwordx4 v221, s[40:43], s80 offen lds\n"
"v_mfma_f32_16x16x32_bf16 v[60:63], a[212:215], a[72:75], v[60:63]\n"
"s_xor_b32 s80, s88, 0x3c0000\n"
"ds_read_b64_tr_b16 a[204:205], v218 offset:520\n"
"s_add_u32 m0, s77, 0x18000\n"
"v_mfma_f32_16x16x32_bf16 v[64:67], a[212:215], a[76:79], v[64:67]\n"
"buffer_load_dwordx4 v220, s[44:47], s79 offen offset:2048 nt lds\n"
"ds_read_b64_tr_b16 a[206:207], v218 offset:776\n"
"v_mfma_f32_16x16x32_bf16 v[68:71], a[212:215], a[80:83], v[68:71]\n"
"ds_read_b64_tr_b16 a[208:209], v218 offset:1024\n"
"ds_read_b64_tr_b16 a[210:211], v218 offset:1280\n"
"s_add_u32 m0, s78, 0xe000\n"
"v_mfma_f32_16x16x32_bf16 v[72:75], a[216:219], a[72:75], v[72:75]\n"
"buffer_load_dwordx4 v221, s[40:43], s80 offen lds\n"
"v_mfma_f32_16x16x32_bf16 v[76:79], a[216:219], a[76:79], v[76:79]\n"
"s_xor_b32 s80, s88, 0x3e0000\n"
"ds_read_b64_tr_b16 a[212:213], v218 offset:1032\n"
"v_mfma_f32_16x16x32_bf16 v[80:83], a[216:219], a[80:83], v[80:83]\n"
"ds_read_b64_tr_b16 a[214:215], v218 offset:1288\n"
"ds_read_b64_tr_b16 a[216:217], v218 offset:1536\n"
"v_mfma_f32_16x16x32_bf16 v[84:87], a[220:223], a[72:75], v[84:87]\n"
"ds_read_b64_tr_b16 a[218:219], v218 offset:1792\n"
"s_add_u32 m0, s78, 0xf000\n"
"v_mfma_f32_16x16x32_bf16 v[88:91], a[220:223], a[76:79], v[88:91]\n"
"buffer_load_dwordx4 v221, s[40:43], s80 offen lds\n"
"v_mfma_f32_16x16x32_bf16 v[92:95], a[220:223], a[80:83], v[92:95]\n"
"ds_read_b64_tr_b16 a[220:221], v218 offset:1544\n"
"ds_read_b64_tr_b16 a[222:223], v218 offset:1800\n"
"v_mfma_f32_16x16x32_bf16 v[0:3], a[224:227], a[84:87], v[0:3]\n"
"v_mfma_f32_16x16x32_bf16 v[4:7], a[224:227], a[88:91], v[4:7]\n"
"v_mfma_f32_16x16x32_bf16 v[8:11], a[224:227], a[92:95], v[8:11]\n"
"ds_read_b64_tr_b16 a[224:225], v218 offset:8192\n"
"ds_read_b64_tr_b16 a[226:227], v218 offset:8448\n"
"v_mfma_f32_16x16x32_bf16 v[12:15], a[228:231], a[84:87], v[12:15]\n"
"v_mfma_f32_16x16x32_bf16 v[16:19], a[228:231], a[88:91], v[16:19]\n"
"v_mfma_f32_16x16x32_bf16 v[20:23], a[228:231], a[92:95], v[20:23]\n"
"ds_read_b64_tr_b16 a[228:229], v218 offset:8200\n"
"ds_read_b64_tr_b16 a[230:231], v218 offset:8456\n"
"s_add_u32 m0, s77, 0x1a000\n"
"v_mfma_f32_16x16x32_bf16 v[24:27], a[232:235], a[84:87], v[24:27]\n"
"buffer_load_dwordx4 v220, s[48:51], s79 offen nt lds\n"
"v_mfma_f32_16x16x32_bf16 v[28:31], a[232:235], a[88:91], v[28:31]\n"
"v_mfma_f32_16x16x32_bf16 v[32:35], a[232:235], a[92:95], v[32:35]\n"
"ds_read_b64_tr_b16 a[232:233], v218 offset:8704\n"
"ds_read_b64_tr_b16 a[234:235], v218 offset:8960\n"
"v_mfma_f32_16x16x32_bf16 v[36:39], a[236:239], a[84:87], v[36:39]\n"
"v_mfma_f32_16x16x32_bf16 v[40:43], a[236:239], a[88:91], v[40:43]\n"
"v_mfma_f32_16x16x32_bf16 v[44:47], a[236:239], a[92:95], v[44:47]\n"
"s_waitcnt vmcnt(15) lgkmcnt(2)\n"
"ds_read_b128 a[96:99], v225 offset:0\n"
"v_mfma_f32_16x16x32_bf16 v[48:51], a[240:243], a[84:87], v[48:51]\n"
"ds_read_b128 a[100:103], v225 offset:2048\n"
"ds_read_b128 a[104:107], v225 offset:4096\n"
"v_mfma_f32_16x16x32_bf16 v[52:55], a[240:243], a[88:91], v[52:55]\n"
"ds_read_b128 a[108:111], v225 offset:512\n"
"ds_read_b128 a[112:115], v225 offset:2560\n"
"ds_read_b128 a[116:119], v225 offset:4608\n"
"v_mfma_f32_16x16x32_bf16 v[56:59], a[240:243], a[92:95], v[56:59]\n"
"ds_read_b64_tr_b16 a[236:237], v218 offset:8712\n"
"ds_read_b64_tr_b16 a[238:239], v218 offset:8968\n"
"v_mfma_f32_16x16x32_bf16 v[60:63], a[244:247], a[84:87], v[60:63]\n"
"ds_read_b64_tr_b16 a[240:241], v218 offset:9216\n"
"ds_read_b64_tr_b16 a[242:243], v218 offset:9472\n"
"v_mfma_f32_16x16x32_bf16 v[64:67], a[244:247], a[88:91], v[64:67]\n"
"v_mfma_f32_16x16x32_bf16 v[68:71], a[244:247], a[92:95], v[68:71]\n"
"ds_read_b64_tr_b16 a[244:245], v218 offset:9224\n"
"ds_read_b64_tr_b16 a[246:247], v218 offset:9480\n"
"v_mfma_f32_16x16x32_bf16 v[72:75], a[248:251], a[84:87], v[72:75]\n"
"v_mfma_f32_16x16x32_bf16 v[76:79], a[248:251], a[88:91], v[76:79]\n"
"s_waitcnt lgkmcnt(4)\n"
"s_xor_b32 s81, s89, 0x300\n"
"v_mfma_f32_16x16x32_bf16 v[80:83], a[248:251], a[92:95], v[80:83]\n"
"buffer_load_dwordx4 v220, s[48:51], s79 offen offset:2048 nt lds\n"
"s_xor_b32 s79, s100, 0x4000\n"
"s_add_u32 m0, s87, 0x1c000\n"
"v_mfma_f32_16x16x32_bf16 v[84:87], a[252:255], a[84:87], v[84:87]\n"
"buffer_load_dwordx4 v222, s[36:39], s81 offen lds\n"
"v_mfma_f32_16x16x32_bf16 v[88:91], a[252:255], a[88:91], v[88:91]\n"
"s_xor_b32 s81, s89, 0x2300\n"
"ds_read_b64_tr_b16 a[248:249], v218 offset:9728\n"
"v_mfma_f32_16x16x32_bf16 v[92:95], a[252:255], a[92:95], v[92:95]\n"
"ds_read_b64_tr_b16 a[250:251], v218 offset:9984\n"
"ds_read_b64_tr_b16 a[252:253], v218 offset:9736\n"
"v_mfma_f32_16x16x32_bf16 v[0:3], a[192:195], a[96:99], v[0:3]\n"
"ds_read_b64_tr_b16 a[254:255], v218 offset:9992\n"
"s_add_u32 m0, s87, 0x1c400\n"
"v_mfma_f32_16x16x32_bf16 v[4:7], a[192:195], a[100:103], v[4:7]\n"
"buffer_load_dwordx4 v222, s[36:39], s81 offen lds\n"
"v_mfma_f32_16x16x32_bf16 v[8:11], a[192:195], a[104:107], v[8:11]\n"
"s_xor_b32 s81, s89, 0x4300\n"
"v_mfma_f32_16x16x32_bf16 v[12:15], a[196:199], a[96:99], v[12:15]\n"
"v_mfma_f32_16x16x32_bf16 v[16:19], a[196:199], a[100:103], v[16:19]\n"
"s_add_u32 m0, s87, 0x1c800\n"
"v_mfma_f32_16x16x32_bf16 v[20:23], a[196:199], a[104:107], v[20:23]\n"
"buffer_load_dwordx4 v222, s[36:39], s81 offen lds\n"
"v_mfma_f32_16x16x32_bf16 v[24:27], a[200:203], a[96:99], v[24:27]\n"
"s_xor_b32 s81, s89, 0x6300\n"
"v_mfma_f32_16x16x32_bf16 v[28:31], a[200:203], a[100:103], v[28:31]\n"
"v_mfma_f32_16x16x32_bf16 v[32:35], a[200:203], a[104:107], v[32:35]\n"
"s_waitcnt lgkmcnt(0)\n"
"s_barrier\n"
"s_add_u32 m0, s87, 0x1cc00\n"
"v_mfma_f32_16x16x32_bf16 v[36:39], a[204:207], a[96:99], v[36:39]\n"
"buffer_load_dwordx4 v222, s[36:39], s81 offen lds\n"
"s_add_u32 m0, s77, 0x4000\n"
"v_mfma_f32_16x16x32_bf16 v[40:43], a[204:207], a[100:103], v[40:43]\n"
"buffer_load_dwordx4 v220, s[44:47], s79 offen nt lds\n"
"v_mfma_f32_16x16x32_bf16 v[44:47], a[204:207], a[104:107], v[44:47]\n"
"s_xor_b32 s81, s89, 0x8300\n"
"ds_read_b64_tr_b16 a[192:193], v217 offset:0\n"
"v_mfma_f32_16x16x32_bf16 v[48:51], a[208:211], a[96:99], v[48:51]\n"
"ds_read_b64_tr_b16 a[194:195], v217 offset:256\n"
"ds_read_b64_tr_b16 a[196:197], v217 offset:8\n"
"s_add_u32 m0, s87, 0x1d000\n"
"v_mfma_f32_16x16x32_bf16 v[52:55], a[208:211], a[100:103], v[52:55]\n"
"buffer_load_dwordx4 v222, s[36:39], s81 offen lds\n"
"v_mfma_f32_16x16x32_bf16 v[56:59], a[208:211], a[104:107], v[56:59]\n"
"s_xor_b32 s81, s89, 0xa300\n"
"ds_read_b64_tr_b16 a[198:199], v217 offset:264\n"
"v_mfma_f32_16x16x32_bf16 v[60:63], a[212:215], a[96:99], v[60:63]\n"
"ds_read_b64_tr_b16 a[200:201], v217 offset:512\n"
"ds_read_b64_tr_b16 a[202:203], v217 offset:768\n"
"v_mfma_f32_16x16x32_bf16 v[64:67], a[212:215], a[100:103], v[64:67]\n"
"ds_read_b64_tr_b16 a[204:205], v217 offset:520\n"
"ds_read_b64_tr_b16 a[206:207], v217 offset:776\n"
"s_add_u32 m0, s87, 0x1d400\n"
"v_mfma_f32_16x16x32_bf16 v[68:71], a[212:215], a[104:107], v[68:71]\n"
"buffer_load_dwordx4 v222, s[36:39], s81 offen lds\n"
"v_mfma_f32_16x16x32_bf16 v[72:75], a[216:219], a[96:99], v[72:75]\n"
"ds_read_b64_tr_b16 a[208:209], v217 offset:1024\n"
"ds_read_b64_tr_b16 a[210:211], v217 offset:1280\n"
"v_mfma_f32_16x16x32_bf16 v[76:79], a[216:219], a[100:103], v[76:79]\n"
"s_waitcnt vmcnt(14) lgkmcnt(6)\n"
"ds_read_b128 a[120:123], v225 offset:6144\n"
"v_mfma_f32_16x16x32_bf16 v[80:83], a[216:219], a[104:107], v[80:83]\n"
"ds_read_b128 a[124:127], v225 offset:8192\n"
"ds_read_b128 a[128:131], v225 offset:10240\n"
"v_mfma_f32_16x16x32_bf16 v[84:87], a[220:223], a[96:99], v[84:87]\n"
"ds_read_b128 a[132:135], v225 offset:6656\n"
"ds_read_b128 a[136:139], v225 offset:8704\n"
"ds_read_b128 a[140:143], v225 offset:10752\n"
"v_mfma_f32_16x16x32_bf16 v[88:91], a[220:223], a[100:103], v[88:91]\n"
"ds_read_b64_tr_b16 a[212:213], v217 offset:1032\n"
"ds_read_b64_tr_b16 a[214:215], v217 offset:1288\n"
"v_mfma_f32_16x16x32_bf16 v[92:95], a[220:223], a[104:107], v[92:95]\n"
"ds_read_b64_tr_b16 a[216:217], v217 offset:1536\n"
"ds_read_b64_tr_b16 a[218:219], v217 offset:1792\n"
"s_add_u32 m0, s77, 0x4000\n"
"v_mfma_f32_16x16x32_bf16 v[0:3], a[224:227], a[108:111], v[0:3]\n"
"buffer_load_dwordx4 v220, s[44:47], s79 offen offset:2048 nt lds\n"
"ds_read_b64_tr_b16 a[220:221], v217 offset:1544\n"
"v_mfma_f32_16x16x32_bf16 v[4:7], a[224:227], a[112:115], v[4:7]\n"
"ds_read_b64_tr_b16 a[222:223], v217 offset:1800\n"
"v_mfma_f32_16x16x32_bf16 v[8:11], a[224:227], a[116:119], v[8:11]\n"
"ds_read_b64_tr_b16 a[224:225], v217 offset:8192\n"
"ds_read_b64_tr_b16 a[226:227], v217 offset:8448\n"
"v_mfma_f32_16x16x32_bf16 v[12:15], a[228:231], a[108:111], v[12:15]\n"
"s_waitcnt lgkmcnt(6)\n"
"s_xor_b32 s81, s89, 0x380\n"
"s_add_u32 m0, s87, 0x1d800\n"
"v_mfma_f32_16x16x32_bf16 v[16:19], a[228:231], a[112:115], v[16:19]\n"
"buffer_load_dwordx4 v222, s[36:39], s81 offen lds\n"
"v_mfma_f32_16x16x32_bf16 v[20:23], a[228:231], a[116:119], v[20:23]\n"
"s_xor_b32 s81, s89, 0x2380\n"
"ds_read_b64_tr_b16 a[228:229], v217 offset:8200\n"
"v_mfma_f32_16x16x32_bf16 v[24:27], a[232:235], a[108:111], v[24:27]\n"
"ds_read_b64_tr_b16 a[230:231], v217 offset:8456\n"
"v_mfma_f32_16x16x32_bf16 v[28:31], a[232:235], a[112:115], v[28:31]\n"
"s_add_u32 m0, s87, 0x1dc00\n"
"v_mfma_f32_16x16x32_bf16 v[32:35], a[232:235], a[116:119], v[32:35]\n"
"buffer_load_dwordx4 v222, s[36:39], s81 offen lds\n"
"v_mfma_f32_16x16x32_bf16 v[36:39], a[236:239], a[108:111], v[36:39]\n"
"s_xor_b32 s81, s89, 0x4380\n"
"ds_read_b64_tr_b16 a[232:233], v217 offset:8704\n"
"v_mfma_f32_16x16x32_bf16 v[40:43], a[236:239], a[112:115], v[40:43]\n"
"ds_read_b64_tr_b16 a[234:235], v217 offset:8960\n"
"v_mfma_f32_16x16x32_bf16 v[44:47], a[236:239], a[116:119], v[44:47]\n"
"ds_read_b64_tr_b16 a[236:237], v217 offset:8712\n"
"ds_read_b64_tr_b16 a[238:239], v217 offset:8968\n"
"s_add_u32 m0, s87, 0x1e000\n"
"v_mfma_f32_16x16x32_bf16 v[48:51], a[240:243], a[108:111], v[48:51]\n"
"buffer_load_dwordx4 v222, s[36:39], s81 offen lds\n"
"v_mfma_f32_16x16x32_bf16 v[52:55], a[240:243], a[112:115], v[52:55]\n"
"s_xor_b32 s81, s89, 0x6380\n"
"s_add_u32 m0, s77, 0x6000\n"
"v_mfma_f32_16x16x32_bf16 v[56:59], a[240:243], a[116:119], v[56:59]\n"
"buffer_load_dwordx4 v220, s[48:51], s79 offen nt lds\n"
"v_mfma_f32_16x16x32_bf16 v[60:63], a[244:247], a[108:111], v[60:63]\n"
"ds_read_b64_tr_b16 a[240:241], v217 offset:9216\n"
"ds_read_b64_tr_b16 a[242:243], v217 offset:9472\n"
"s_add_u32 m0, s87, 0x1e400\n"
"v_mfma_f32_16x16x32_bf16 v[64:67], a[244:247], a[112:115], v[64:67]\n"
"buffer_load_dwordx4 v222, s[36:39], s81 offen lds\n"
"v_mfma_f32_16x16x32_bf16 v[68:71], a[244:247], a[116:119], v[68:71]\n"
"s_xor_b32 s81, s89, 0x8380\n"
"ds_read_b64_tr_b16 a[244:245], v217 offset:9224\n"
"v_mfma_f32_16x16x32_bf16 v[72:75], a[248:251], a[108:111], v[72:75]\n"
"ds_read_b64_tr_b16 a[246:247], v217 offset:9480\n"
"v_mfma_f32_16x16x32_bf16 v[76:79], a[248:251], a[112:115], v[76:79]\n"
"s_add_u32 m0, s87, 0x1e800\n"
"v_mfma_f32_16x16x32_bf16 v[80:83], a[248:251], a[116:119], v[80:83]\n"
"buffer_load_dwordx4 v222, s[36:39], s81 offen lds\n"
"v_mfma_f32_16x16x32_bf16 v[84:87], a[252:255], a[108:111], v[84:87]\n"
"s_xor_b32 s81, s89, 0xa380\n"
"ds_read_b64_tr_b16 a[248:249], v217 offset:9728\n"
"v_mfma_f32_16x16x32_bf16 v[88:91], a[252:255], a[112:115], v[88:91]\n"
"ds_read_b64_tr_b16 a[250:251], v217 offset:9984\n"
"v_mfma_f32_16x16x32_bf16 v[92:95], a[252:255], a[116:119], v[92:95]\n"
"ds_read_b64_tr_b16 a[252:253], v217 offset:9736\n"
"ds_read_b64_tr_b16 a[254:255], v217 offset:9992\n"
"s_add_u32 m0, s87, 0x1ec00\n"
"v_mfma_f32_16x16x32_bf16 v[0:3], a[192:195], a[120:123], v[0:3]\n"
"buffer_load_dwordx4 v222, s[36:39], s81 offen lds\n"
"v_mfma_f32_16x16x32_bf16 v[4:7], a[192:195], a[124:127], v[4:7]\n"
"v_mfma_f32_16x16x32_bf16 v[8:11], a[192:195], a[128:131], v[8:11]\n"
"v_mfma_f32_16x16x32_bf16 v[12:15], a[196:199], a[120:123], v[12:15]\n"
"s_add_u32 m0, s77, 0x6000\n"
"v_mfma_f32_16x16x32_bf16 v[16:19], a[196:199], a[124:127], v[16:19]\n"
"buffer_load_dwordx4 v220, s[48:51], s79 offen offset:2048 nt lds\n"
"s_xor_b32 s79, s100, 0x6000\n"
"v_mfma_f32_16x16x32_bf16 v[20:23], a[196:199], a[128:131], v[20:23]\n"
"v_mfma_f32_16x16x32_bf16 v[24:27], a[200:203], a[120:123], v[24:27]\n"
"v_mfma_f32_16x16x32_bf16 v[28:31], a[200:203], a[124:127], v[28:31]\n"
"v_mfma_f32_16x16x32_bf16 v[32:35], a[200:203], a[128:131], v[32:35]\n"
"s_waitcnt lgkmcnt(0)\n"
"s_barrier\n"
"v_mfma_f32_16x16x32_bf16 v[36:39], a[204:207], a[120:123], v[36:39]\n"
"ds_read_b64_tr_b16 a[192:193], v217 offset:32768\n"
"ds_read_b64_tr_b16 a[194:195], v217 offset:33024\n"
"v_mfma_f32_16x16x32_bf16 v[40:43], a[204:207], a[124:127], v[40:43]\n"
"ds_read_b64_tr_b16 a[196:197], v217 offset:32776\n"
"ds_read_b64_tr_b16 a[198:199], v217 offset:33032\n"
"v_mfma_f32_16x16x32_bf16 v[44:47], a[204:207], a[128:131], v[44:47]\n"
"ds_read_b64_tr_b16 a[200:201], v217 offset:33280\n"
"ds_read_b64_tr_b16 a[202:203], v217 offset:33536\n"
"v_mfma_f32_16x16x32_bf16 v[48:51], a[208:211], a[120:123], v[48:51]\n"
"ds_read_b64_tr_b16 a[204:205], v217 offset:33288\n"
"ds_read_b64_tr_b16 a[206:207], v217 offset:33544\n"
"v_mfma_f32_16x16x32_bf16 v[52:55], a[208:211], a[124:127], v[52:55]\n"
"v_mfma_f32_16x16x32_bf16 v[56:59], a[208:211], a[128:131], v[56:59]\n"
"ds_read_b64_tr_b16 a[208:209], v217 offset:33792\n"
"ds_read_b64_tr_b16 a[210:211], v217 offset:34048\n"
"v_mfma_f32_16x16x32_bf16 v[60:63], a[212:215], a[120:123], v[60:63]\n"
"v_mfma_f32_16x16x32_bf16 v[64:67], a[212:215], a[124:127], v[64:67]\n"
"v_mfma_f32_16x16x32_bf16 v[68:71], a[212:215], a[128:131], v[68:71]\n"
"ds_read_b64_tr_b16 a[212:213], v217 offset:33800\n"
"ds_read_b64_tr_b16 a[214:215], v217 offset:34056\n"
"s_add_u32 m0, s77, 0x10000\n"
"v_mfma_f32_16x16x32_bf16 v[72:75], a[216:219], a[120:123], v[72:75]\n"
"buffer_load_dwordx4 v220, s[44:47], s79 offen nt lds\n"
"v_mfma_f32_16x16x32_bf16 v[76:79], a[216:219], a[124:127], v[76:79]\n"
"v_mfma_f32_16x16x32_bf16 v[80:83], a[216:219], a[128:131], v[80:83]\n"
"ds_read_b64_tr_b16 a[216:217], v217 offset:34304\n"
"ds_read_b64_tr_b16 a[218:219], v217 offset:34560\n"
"v_mfma_f32_16x16x32_bf16 v[84:87], a[220:223], a[120:123], v[84:87]\n"
"v_mfma_f32_16x16x32_bf16 v[88:91], a[220:223], a[124:127], v[88:91]\n"
"v_mfma_f32_16x16x32_bf16 v[92:95], a[220:223], a[128:131], v[92:95]\n"
"ds_read_b64_tr_b16 a[220:221], v217 offset:34312\n"
"ds_read_b64_tr_b16 a[222:223], v217 offset:34568\n"
"v_mfma_f32_16x16x32_bf16 v[0:3], a[224:227], a[132:135], v[0:3]\n"
"v_mfma_f32_16x16x32_bf16 v[4:7], a[224:227], a[136:139], v[4:7]\n"
"v_mfma_f32_16x16x32_bf16 v[8:11], a[224:227], a[140:143], v[8:11]\n"
"ds_read_b64_tr_b16 a[224:225], v217 offset:40960\n"
"ds_read_b64_tr_b16 a[226:227], v217 offset:41216\n"
"v_mfma_f32_16x16x32_bf16 v[12:15], a[228:231], a[132:135], v[12:15]\n"
"v_mfma_f32_16x16x32_bf16 v[16:19], a[228:231], a[136:139], v[16:19]\n"
"v_mfma_f32_16x16x32_bf16 v[20:23], a[228:231], a[140:143], v[20:23]\n"
"s_waitcnt vmcnt(10) lgkmcnt(2)\n"
"ds_read_b128 a[144:147], v225 offset:0\n"
"v_mfma_f32_16x16x32_bf16 v[24:27], a[232:235], a[132:135], v[24:27]\n"
"ds_read_b128 a[148:151], v225 offset:2048\n"
"ds_read_b128 a[152:155], v225 offset:4096\n"
"v_mfma_f32_16x16x32_bf16 v[28:31], a[232:235], a[136:139], v[28:31]\n"
"ds_read_b128 a[156:159], v225 offset:512\n"
"ds_read_b128 a[160:163], v225 offset:2560\n"
"ds_read_b128 a[164:167], v225 offset:4608\n"
"v_mfma_f32_16x16x32_bf16 v[32:35], a[232:235], a[140:143], v[32:35]\n"
"buffer_load_dwordx4 v220, s[44:47], s79 offen offset:2048 nt lds\n"
"ds_read_b64_tr_b16 a[228:229], v217 offset:40968\n"
"v_mfma_f32_16x16x32_bf16 v[36:39], a[236:239], a[132:135], v[36:39]\n"
"ds_read_b64_tr_b16 a[230:231], v217 offset:41224\n"
"ds_read_b64_tr_b16 a[232:233], v217 offset:41472\n"
"v_mfma_f32_16x16x32_bf16 v[40:43], a[236:239], a[136:139], v[40:43]\n"
"ds_read_b64_tr_b16 a[234:235], v217 offset:41728\n"
"v_mfma_f32_16x16x32_bf16 v[44:47], a[236:239], a[140:143], v[44:47]\n"
"ds_read_b64_tr_b16 a[236:237], v217 offset:41480\n"
"ds_read_b64_tr_b16 a[238:239], v217 offset:41736\n"
"v_mfma_f32_16x16x32_bf16 v[48:51], a[240:243], a[132:135], v[48:51]\n"
"v_mfma_f32_16x16x32_bf16 v[52:55], a[240:243], a[136:139], v[52:55]\n"
"s_waitcnt lgkmcnt(5)\n"
"v_mfma_f32_16x16x32_bf16 v[56:59], a[240:243], a[140:143], v[56:59]\n"
"ds_read_b64_tr_b16 a[240:241], v217 offset:41984\n"
"ds_read_b64_tr_b16 a[242:243], v217 offset:42240\n"
"v_mfma_f32_16x16x32_bf16 v[60:63], a[244:247], a[132:135], v[60:63]\n"
"v_mfma_f32_16x16x32_bf16 v[64:67], a[244:247], a[136:139], v[64:67]\n"
"v_mfma_f32_16x16x32_bf16 v[68:71], a[244:247], a[140:143], v[68:71]\n"
"ds_read_b64_tr_b16 a[244:245], v217 offset:41992\n"
"ds_read_b64_tr_b16 a[246:247], v217 offset:42248\n"
"v_mfma_f32_16x16x32_bf16 v[72:75], a[248:251], a[132:135], v[72:75]\n"
"v_mfma_f32_16x16x32_bf16 v[76:79], a[248:251], a[136:139], v[76:79]\n"
"v_mfma_f32_16x16x32_bf16 v[80:83], a[248:251], a[140:143], v[80:83]\n"
"ds_read_b64_tr_b16 a[248:249], v217 offset:42496\n"
"ds_read_b64_tr_b16 a[250:251], v217 offset:42752\n"
"v_mfma_f32_16x16x32_bf16 v[84:87], a[252:255], a[132:135], v[84:87]\n"
"s_add_u32 m0, s77, 0x12000\n"
"v_mfma_f32_16x16x32_bf16 v[88:91], a[252:255], a[136:139], v[88:91]\n"
"buffer_load_dwordx4 v220, s[48:51], s79 offen nt lds\n"
"v_mfma_f32_16x16x32_bf16 v[92:95], a[252:255], a[140:143], v[92:95]\n"
"ds_read_b64_tr_b16 a[252:253], v217 offset:42504\n"
"ds_read_b64_tr_b16 a[254:255], v217 offset:42760\n"
"v_mfma_f32_16x16x32_bf16 v[0:3], a[192:195], a[144:147], v[0:3]\n"
"v_mfma_f32_16x16x32_bf16 v[4:7], a[192:195], a[148:151], v[4:7]\n"
"v_mfma_f32_16x16x32_bf16 v[8:11], a[192:195], a[152:155], v[8:11]\n"
"v_mfma_f32_16x16x32_bf16 v[12:15], a[196:199], a[144:147], v[12:15]\n"
"v_mfma_f32_16x16x32_bf16 v[16:19], a[196:199], a[148:151], v[16:19]\n"
"v_mfma_f32_16x16x32_bf16 v[20:23], a[196:199], a[152:155], v[20:23]\n"
"v_mfma_f32_16x16x32_bf16 v[24:27], a[200:203], a[144:147], v[24:27]\n"
"v_mfma_f32_16x16x32_bf16 v[28:31], a[200:203], a[148:151], v[28:31]\n"
"v_mfma_f32_16x16x32_bf16 v[32:35], a[200:203], a[152:155], v[32:35]\n"
"s_waitcnt lgkmcnt(0)\n"
"s_barrier\n"
"v_mfma_f32_16x16x32_bf16 v[36:39], a[204:207], a[144:147], v[36:39]\n"
"ds_read_b64_tr_b16 a[192:193], v217 offset:49152\n"
"ds_read_b64_tr_b16 a[194:195], v217 offset:49408\n"
"v_mfma_f32_16x16x32_bf16 v[40:43], a[204:207], a[148:151], v[40:43]\n"
"ds_read_b64_tr_b16 a[196:197], v217 offset:49160\n"
"ds_read_b64_tr_b16 a[198:199], v217 offset:49416\n"
"v_mfma_f32_16x16x32_bf16 v[44:47], a[204:207], a[152:155], v[44:47]\n"
"ds_read_b64_tr_b16 a[200:201], v217 offset:49664\n"
"ds_read_b64_tr_b16 a[202:203], v217 offset:49920\n"
"v_mfma_f32_16x16x32_bf16 v[48:51], a[208:211], a[144:147], v[48:51]\n"
"s_waitcnt vmcnt(4)\n"
"buffer_load_dwordx4 v220, s[48:51], s79 offen offset:2048 nt lds\n"
"v_mfma_f32_16x16x32_bf16 v[52:55], a[208:211], a[148:151], v[52:55]\n"
"ds_read_b128 a[168:171], v225 offset:6144\n"
"ds_read_b128 a[172:175], v225 offset:8192\n"
"v_mfma_f32_16x16x32_bf16 v[56:59], a[208:211], a[152:155], v[56:59]\n"
"ds_read_b128 a[176:179], v225 offset:10240\n"
"ds_read_b128 a[180:183], v225 offset:6656\n"
"ds_read_b128 a[184:187], v225 offset:8704\n"
"v_mfma_f32_16x16x32_bf16 v[60:63], a[212:215], a[144:147], v[60:63]\n"
"ds_read_b128 a[188:191], v225 offset:10752\n"
"s_xor_b32 s79, s100, 0x8000\n"
"v_mfma_f32_16x16x32_bf16 v[64:67], a[212:215], a[148:151], v[64:67]\n"
"ds_read_b64_tr_b16 a[204:205], v217 offset:49672\n"
"ds_read_b64_tr_b16 a[206:207], v217 offset:49928\n"
"v_mfma_f32_16x16x32_bf16 v[68:71], a[212:215], a[152:155], v[68:71]\n"
"ds_read_b64_tr_b16 a[208:209], v217 offset:50176\n"
"ds_read_b64_tr_b16 a[210:211], v217 offset:50432\n"
"v_mfma_f32_16x16x32_bf16 v[72:75], a[216:219], a[144:147], v[72:75]\n"
"ds_read_b64_tr_b16 a[212:213], v217 offset:50184\n"
"ds_read_b64_tr_b16 a[214:215], v217 offset:50440\n"
"v_mfma_f32_16x16x32_bf16 v[76:79], a[216:219], a[148:151], v[76:79]\n"
"v_lshrrev_b32 v225, 1, v223\n"
"v_and_b32 v225, 0x18, v225\n"
"v_mfma_f32_16x16x32_bf16 v[80:83], a[216:219], a[152:155], v[80:83]\n"
"ds_read_b64_tr_b16 a[216:217], v217 offset:50688\n"
"ds_read_b64_tr_b16 a[218:219], v217 offset:50944\n"
"v_mfma_f32_16x16x32_bf16 v[84:87], a[220:223], a[144:147], v[84:87]\n"
"s_waitcnt lgkmcnt(6)\n"
"v_mfma_f32_16x16x32_bf16 v[88:91], a[220:223], a[148:151], v[88:91]\n"
"v_mfma_f32_16x16x32_bf16 v[92:95], a[220:223], a[152:155], v[92:95]\n"
"ds_read_b64_tr_b16 a[220:221], v217 offset:50696\n"
"ds_read_b64_tr_b16 a[222:223], v217 offset:50952\n"
"v_mfma_f32_16x16x32_bf16 v[0:3], a[224:227], a[156:159], v[0:3]\n"
"v_mfma_f32_16x16x32_bf16 v[4:7], a[224:227], a[160:163], v[4:7]\n"
"s_add_u32 m0, s77, 0x0\n"
"v_mfma_f32_16x16x32_bf16 v[8:11], a[224:227], a[164:167], v[8:11]\n"
"buffer_load_dwordx4 v220, s[44:47], s79 offen nt lds\n"
"v_mfma_f32_16x16x32_bf16 v[12:15], a[228:231], a[156:159], v[12:15]\n"
"ds_read_b64_tr_b16 a[224:225], v217 offset:57344\n"
"ds_read_b64_tr_b16 a[226:227], v217 offset:57600\n"
"v_mfma_f32_16x16x32_bf16 v[16:19], a[228:231], a[160:163], v[16:19]\n"
"v_mfma_f32_16x16x32_bf16 v[20:23], a[228:231], a[164:167], v[20:23]\n"
"ds_read_b64_tr_b16 a[228:229], v217 offset:57352\n"
"ds_read_b64_tr_b16 a[230:231], v217 offset:57608\n"
"v_mfma_f32_16x16x32_bf16 v[24:27], a[232:235], a[156:159], v[24:27]\n"
"v_mfma_f32_16x16x32_bf16 v[28:31], a[232:235], a[160:163], v[28:31]\n"
"v_mfma_f32_16x16x32_bf16 v[32:35], a[232:235], a[164:167], v[32:35]\n"
"ds_read_b64_tr_b16 a[232:233], v217 offset:57856\n"
"ds_read_b64_tr_b16 a[234:235], v217 offset:58112\n"
"v_mfma_f32_16x16x32_bf16 v[36:39], a[236:239], a[156:159], v[36:39]\n"
"v_mfma_f32_16x16x32_bf16 v[40:43], a[236:239], a[160:163], v[40:43]\n"
"v_mfma_f32_16x16x32_bf16 v[44:47], a[236:239], a[164:167], v[44:47]\n"
"ds_read_b64_tr_b16 a[236:237], v217 offset:57864\n"
"ds_read_b64_tr_b16 a[238:239], v217 offset:58120\n"
"v_mfma_f32_16x16x32_bf16 v[48:51], a[240:243], a[156:159], v[48:51]\n"
"v_mfma_f32_16x16x32_bf16 v[52:55], a[240:243], a[160:163], v[52:55]\n"
"v_mfma_f32_16x16x32_bf16 v[56:59], a[240:243], a[164:167], v[56:59]\n"
"ds_read_b64_tr_b16 a[240:241], v217 offset:58368\n"
"ds_read_b64_tr_b16 a[242:243], v217 offset:58624\n"
"v_mfma_f32_16x16x32_bf16 v[60:63], a[244:247], a[156:159], v[60:63]\n"
"v_mfma_f32_16x16x32_bf16 v[64:67], a[244:247], a[160:163], v[64:67]\n"
"buffer_load_dwordx4 v220, s[44:47], s79 offen offset:2048 nt lds\n"
"v_mfma_f32_16x16x32_bf16 v[68:71], a[244:247], a[164:167], v[68:71]\n"
"ds_read_b64_tr_b16 a[244:245], v217 offset:58376\n"
"ds_read_b64_tr_b16 a[246:247], v217 offset:58632\n"
"v_mfma_f32_16x16x32_bf16 v[72:75], a[248:251], a[156:159], v[72:75]\n"
"v_mfma_f32_16x16x32_bf16 v[76:79], a[248:251], a[160:163], v[76:79]\n"
"v_mfma_f32_16x16x32_bf16 v[80:83], a[248:251], a[164:167], v[80:83]\n"
"ds_read_b64_tr_b16 a[248:249], v217 offset:58880\n"
"ds_read_b64_tr_b16 a[250:251], v217 offset:59136\n"
"v_mfma_f32_16x16x32_bf16 v[84:87], a[252:255], a[156:159], v[84:87]\n"
"v_mfma_f32_16x16x32_bf16 v[88:91], a[252:255], a[160:163], v[88:91]\n"
"v_mfma_f32_16x16x32_bf16 v[92:95], a[252:255], a[164:167], v[92:95]\n"
"ds_read_b64_tr_b16 a[252:253], v217 offset:58888\n"
"ds_read_b64_tr_b16 a[254:255], v217 offset:59144\n"
"v_mfma_f32_16x16x32_bf16 v[0:3], a[192:195], a[168:171], v[0:3]\n"
"s_waitcnt lgkmcnt(2)\n"
"v_mfma_f32_16x16x32_bf16 v[0:3], a[224:227], a[180:183], v[0:3]\n"
"v_mfma_f32_16x16x32_bf16 v[4:7], a[192:195], a[172:175], v[4:7]\n"
"v_mfma_f32_16x16x32_bf16 v[4:7], a[224:227], a[184:187], v[4:7]\n"
"v_mfma_f32_16x16x32_bf16 v[8:11], a[192:195], a[176:179], v[8:11]\n"
"v_mfma_f32_16x16x32_bf16 v[8:11], a[224:227], a[188:191], v[8:11]\n"
"s_add_u32 m0, s77, 0x2000\n"
"v_mfma_f32_16x16x32_bf16 v[12:15], a[196:199], a[168:171], v[12:15]\n"
"buffer_load_dwordx4 v220, s[48:51], s79 offen nt lds\n"
"v_mfma_f32_16x16x32_bf16 v[12:15], a[228:231], a[180:183], v[12:15]\n"
"v_mul_f32 v0, s85, v0\n"
"v_mul_f32 v1, s85, v1\n"
"v_mfma_f32_16x16x32_bf16 v[16:19], a[196:199], a[172:175], v[16:19]\n"
"s_waitcnt lgkmcnt(0)\n"
"s_barrier\n"
"v_mfma_f32_16x16x32_bf16 v[16:19], a[228:231], a[184:187], v[16:19]\n"
"ds_read_b128 a[192:195], v209 offset:16384\n"
"v_mul_f32 v2, s85, v2\n"
"v_mfma_f32_16x16x32_bf16 v[20:23], a[196:199], a[176:179], v[20:23]\n"
"ds_read_b128 a[196:199], v209 offset:16896\n"
"v_mul_f32 v3, s85, v3\n"
"v_mfma_f32_16x16x32_bf16 v[20:23], a[228:231], a[188:191], v[20:23]\n"
"v_cvt_pk_bf16_f32 v96, v0, v1\n"
"v_cvt_pk_bf16_f32 v97, v2, v3\n"
"v_mfma_f32_16x16x32_bf16 v[24:27], a[200:203], a[168:171], v[24:27]\n"
"v_mul_f32 v12, s85, v12\n"
"v_mul_f32 v13, s85, v13\n"
"v_mfma_f32_16x16x32_bf16 v[24:27], a[232:235], a[180:183], v[24:27]\n"
"v_mul_f32 v14, s85, v14\n"
"v_mul_f32 v15, s85, v15\n"
"v_mfma_f32_16x16x32_bf16 v[28:31], a[200:203], a[172:175], v[28:31]\n"
"v_cvt_pk_bf16_f32 v98, v12, v13\n"
"v_cvt_pk_bf16_f32 v99, v14, v15\n"
"v_mfma_f32_16x16x32_bf16 v[28:31], a[232:235], a[184:187], v[28:31]\n"
"v_mul_f32 v4, s85, v4\n"
"v_mul_f32 v5, s85, v5\n"
"v_mfma_f32_16x16x32_bf16 v[32:35], a[200:203], a[176:179], v[32:35]\n"
"ds_read_b128 a[200:203], v209 offset:17408\n"
"v_mul_f32 v6, s85, v6\n"
"v_mfma_f32_16x16x32_bf16 v[32:35], a[232:235], a[188:191], v[32:35]\n"
"v_mul_f32 v7, s85, v7\n"
"v_cvt_pk_bf16_f32 v112, v4, v5\n"
"v_mfma_f32_16x16x32_bf16 v[36:39], a[204:207], a[168:171], v[36:39]\n"
"v_mul_f32 v24, s85, v24\n"
"v_mul_f32 v25, s85, v25\n"
"v_mfma_f32_16x16x32_bf16 v[36:39], a[236:239], a[180:183], v[36:39]\n"
"v_mul_f32 v26, s85, v26\n"
"v_mul_f32 v27, s85, v27\n"
"v_mfma_f32_16x16x32_bf16 v[40:43], a[204:207], a[172:175], v[40:43]\n"
"buffer_load_dwordx4 v220, s[48:51], s79 offen offset:2048 nt lds\n"
"s_xor_b32 s79, s100, 0xa000\n"
"v_mfma_f32_16x16x32_bf16 v[40:43], a[236:239], a[184:187], v[40:43]\n"
"v_cvt_pk_bf16_f32 v100, v24, v25\n"
"v_cvt_pk_bf16_f32 v101, v26, v27\n"
"v_mfma_f32_16x16x32_bf16 v[44:47], a[204:207], a[176:179], v[44:47]\n"
"ds_read_b128 a[204:207], v209 offset:17920\n"
"v_cvt_pk_bf16_f32 v113, v6, v7\n"
"v_mfma_f32_16x16x32_bf16 v[44:47], a[236:239], a[188:191], v[44:47]\n"
"v_mul_f32 v16, s85, v16\n"
"v_mul_f32 v17, s85, v17\n"
"v_mfma_f32_16x16x32_bf16 v[48:51], a[208:211], a[168:171], v[48:51]\n"
"v_mul_f32 v36, s85, v36\n"
"v_mul_f32 v37, s85, v37\n"
"v_mfma_f32_16x16x32_bf16 v[48:51], a[240:243], a[180:183], v[48:51]\n"
"v_mul_f32 v38, s85, v38\n"
"v_mul_f32 v39, s85, v39\n"
"v_mfma_f32_16x16x32_bf16 v[52:55], a[208:211], a[172:175], v[52:55]\n"
"v_cvt_pk_bf16_f32 v102, v36, v37\n"
"v_cvt_pk_bf16_f32 v103, v38, v39\n"
"v_mfma_f32_16x16x32_bf16 v[52:55], a[240:243], a[184:187], v[52:55]\n"
"v_mul_f32 v18, s85, v18\n"
"v_mul_f32 v19, s85, v19\n"
"v_mfma_f32_16x16x32_bf16 v[56:59], a[208:211], a[176:179], v[56:59]\n"
"ds_read_b128 a[208:211], v209 offset:20480\n"
"v_cvt_pk_bf16_f32 v114, v16, v17\n"
"v_mfma_f32_16x16x32_bf16 v[56:59], a[240:243], a[188:191], v[56:59]\n"
"v_cvt_pk_bf16_f32 v115, v18, v19\n"
"v_mul_f32 v28, s85, v28\n"
"v_mfma_f32_16x16x32_bf16 v[60:63], a[212:215], a[168:171], v[60:63]\n"
"v_mul_f32 v48, s85, v48\n"
"v_mul_f32 v49, s85, v49\n"
"v_mfma_f32_16x16x32_bf16 v[60:63], a[244:247], a[180:183], v[60:63]\n"
"v_mul_f32 v50, s85, v50\n"
"v_mul_f32 v51, s85, v51\n"
"v_mfma_f32_16x16x32_bf16 v[64:67], a[212:215], a[172:175], v[64:67]\n"
"v_cvt_pk_bf16_f32 v104, v48, v49\n"
"v_cvt_pk_bf16_f32 v105, v50, v51\n"
"v_mfma_f32_16x16x32_bf16 v[64:67], a[244:247], a[184:187], v[64:67]\n"
"v_mul_f32 v29, s85, v29\n"
"v_mul_f32 v30, s85, v30\n"
"s_add_u32 m0, s77, 0x8000\n"
"v_mfma_f32_16x16x32_bf16 v[68:71], a[212:215], a[176:179], v[68:71]\n"
"buffer_load_dwordx4 v220, s[44:47], s79 offen nt lds\n"
"v_mfma_f32_16x16x32_bf16 v[68:71], a[244:247], a[188:191], v[68:71]\n"
"ds_read_b128 a[212:215], v209 offset:20992\n"
"v_mul_f32 v31, s85, v31\n"
"v_mfma_f32_16x16x32_bf16 v[72:75], a[216:219], a[168:171], v[72:75]\n"
"v_mul_f32 v60, s85, v60\n"
"v_mul_f32 v61, s85, v61\n"
"v_mfma_f32_16x16x32_bf16 v[72:75], a[248:251], a[180:183], v[72:75]\n"
"v_mul_f32 v62, s85, v62\n"
"v_mul_f32 v63, s85, v63\n"
"v_mfma_f32_16x16x32_bf16 v[76:79], a[216:219], a[172:175], v[76:79]\n"
"v_cvt_pk_bf16_f32 v106, v60, v61\n"
"v_cvt_pk_bf16_f32 v107, v62, v63\n"
"v_mfma_f32_16x16x32_bf16 v[76:79], a[248:251], a[184:187], v[76:79]\n"
"v_cvt_pk_bf16_f32 v116, v28, v29\n"
"v_cvt_pk_bf16_f32 v117, v30, v31\n"
"v_mfma_f32_16x16x32_bf16 v[80:83], a[216:219], a[176:179], v[80:83]\n"
"ds_read_b128 a[216:219], v209 offset:21504\n"
"v_mul_f32 v40, s85, v40\n"
"v_mfma_f32_16x16x32_bf16 v[80:83], a[248:251], a[188:191], v[80:83]\n"
"v_mul_f32 v41, s85, v41\n"
"v_mul_f32 v42, s85, v42\n"
"v_mfma_f32_16x16x32_bf16 v[84:87], a[220:223], a[168:171], v[84:87]\n"
"v_mul_f32 v72, s85, v72\n"
"v_mul_f32 v73, s85, v73\n"
"v_mfma_f32_16x16x32_bf16 v[84:87], a[252:255], a[180:183], v[84:87]\n"
"v_mul_f32 v74, s85, v74\n"
"v_mul_f32 v75, s85, v75\n"
"v_mfma_f32_16x16x32_bf16 v[88:91], a[220:223], a[172:175], v[88:91]\n"
"v_cvt_pk_bf16_f32 v108, v72, v73\n"
"v_cvt_pk_bf16_f32 v109, v74, v75\n"
"v_mfma_f32_16x16x32_bf16 v[88:91], a[252:255], a[184:187], v[88:91]\n"
"v_mul_f32 v43, s85, v43\n"
"v_cvt_pk_bf16_f32 v118, v40, v41\n"
"v_mfma_f32_16x16x32_bf16 v[92:95], a[220:223], a[176:179], v[92:95]\n"
"ds_read_b128 a[220:223], v209 offset:22016\n"
"v_cvt_pk_bf16_f32 v119, v42, v43\n"
"v_mfma_f32_16x16x32_bf16 v[92:95], a[252:255], a[188:191], v[92:95]\n"
"v_mul_f32 v52, s85, v52\n"
"v_mul_f32 v53, s85, v53\n"
"s_waitcnt lgkmcnt(1)\n"
"v_mfma_f32_16x16x32_bf16 v[144:147], a[192:195], v[96:99], 0\n"
"buffer_load_dwordx4 v220, s[44:47], s79 offen offset:2048 nt lds\n"
"v_mul_f32 v84, s85, v84\n"
"v_mfma_f32_16x16x32_bf16 v[144:147], a[196:199], v[100:103], v[144:147]\n"
"v_mul_f32 v85, s85, v85\n"
"v_mul_f32 v86, s85, v86\n"
"v_mul_f32 v87, s85, v87\n"
"v_mfma_f32_16x16x32_bf16 v[144:147], a[200:203], v[104:107], v[144:147]\n"
"v_cvt_pk_bf16_f32 v110, v84, v85\n"
"v_cvt_pk_bf16_f32 v111, v86, v87\n"
"s_nop 1\n"
"v_mfma_f32_16x16x32_bf16 v[144:147], a[204:207], v[108:111], v[144:147]\n"
"v_mul_f32 v54, s85, v54\n"
"v_mul_f32 v55, s85, v55\n"
"v_mfma_f32_16x16x32_bf16 v[148:151], a[208:211], v[96:99], 0\n"
"v_cvt_pk_bf16_f32 v120, v52, v53\n"
"v_cvt_pk_bf16_f32 v121, v54, v55\n"
"v_mfma_f32_16x16x32_bf16 v[148:151], a[212:215], v[100:103], v[148:151]\n"
"v_mul_f32 v64, s85, v64\n"
"v_mul_f32 v65, s85, v65\n"
"v_mfma_f32_16x16x32_bf16 v[148:151], a[216:219], v[104:107], v[148:151]\n"
"v_mul_f32 v66, s85, v66\n"
"v_mul_f32 v67, s85, v67\n"
"s_waitcnt lgkmcnt(0)\n"
"v_mfma_f32_16x16x32_bf16 v[148:151], a[220:223], v[108:111], v[148:151]\n"
"v_cvt_pk_bf16_f32 v122, v64, v65\n"
"v_cvt_pk_bf16_f32 v123, v66, v67\n"
"v_mfma_f32_16x16x32_bf16 v[152:155], a[192:195], v[112:115], 0\n"
"ds_read_b64_tr_b16 a[224:225], v212 offset:16384\n"
"ds_read_b64_tr_b16 a[226:227], v212 offset:20480\n"
"v_mfma_f32_16x16x32_bf16 v[152:155], a[196:199], v[116:119], v[152:155]\n"
"v_mul_f32 v76, s85, v76\n"
"v_mul_f32 v77, s85, v77\n"
"v_mul_f32 v78, s85, v78\n"
"v_mfma_f32_16x16x32_bf16 v[152:155], a[200:203], v[120:123], v[152:155]\n"
"v_mul_f32 v79, s85, v79\n"
"v_cvt_pk_bf16_f32 v124, v76, v77\n"
"v_cvt_pk_bf16_f32 v125, v78, v79\n"
"v_mul_f32 v88, s85, v88\n"
"v_mul_f32 v89, s85, v89\n"
"v_mul_f32 v90, s85, v90\n"
"v_mul_f32 v91, s85, v91\n"
"v_cvt_pk_bf16_f32 v126, v88, v89\n"
"v_cvt_pk_bf16_f32 v127, v90, v91\n"
"s_nop 1\n"
"v_mfma_f32_16x16x32_bf16 v[152:155], a[204:207], v[124:127], v[152:155]\n"
"ds_read_b64_tr_b16 a[228:229], v215 offset:16384\n"
"ds_read_b64_tr_b16 a[230:231], v215 offset:20480\n"
"ds_read_b64_tr_b16 a[232:233], v212 offset:16896\n"
"v_mfma_f32_16x16x32_bf16 v[156:159], a[208:211], v[112:115], 0\n"
"ds_read_b64_tr_b16 a[234:235], v212 offset:20992\n"
"v_max3_f32 v245, v144, v145, v146\n"
"v_max3_f32 v246, v147, v148, v149\n"
"v_mfma_f32_16x16x32_bf16 v[156:159], a[212:215], v[116:119], v[156:159]\n"
"v_max3_f32 v245, v245, v150, v151\n"
"v_max_f32 v245, v245, v246\n"
"v_mov_b32 v246, v245\n"
"v_mfma_f32_16x16x32_bf16 v[156:159], a[216:219], v[120:123], v[156:159]\n"
"ds_read_b64_tr_b16 a[236:237], v215 offset:16896\n"
"ds_read_b64_tr_b16 a[238:239], v215 offset:20992\n"
"v_permlane32_swap_b32 v245, v246\n"
"v_mfma_f32_16x16x32_bf16 v[156:159], a[220:223], v[124:127], v[156:159]\n"
"v_max_f32 v245, v245, v246\n"
"v_mov_b32 v246, v245\n"
"ds_read_b64_tr_b16 a[240:241], v212 offset:17408\n"
"s_nop 0\n"
"v_permlane16_swap_b32 v245, v246\n"
"v_min_f32_e64 v160, -v245, -v246\n"
"v_add_f32 v144, v160, v144\n"
"v_add_f32 v145, v160, v145\n"
"v_add_f32 v146, v160, v146\n"
"v_add_f32 v147, v160, v147\n"
"v_add_f32 v148, v160, v148\n"
"v_add_f32 v149, v160, v149\n"
"v_add_f32 v150, v160, v150\n"
"v_add_f32 v151, v160, v151\n"
"v_exp_f32 v144, v144\n"
"v_exp_f32 v145, v145\n"
"v_exp_f32 v146, v146\n"
"v_exp_f32 v147, v147\n"
"v_exp_f32 v148, v148\n"
"v_exp_f32 v149, v149\n"
"v_exp_f32 v150, v150\n"
"v_exp_f32 v151, v151\n"
"v_cvt_pk_bf16_f32 v144, v144, v145\n"
"v_cvt_pk_bf16_f32 v145, v146, v147\n"
"v_cvt_pk_bf16_f32 v146, v148, v149\n"
"v_cvt_pk_bf16_f32 v147, v150, v151\n"
"s_nop 1\n"
"v_mfma_f32_16x16x32_bf16 v[192:195], v[204:207], v[144:147], 0\n"
"ds_read_b64_tr_b16 a[242:243], v212 offset:21504\n"
"ds_read_b64_tr_b16 a[244:245], v215 offset:17408\n"
"ds_read_b64_tr_b16 a[246:247], v215 offset:21504\n"
"s_add_u32 m0, s77, 0xa000\n"
"s_waitcnt lgkmcnt(6)\n"
"v_mfma_f32_16x16x32_bf16 v[0:3], a[224:227], v[144:147], 0\n"
"buffer_load_dwordx4 v220, s[48:51], s79 offen nt lds\n"
"ds_read_b64_tr_b16 a[248:249], v212 offset:17920\n"
"v_mfma_f32_16x16x32_bf16 v[12:15], a[228:231], v[144:147], 0\n"
"ds_read_b64_tr_b16 a[250:251], v212 offset:22016\n"
"ds_read_b64_tr_b16 a[252:253], v215 offset:17920\n"
"ds_read_b64_tr_b16 a[254:255], v215 offset:22016\n"
"v_mfma_f32_16x16x32_bf16 v[24:27], a[232:235], v[144:147], 0\n"
"v_mul_f32 v8, s85, v8\n"
"v_mul_f32 v9, s85, v9\n"
"s_waitcnt lgkmcnt(7)\n"
"v_mfma_f32_16x16x32_bf16 v[36:39], a[236:239], v[144:147], 0\n"
"v_mul_f32 v10, s85, v10\n"
"v_mul_f32 v11, s85, v11\n"
"s_waitcnt lgkmcnt(4)\n"
"v_mfma_f32_16x16x32_bf16 v[48:51], a[240:243], v[144:147], 0\n"
"v_cvt_pk_bf16_f32 v128, v8, v9\n"
"v_cvt_pk_bf16_f32 v129, v10, v11\n"
"v_mfma_f32_16x16x32_bf16 v[60:63], a[244:247], v[144:147], 0\n"
"v_mul_f32 v20, s85, v20\n"
"v_mul_f32 v21, s85, v21\n"
"s_waitcnt lgkmcnt(0)\n"
"v_mfma_f32_16x16x32_bf16 v[72:75], a[248:251], v[144:147], 0\n"
"v_mul_f32 v22, s85, v22\n"
"v_mul_f32 v23, s85, v23\n"
"v_cvt_pk_bf16_f32 v130, v20, v21\n"
"v_mfma_f32_16x16x32_bf16 v[84:87], a[252:255], v[144:147], 0\n"
"v_cvt_pk_bf16_f32 v131, v22, v23\n"
"v_mul_f32 v32, s85, v32\n"
"v_mul_f32 v33, s85, v33\n"
"s_barrier\n"
"v_mfma_f32_16x16x32_bf16 v[144:147], a[192:195], v[128:131], 0\n"
"v_mul_f32 v34, s85, v34\n"
"v_mul_f32 v35, s85, v35\n"
"v_cvt_pk_bf16_f32 v132, v32, v33\n"
"v_cvt_pk_bf16_f32 v133, v34, v35\n"
"v_mul_f32 v44, s85, v44\n"
"v_mul_f32 v45, s85, v45\n"
"v_mul_f32 v46, s85, v46\n"
"v_mul_f32 v47, s85, v47\n"
"v_cvt_pk_bf16_f32 v134, v44, v45\n"
"v_cvt_pk_bf16_f32 v135, v46, v47\n"
"s_nop 1\n"
"v_mfma_f32_16x16x32_bf16 v[144:147], a[196:199], v[132:135], v[144:147]\n"
"v_mul_f32 v56, s85, v56\n"
"v_mul_f32 v57, s85, v57\n"
"v_mul_f32 v58, s85, v58\n"
"v_mul_f32 v59, s85, v59\n"
"v_cvt_pk_bf16_f32 v136, v56, v57\n"
"v_cvt_pk_bf16_f32 v137, v58, v59\n"
"v_mul_f32 v68, s85, v68\n"
"v_mul_f32 v69, s85, v69\n"
"v_mul_f32 v70, s85, v70\n"
"v_mul_f32 v71, s85, v71\n"
"v_cvt_pk_bf16_f32 v138, v68, v69\n"
"v_cvt_pk_bf16_f32 v139, v70, v71\n"
"s_nop 1\n"
"v_mfma_f32_16x16x32_bf16 v[144:147], a[200:203], v[136:139], v[144:147]\n"
"v_mul_f32 v80, s85, v80\n"
"v_mul_f32 v81, s85, v81\n"
"v_mul_f32 v82, s85, v82\n"
"v_mul_f32 v83, s85, v83\n"
"v_cvt_pk_bf16_f32 v140, v80, v81\n"
"v_cvt_pk_bf16_f32 v141, v82, v83\n"
"v_mul_f32 v92, s85, v92\n"
"v_mul_f32 v93, s85, v93\n"
"v_mul_f32 v94, s85, v94\n"
"v_mul_f32 v95, s85, v95\n"
"v_cvt_pk_bf16_f32 v142, v92, v93\n"
"v_cvt_pk_bf16_f32 v143, v94, v95\n"
"s_nop 1\n"
"v_mfma_f32_16x16x32_bf16 v[144:147], a[204:207], v[140:143], v[144:147]\n"
"v_max3_f32 v247, v152, v153, v154\n"
"v_max3_f32 v248, v155, v156, v157\n"
"v_max3_f32 v247, v247, v158, v159\n"
"v_mfma_f32_16x16x32_bf16 v[148:151], a[208:211], v[128:131], 0\n"
"v_max_f32 v247, v247, v248\n"
"v_mov_b32 v248, v247\n"
"v_mfma_f32_16x16x32_bf16 v[148:151], a[212:215], v[132:135], v[148:151]\n"
"s_nop 0\n"
"v_permlane32_swap_b32 v247, v248\n"
"v_max_f32 v247, v247, v248\n"
"v_mov_b32 v248, v247\n"
"v_mfma_f32_16x16x32_bf16 v[148:151], a[216:219], v[136:139], v[148:151]\n"
"buffer_load_dwordx4 v220, s[48:51], s79 offen offset:2048 nt lds\n"
"v_permlane16_swap_b32 v247, v248\n"
"v_min_f32_e64 v164, -v247, -v248\n"
"v_mfma_f32_16x16x32_bf16 v[148:151], a[220:223], v[140:143], v[148:151]\n"
"v_add_f32 v152, v164, v152\n"
"v_add_f32 v153, v164, v153\n"
"v_add_f32 v154, v164, v154\n"
"v_add_f32 v155, v164, v155\n"
"v_add_f32 v156, v164, v156\n"
"v_add_f32 v157, v164, v157\n"
"v_add_f32 v158, v164, v158\n"
"v_add_f32 v159, v164, v159\n"
"v_exp_f32 v152, v152\n"
"v_exp_f32 v153, v153\n"
"v_exp_f32 v154, v154\n"
"v_exp_f32 v155, v155\n"
"v_exp_f32 v156, v156\n"
"v_exp_f32 v157, v157\n"
"v_exp_f32 v158, v158\n"
"v_exp_f32 v159, v159\n"
"v_cvt_pk_bf16_f32 v152, v152, v153\n"
"v_cvt_pk_bf16_f32 v153, v154, v155\n"
"v_cvt_pk_bf16_f32 v154, v156, v157\n"
"v_cvt_pk_bf16_f32 v155, v158, v159\n"
"s_nop 1\n"
"v_mfma_f32_16x16x32_bf16 v[196:199], v[204:207], v[152:155], 0\n"
"ds_read_b128 a[192:195], v209 offset:32768\n"
"ds_read_b128 a[196:199], v209 offset:33280\n"
"v_mfma_f32_16x16x32_bf16 v[4:7], a[224:227], v[152:155], 0\n"
"s_xor_b32 s79, s100, 0xc000\n"
"ds_read_b128 a[200:203], v209 offset:33792\n"
"v_mfma_f32_16x16x32_bf16 v[16:19], a[228:231], v[152:155], 0\n"
"ds_read_b128 a[204:207], v209 offset:34304\n"
"ds_read_b128 a[208:211], v209 offset:36864\n"
"v_mfma_f32_16x16x32_bf16 v[28:31], a[232:235], v[152:155], 0\n"
"ds_read_b128 a[212:215], v209 offset:37376\n"
"ds_read_b128 a[216:219], v209 offset:37888\n"
"v_mfma_f32_16x16x32_bf16 v[40:43], a[236:239], v[152:155], 0\n"
"v_mov_b32 v161, v160\n"
"v_mov_b32 v162, v160\n"
"v_mfma_f32_16x16x32_bf16 v[52:55], a[240:243], v[152:155], 0\n"
"v_mov_b32 v163, v160\n"
"ds_read_b128 a[220:223], v209 offset:38400\n"
"v_mfma_f32_16x16x32_bf16 v[64:67], a[244:247], v[152:155], 0\n"
"v_max3_f32 v249, v144, v145, v146\n"
"v_max3_f32 v250, v147, v148, v149\n"
"v_mfma_f32_16x16x32_bf16 v[76:79], a[248:251], v[152:155], 0\n"
"v_max3_f32 v249, v249, v150, v151\n"
"v_max_f32 v249, v249, v250\n"
"v_mfma_f32_16x16x32_bf16 v[88:91], a[252:255], v[152:155], 0\n"
"v_mov_b32 v250, v249\n"
"v_mov_b32 v165, v164\n"
"s_waitcnt lgkmcnt(1)\n"
"v_mfma_f32_16x16x32_bf16 v[152:155], a[192:195], v[96:99], v[160:163]\n"
"v_permlane32_swap_b32 v249, v250\n"
"v_max_f32 v249, v249, v250\n"
"v_mfma_f32_16x16x32_bf16 v[152:155], a[196:199], v[100:103], v[152:155]\n"
"v_mov_b32 v250, v249\n"
"v_mov_b32 v166, v164\n"
"v_mfma_f32_16x16x32_bf16 v[152:155], a[200:203], v[104:107], v[152:155]\n"
"v_permlane16_swap_b32 v249, v250\n"
"v_min_f32_e64 v168, -v249, -v250\n"
"s_add_u32 m0, s77, 0xc000\n"
"v_mfma_f32_16x16x32_bf16 v[152:155], a[204:207], v[108:111], v[152:155]\n"
"buffer_load_dwordx4 v220, s[44:47], s79 offen nt lds\n"
"v_mfma_f32_16x16x32_bf16 v[156:159], a[208:211], v[96:99], v[160:163]\n"
"v_add_f32 v144, v168, v144\n"
"v_add_f32 v145, v168, v145\n"
"v_mfma_f32_16x16x32_bf16 v[156:159], a[212:215], v[100:103], v[156:159]\n"
"v_add_f32 v146, v168, v146\n"
"v_add_f32 v147, v168, v147\n"
"v_add_f32 v148, v168, v148\n"
"v_mfma_f32_16x16x32_bf16 v[156:159], a[216:219], v[104:107], v[156:159]\n"
"v_add_f32 v149, v168, v149\n"
"v_add_f32 v150, v168, v150\n"
"v_add_f32 v151, v168, v151\n"
"s_waitcnt lgkmcnt(0)\n"
"v_mfma_f32_16x16x32_bf16 v[156:159], a[220:223], v[108:111], v[156:159]\n"
"v_exp_f32 v144, v144\n"
"v_exp_f32 v145, v145\n"
"v_exp_f32 v146, v146\n"
"v_exp_f32 v147, v147\n"
"v_exp_f32 v148, v148\n"
"v_exp_f32 v149, v149\n"
"v_exp_f32 v150, v150\n"
"v_exp_f32 v151, v151\n"
"v_cvt_pk_bf16_f32 v144, v144, v145\n"
"v_cvt_pk_bf16_f32 v145, v146, v147\n"
"v_cvt_pk_bf16_f32 v146, v148, v149\n"
"v_cvt_pk_bf16_f32 v147, v150, v151\n"
"s_nop 1\n"
"v_mfma_f32_16x16x32_bf16 v[200:203], v[204:207], v[144:147], 0\n"
"v_mov_b32 v167, v164\n"
"ds_read_b64_tr_b16 v[172:173], v215 offset:33280\n"
"v_mfma_f32_16x16x32_bf16 v[8:11], a[224:227], v[144:147], 0\n"
"ds_read_b64_tr_b16 a[224:225], v212 offset:32768\n"
"ds_read_b64_tr_b16 a[226:227], v212 offset:36864\n"
"v_mfma_f32_16x16x32_bf16 v[20:23], a[228:231], v[144:147], 0\n"
"ds_read_b64_tr_b16 a[228:229], v215 offset:32768\n"
"ds_read_b64_tr_b16 a[230:231], v215 offset:36864\n"
"v_mfma_f32_16x16x32_bf16 v[32:35], a[232:235], v[144:147], 0\n"
"ds_read_b64_tr_b16 a[232:233], v212 offset:33280\n"
"ds_read_b64_tr_b16 a[234:235], v212 offset:37376\n"
"v_mfma_f32_16x16x32_bf16 v[44:47], a[236:239], v[144:147], 0\n"
"ds_read_b64_tr_b16 v[174:175], v215 offset:37376\n"
"ds_read_b64_tr_b16 v[176:177], v212 offset:33792\n"
"v_mfma_f32_16x16x32_bf16 v[56:59], a[240:243], v[144:147], 0\n"
"ds_read_b64_tr_b16 v[178:179], v212 offset:37888\n"
"ds_read_b64_tr_b16 v[180:181], v215 offset:33792\n"
"v_mfma_f32_16x16x32_bf16 v[68:71], a[244:247], v[144:147], 0\n"
"ds_read_b64_tr_b16 v[182:183], v215 offset:37888\n"
"ds_read_b64_tr_b16 v[184:185], v212 offset:34304\n"
"v_mfma_f32_16x16x32_bf16 v[80:83], a[248:251], v[144:147], 0\n"
"v_exp_f32 v152, v152\n"
"v_mfma_f32_16x16x32_bf16 v[92:95], a[252:255], v[144:147], 0\n"
"v_exp_f32 v153, v153\n"
"v_mfma_f32_16x16x32_bf16 v[144:147], a[192:195], v[112:115], v[164:167]\n"
"buffer_load_dwordx4 v220, s[44:47], s79 offen offset:2048 nt lds\n"
"ds_read_b64_tr_b16 v[186:187], v212 offset:38400\n"
"v_mfma_f32_16x16x32_bf16 v[144:147], a[196:199], v[116:119], v[144:147]\n"
"v_exp_f32 v154, v154\n"
"v_mfma_f32_16x16x32_bf16 v[144:147], a[200:203], v[120:123], v[144:147]\n"
"v_exp_f32 v155, v155\n"
"v_mfma_f32_16x16x32_bf16 v[144:147], a[204:207], v[124:127], v[144:147]\n"
"v_exp_f32 v156, v156\n"
"v_mfma_f32_16x16x32_bf16 v[148:151], a[208:211], v[112:115], v[164:167]\n"
"v_exp_f32 v157, v157\n"
"v_mfma_f32_16x16x32_bf16 v[148:151], a[212:215], v[116:119], v[148:151]\n"
"v_exp_f32 v158, v158\n"
"v_mfma_f32_16x16x32_bf16 v[148:151], a[216:219], v[120:123], v[148:151]\n"
"v_exp_f32 v159, v159\n"
"v_cvt_pk_bf16_f32 v152, v152, v153\n"
"v_mfma_f32_16x16x32_bf16 v[148:151], a[220:223], v[124:127], v[148:151]\n"
"v_cvt_pk_bf16_f32 v153, v154, v155\n"
"v_cvt_pk_bf16_f32 v154, v156, v157\n"
"v_cvt_pk_bf16_f32 v155, v158, v159\n"
"s_nop 1\n"
"v_mfma_f32_16x16x32_bf16 v[192:195], v[204:207], v[152:155], v[192:195]\n"
"ds_read_b64_tr_b16 v[188:189], v215 offset:34304\n"
"ds_read_b64_tr_b16 v[190:191], v215 offset:38400\n"
"s_waitcnt lgkmcnt(2)\n"
"v_mfma_f32_16x16x32_bf16 v[0:3], a[224:227], v[152:155], v[0:3]\n"
"v_mov_b32 v169, v168\n"
"v_mov_b32 v170, v168\n"
"v_mfma_f32_16x16x32_bf16 v[12:15], a[228:231], v[152:155], v[12:15]\n"
"v_mov_b32 v171, v168\n"
"v_mfma_f32_16x16x32_bf16 v[24:27], a[232:235], v[152:155], v[24:27]\n"
"v_exp_f32 v144, v144\n"
"v_mfma_f32_16x16x32_bf16 v[36:39], v[172:175], v[152:155], v[36:39]\n"
"v_exp_f32 v145, v145\n"
"v_mfma_f32_16x16x32_bf16 v[48:51], v[176:179], v[152:155], v[48:51]\n"
"v_exp_f32 v146, v146\n"
"s_add_u32 m0, s77, 0xe000\n"
"v_mfma_f32_16x16x32_bf16 v[60:63], v[180:183], v[152:155], v[60:63]\n"
"buffer_load_dwordx4 v220, s[48:51], s79 offen nt lds\n"
"v_mfma_f32_16x16x32_bf16 v[72:75], v[184:187], v[152:155], v[72:75]\n"
"v_exp_f32 v147, v147\n"
"s_waitcnt lgkmcnt(0)\n"
"v_mfma_f32_16x16x32_bf16 v[84:87], v[188:191], v[152:155], v[84:87]\n"
"s_waitcnt vmcnt(15)\n"
"s_barrier\n"
"v_mfma_f32_16x16x32_bf16 v[152:155], a[192:195], v[128:131], v[168:171]\n"
"v_exp_f32 v148, v148\n"
"v_mfma_f32_16x16x32_bf16 v[152:155], a[196:199], v[132:135], v[152:155]\n"
"v_exp_f32 v149, v149\n"
"v_mfma_f32_16x16x32_bf16 v[152:155], a[200:203], v[136:139], v[152:155]\n"
"v_exp_f32 v150, v150\n"
"v_mfma_f32_16x16x32_bf16 v[152:155], a[204:207], v[140:143], v[152:155]\n"
"v_exp_f32 v151, v151\n"
"v_mfma_f32_16x16x32_bf16 v[156:159], a[208:211], v[128:131], v[168:171]\n"
"v_cvt_pk_bf16_f32 v144, v144, v145\n"
"v_cvt_pk_bf16_f32 v145, v146, v147\n"
"v_mfma_f32_16x16x32_bf16 v[156:159], a[212:215], v[132:135], v[156:159]\n"
"v_cvt_pk_bf16_f32 v146, v148, v149\n"
"v_cvt_pk_bf16_f32 v147, v150, v151\n"
"v_mfma_f32_16x16x32_bf16 v[156:159], a[216:219], v[136:139], v[156:159]\n"
"ds_read_b128 a[192:195], v208 offset:16384\n"
"ds_read_b128 a[196:199], v208 offset:16896\n"
"v_mfma_f32_16x16x32_bf16 v[156:159], a[220:223], v[140:143], v[156:159]\n"
"ds_read_b128 a[200:203], v208 offset:17408\n"
"ds_read_b128 a[204:207], v208 offset:17920\n"
"v_mfma_f32_16x16x32_bf16 v[196:199], v[204:207], v[144:147], v[196:199]\n"
"ds_read_b128 a[208:211], v208 offset:20480\n"
"ds_read_b128 a[212:215], v208 offset:20992\n"
"v_mfma_f32_16x16x32_bf16 v[4:7], a[224:227], v[144:147], v[4:7]\n"
"ds_read_b128 a[216:219], v208 offset:21504\n"
"ds_read_b128 a[220:223], v208 offset:22016\n"
"v_mfma_f32_16x16x32_bf16 v[16:19], a[228:231], v[144:147], v[16:19]\n"
"v_exp_f32 v152, v152\n"
"v_mfma_f32_16x16x32_bf16 v[28:31], a[232:235], v[144:147], v[28:31]\n"
"buffer_load_dwordx4 v220, s[48:51], s79 offen offset:2048 nt lds\n"
"s_xor_b32 s79, s100, 0xe000\n"
"v_mfma_f32_16x16x32_bf16 v[40:43], v[172:175], v[144:147], v[40:43]\n"
"v_exp_f32 v153, v153\n"
"v_mfma_f32_16x16x32_bf16 v[52:55], v[176:179], v[144:147], v[52:55]\n"
"v_exp_f32 v154, v154\n"
"v_mfma_f32_16x16x32_bf16 v[64:67], v[180:183], v[144:147], v[64:67]\n"
"v_exp_f32 v155, v155\n"
"v_mfma_f32_16x16x32_bf16 v[76:79], v[184:187], v[144:147], v[76:79]\n"
"v_exp_f32 v156, v156\n"
"v_mfma_f32_16x16x32_bf16 v[88:91], v[188:191], v[144:147], v[88:91]\n"
"v_exp_f32 v157, v157\n"
"s_waitcnt lgkmcnt(0)\n"
"v_mfma_f32_16x16x32_bf16 v[144:147], a[192:195], v[96:99], v[160:163]\n"
"v_exp_f32 v158, v158\n"
"v_mfma_f32_16x16x32_bf16 v[144:147], a[196:199], v[100:103], v[144:147]\n"
"v_exp_f32 v159, v159\n"
"v_mfma_f32_16x16x32_bf16 v[144:147], a[200:203], v[104:107], v[144:147]\n"
"v_cvt_pk_bf16_f32 v152, v152, v153\n"
"v_cvt_pk_bf16_f32 v153, v154, v155\n"
"v_mfma_f32_16x16x32_bf16 v[144:147], a[204:207], v[108:111], v[144:147]\n"
"v_cvt_pk_bf16_f32 v154, v156, v157\n"
"v_cvt_pk_bf16_f32 v155, v158, v159\n"
"v_mfma_f32_16x16x32_bf16 v[148:151], a[208:211], v[96:99], v[160:163]\n"
"ds_read_b64_tr_b16 a[236:237], v214 offset:16896\n"
"ds_read_b64_tr_b16 a[238:239], v214 offset:20992\n"
"v_mfma_f32_16x16x32_bf16 v[148:151], a[212:215], v[100:103], v[148:151]\n"
"ds_read_b64_tr_b16 a[240:241], v211 offset:17408\n"
"ds_read_b64_tr_b16 a[242:243], v211 offset:21504\n"
"v_mfma_f32_16x16x32_bf16 v[148:151], a[216:219], v[104:107], v[148:151]\n"
"ds_read_b64_tr_b16 a[244:245], v214 offset:17408\n"
"ds_read_b64_tr_b16 a[246:247], v214 offset:21504\n"
"v_mfma_f32_16x16x32_bf16 v[148:151], a[220:223], v[108:111], v[148:151]\n"
"ds_read_b64_tr_b16 a[248:249], v211 offset:17920\n"
"ds_read_b64_tr_b16 a[250:251], v211 offset:22016\n"
"s_add_u32 m0, s77, 0x1c000\n"
"v_mfma_f32_16x16x32_bf16 v[200:203], v[204:207], v[152:155], v[200:203]\n"
"buffer_load_dwordx4 v220, s[44:47], s79 offen nt lds\n"
"v_mfma_f32_16x16x32_bf16 v[8:11], a[224:227], v[152:155], v[8:11]\n"
"ds_read_b64_tr_b16 a[224:225], v211 offset:16384\n"
"ds_read_b64_tr_b16 a[226:227], v211 offset:20480\n"
"v_mfma_f32_16x16x32_bf16 v[20:23], a[228:231], v[152:155], v[20:23]\n"
"ds_read_b64_tr_b16 a[228:229], v214 offset:16384\n"
"ds_read_b64_tr_b16 a[230:231], v214 offset:20480\n"
"v_mfma_f32_16x16x32_bf16 v[32:35], a[232:235], v[152:155], v[32:35]\n"
"ds_read_b64_tr_b16 a[232:233], v211 offset:16896\n"
"ds_read_b64_tr_b16 a[234:235], v211 offset:20992\n"
"v_mfma_f32_16x16x32_bf16 v[44:47], v[172:175], v[152:155], v[44:47]\n"
"v_exp_f32 v144, v144\n"
"v_mfma_f32_16x16x32_bf16 v[56:59], v[176:179], v[152:155], v[56:59]\n"
"v_exp_f32 v145, v145\n"
"v_mfma_f32_16x16x32_bf16 v[68:71], v[180:183], v[152:155], v[68:71]\n"
"v_exp_f32 v146, v146\n"
"v_mfma_f32_16x16x32_bf16 v[80:83], v[184:187], v[152:155], v[80:83]\n"
"v_exp_f32 v147, v147\n"
"v_mfma_f32_16x16x32_bf16 v[92:95], v[188:191], v[152:155], v[92:95]\n"
"v_exp_f32 v148, v148\n"
"v_mfma_f32_16x16x32_bf16 v[152:155], a[192:195], v[112:115], v[164:167]\n"
"v_exp_f32 v149, v149\n"
"v_mfma_f32_16x16x32_bf16 v[152:155], a[196:199], v[116:119], v[152:155]\n"
"v_exp_f32 v150, v150\n"
"v_mfma_f32_16x16x32_bf16 v[152:155], a[200:203], v[120:123], v[152:155]\n"
"v_exp_f32 v151, v151\n"
"v_mfma_f32_16x16x32_bf16 v[152:155], a[204:207], v[124:127], v[152:155]\n"
"v_cvt_pk_bf16_f32 v144, v144, v145\n"
"v_cvt_pk_bf16_f32 v145, v146, v147\n"
"v_mfma_f32_16x16x32_bf16 v[156:159], a[208:211], v[112:115], v[164:167]\n"
"v_cvt_pk_bf16_f32 v146, v148, v149\n"
"v_cvt_pk_bf16_f32 v147, v150, v151\n"
"v_mfma_f32_16x16x32_bf16 v[156:159], a[212:215], v[116:119], v[156:159]\n"
"buffer_load_dwordx4 v220, s[44:47], s79 offen offset:2048 nt lds\n"
"ds_read_b64_tr_b16 a[252:253], v214 offset:17920\n"
"v_mfma_f32_16x16x32_bf16 v[156:159], a[216:219], v[120:123], v[156:159]\n"
"ds_read_b64_tr_b16 a[254:255], v214 offset:22016\n"
"v_mfma_f32_16x16x32_bf16 v[156:159], a[220:223], v[124:127], v[156:159]\n"
"v_mfma_f32_16x16x32_bf16 v[192:195], v[204:207], v[144:147], v[192:195]\n"
"v_exp_f32 v152, v152\n"
"s_waitcnt lgkmcnt(2)\n"
"v_mfma_f32_16x16x32_bf16 v[0:3], a[224:227], v[144:147], v[0:3]\n"
"v_exp_f32 v153, v153\n"
"v_mfma_f32_16x16x32_bf16 v[12:15], a[228:231], v[144:147], v[12:15]\n"
"v_exp_f32 v154, v154\n"
"v_mfma_f32_16x16x32_bf16 v[24:27], a[232:235], v[144:147], v[24:27]\n"
"v_exp_f32 v155, v155\n"
"v_mfma_f32_16x16x32_bf16 v[36:39], a[236:239], v[144:147], v[36:39]\n"
"v_exp_f32 v156, v156\n"
"v_mfma_f32_16x16x32_bf16 v[48:51], a[240:243], v[144:147], v[48:51]\n"
"v_exp_f32 v157, v157\n"
"v_mfma_f32_16x16x32_bf16 v[60:63], a[244:247], v[144:147], v[60:63]\n"
"v_exp_f32 v158, v158\n"
"v_mfma_f32_16x16x32_bf16 v[72:75], a[248:251], v[144:147], v[72:75]\n"
"v_exp_f32 v159, v159\n"
"s_waitcnt lgkmcnt(0)\n"
"v_mfma_f32_16x16x32_bf16 v[84:87], a[252:255], v[144:147], v[84:87]\n"
"s_waitcnt vmcnt(14)\n"
"s_barrier\n"
"v_mfma_f32_16x16x32_bf16 v[144:147], a[192:195], v[128:131], v[168:171]\n"
"v_cvt_pk_bf16_f32 v152, v152, v153\n"
"v_cvt_pk_bf16_f32 v153, v154, v155\n"
"v_mfma_f32_16x16x32_bf16 v[144:147], a[196:199], v[132:135], v[144:147]\n"
"v_cvt_pk_bf16_f32 v154, v156, v157\n"
"v_cvt_pk_bf16_f32 v155, v158, v159\n"
"s_add_u32 m0, s77, 0x1e000\n"
"v_mfma_f32_16x16x32_bf16 v[144:147], a[200:203], v[136:139], v[144:147]\n"
"buffer_load_dwordx4 v220, s[48:51], s79 offen nt lds\n"
"v_mfma_f32_16x16x32_bf16 v[144:147], a[204:207], v[140:143], v[144:147]\n"
"ds_read_b128 a[192:195], v209 offset:0\n"
"ds_read_b128 a[196:199], v209 offset:512\n"
"v_mfma_f32_16x16x32_bf16 v[148:151], a[208:211], v[128:131], v[168:171]\n"
"ds_read_b128 a[200:203], v209 offset:1024\n"
"ds_read_b128 a[204:207], v209 offset:1536\n"
"v_mfma_f32_16x16x32_bf16 v[148:151], a[212:215], v[132:135], v[148:151]\n"
"ds_read_b128 a[208:211], v209 offset:4096\n"
"ds_read_b128 a[212:215], v209 offset:4608\n"
"v_mfma_f32_16x16x32_bf16 v[148:151], a[216:219], v[136:139], v[148:151]\n"
"ds_read_b128 a[216:219], v209 offset:5120\n"
"ds_read_b64_tr_b16 v[172:173], v215 offset:512\n"
"v_mfma_f32_16x16x32_bf16 v[148:151], a[220:223], v[140:143], v[148:151]\n"
"ds_read_b128 a[220:223], v209 offset:5632\n"
"ds_read_b64_tr_b16 v[174:175], v215 offset:4608\n"
"v_mfma_f32_16x16x32_bf16 v[196:199], v[204:207], v[152:155], v[196:199]\n"
"v_exp_f32 v144, v144\n"
"v_mfma_f32_16x16x32_bf16 v[4:7], a[224:227], v[152:155], v[4:7]\n"
"v_exp_f32 v145, v145\n"
"v_mfma_f32_16x16x32_bf16 v[16:19], a[228:231], v[152:155], v[16:19]\n"
"v_exp_f32 v146, v146\n"
"v_mfma_f32_16x16x32_bf16 v[28:31], a[232:235], v[152:155], v[28:31]\n"
"v_exp_f32 v147, v147\n"
"v_mfma_f32_16x16x32_bf16 v[40:43], a[236:239], v[152:155], v[40:43]\n"
"v_exp_f32 v148, v148\n"
"v_mfma_f32_16x16x32_bf16 v[52:55], a[240:243], v[152:155], v[52:55]\n"
"v_exp_f32 v149, v149\n"
"v_mfma_f32_16x16x32_bf16 v[64:67], a[244:247], v[152:155], v[64:67]\n"
"v_exp_f32 v150, v150\n"
"v_mfma_f32_16x16x32_bf16 v[76:79], a[248:251], v[152:155], v[76:79]\n"
"v_exp_f32 v151, v151\n"
"v_mfma_f32_16x16x32_bf16 v[88:91], a[252:255], v[152:155], v[88:91]\n"
"buffer_load_dwordx4 v220, s[48:51], s79 offen offset:2048 nt lds\n"
"s_xor_b32 s79, s100, 0x10000\n"
"s_waitcnt lgkmcnt(0)\n"
"v_mfma_f32_16x16x32_bf16 v[152:155], a[192:195], v[96:99], v[160:163]\n"
"v_cvt_pk_bf16_f32 v144, v144, v145\n"
"v_cvt_pk_bf16_f32 v145, v146, v147\n"
"v_mfma_f32_16x16x32_bf16 v[152:155], a[196:199], v[100:103], v[152:155]\n"
"v_cvt_pk_bf16_f32 v146, v148, v149\n"
"v_cvt_pk_bf16_f32 v147, v150, v151\n"
"v_mfma_f32_16x16x32_bf16 v[152:155], a[200:203], v[104:107], v[152:155]\n"
"ds_read_b64_tr_b16 v[176:177], v212 offset:1024\n"
"ds_read_b64_tr_b16 v[178:179], v212 offset:5120\n"
"v_mfma_f32_16x16x32_bf16 v[152:155], a[204:207], v[108:111], v[152:155]\n"
"ds_read_b64_tr_b16 v[180:181], v215 offset:1024\n"
"ds_read_b64_tr_b16 v[182:183], v215 offset:5120\n"
"v_mfma_f32_16x16x32_bf16 v[156:159], a[208:211], v[96:99], v[160:163]\n"
"ds_read_b64_tr_b16 v[184:185], v212 offset:1536\n"
"ds_read_b64_tr_b16 v[186:187], v212 offset:5632\n"
"v_mfma_f32_16x16x32_bf16 v[156:159], a[212:215], v[100:103], v[156:159]\n"
"ds_read_b64_tr_b16 v[188:189], v215 offset:1536\n"
"ds_read_b64_tr_b16 v[190:191], v215 offset:5632\n"
"v_mfma_f32_16x16x32_bf16 v[156:159], a[216:219], v[104:107], v[156:159]\n"
"v_mfma_f32_16x16x32_bf16 v[156:159], a[220:223], v[108:111], v[156:159]\n"
"v_mfma_f32_16x16x32_bf16 v[200:203], v[204:207], v[144:147], v[200:203]\n"
"v_exp_f32 v152, v152\n"
"v_mfma_f32_16x16x32_bf16 v[8:11], a[224:227], v[144:147], v[8:11]\n"
"ds_read_b64_tr_b16 a[224:225], v212 offset:0\n"
"ds_read_b64_tr_b16 a[226:227], v212 offset:4096\n"
"v_mfma_f32_16x16x32_bf16 v[20:23], a[228:231], v[144:147], v[20:23]\n"
"ds_read_b64_tr_b16 a[228:229], v215 offset:0\n"
"ds_read_b64_tr_b16 a[230:231], v215 offset:4096\n"
"v_mfma_f32_16x16x32_bf16 v[32:35], a[232:235], v[144:147], v[32:35]\n"
"ds_read_b64_tr_b16 a[232:233], v212 offset:512\n"
"ds_read_b64_tr_b16 a[234:235], v212 offset:4608\n"
"v_mfma_f32_16x16x32_bf16 v[44:47], a[236:239], v[144:147], v[44:47]\n"
"v_exp_f32 v153, v153\n"
"s_add_u32 m0, s77, 0x20000\n"
"v_mfma_f32_16x16x32_bf16 v[56:59], a[240:243], v[144:147], v[56:59]\n"
"buffer_load_dwordx4 v220, s[44:47], s79 offen nt lds\n"
"v_mfma_f32_16x16x32_bf16 v[68:71], a[244:247], v[144:147], v[68:71]\n"
"v_exp_f32 v154, v154\n"
"v_mfma_f32_16x16x32_bf16 v[80:83], a[248:251], v[144:147], v[80:83]\n"
"v_exp_f32 v155, v155\n"
"v_mfma_f32_16x16x32_bf16 v[92:95], a[252:255], v[144:147], v[92:95]\n"
"v_exp_f32 v156, v156\n"
"v_mfma_f32_16x16x32_bf16 v[144:147], a[192:195], v[112:115], v[164:167]\n"
"v_exp_f32 v157, v157\n"
"v_mfma_f32_16x16x32_bf16 v[144:147], a[196:199], v[116:119], v[144:147]\n"
"v_exp_f32 v158, v158\n"
"v_mfma_f32_16x16x32_bf16 v[144:147], a[200:203], v[120:123], v[144:147]\n"
"v_exp_f32 v159, v159\n"
"v_mfma_f32_16x16x32_bf16 v[144:147], a[204:207], v[124:127], v[144:147]\n"
"v_cvt_pk_bf16_f32 v152, v152, v153\n"
"v_cvt_pk_bf16_f32 v153, v154, v155\n"
"v_mfma_f32_16x16x32_bf16 v[148:151], a[208:211], v[112:115], v[164:167]\n"
"v_cvt_pk_bf16_f32 v154, v156, v157\n"
"v_cvt_pk_bf16_f32 v155, v158, v159\n"
"v_mfma_f32_16x16x32_bf16 v[148:151], a[212:215], v[116:119], v[148:151]\n"
"v_mfma_f32_16x16x32_bf16 v[148:151], a[216:219], v[120:123], v[148:151]\n"
"v_mfma_f32_16x16x32_bf16 v[148:151], a[220:223], v[124:127], v[148:151]\n"
"v_mfma_f32_16x16x32_bf16 v[192:195], v[204:207], v[152:155], v[192:195]\n"
"v_exp_f32 v144, v144\n"
"s_waitcnt lgkmcnt(0)\n"
"v_mfma_f32_16x16x32_bf16 v[0:3], a[224:227], v[152:155], v[0:3]\n"
"v_exp_f32 v145, v145\n"
"v_mfma_f32_16x16x32_bf16 v[12:15], a[228:231], v[152:155], v[12:15]\n"
"buffer_load_dwordx4 v220, s[44:47], s79 offen offset:2048 nt lds\n"
"v_cvt_pk_bf16_f32 v144, v144, v145\n"
"v_mfma_f32_16x16x32_bf16 v[24:27], a[232:235], v[152:155], v[24:27]\n"
"v_exp_f32 v146, v146\n"
"v_mfma_f32_16x16x32_bf16 v[36:39], v[172:175], v[152:155], v[36:39]\n"
"v_exp_f32 v147, v147\n"
"v_mfma_f32_16x16x32_bf16 v[48:51], v[176:179], v[152:155], v[48:51]\n"
"v_exp_f32 v148, v148\n"
"v_mfma_f32_16x16x32_bf16 v[60:63], v[180:183], v[152:155], v[60:63]\n"
"v_exp_f32 v149, v149\n"
"v_mfma_f32_16x16x32_bf16 v[72:75], v[184:187], v[152:155], v[72:75]\n"
"v_exp_f32 v150, v150\n"
"v_mfma_f32_16x16x32_bf16 v[84:87], v[188:191], v[152:155], v[84:87]\n"
"s_waitcnt vmcnt(14)\n"
"s_barrier\n"
"v_mfma_f32_16x16x32_bf16 v[152:155], a[192:195], v[128:131], v[168:171]\n"
"v_exp_f32 v151, v151\n"
"v_mfma_f32_16x16x32_bf16 v[152:155], a[196:199], v[132:135], v[152:155]\n"
"v_cvt_pk_bf16_f32 v145, v146, v147\n"
"v_cvt_pk_bf16_f32 v146, v148, v149\n"
"v_mfma_f32_16x16x32_bf16 v[152:155], a[200:203], v[136:139], v[152:155]\n"
"v_cvt_pk_bf16_f32 v147, v150, v151\n"
"ds_read_b128 a[192:195], v208 offset:0\n"
"v_mfma_f32_16x16x32_bf16 v[152:155], a[204:207], v[140:143], v[152:155]\n"
"ds_read_b128 a[196:199], v208 offset:512\n"
"ds_read_b128 a[200:203], v208 offset:1024\n"
"v_mfma_f32_16x16x32_bf16 v[156:159], a[208:211], v[128:131], v[168:171]\n"
"ds_read_b128 a[204:207], v208 offset:1536\n"
"ds_read_b128 a[208:211], v208 offset:4096\n"
"v_mfma_f32_16x16x32_bf16 v[156:159], a[212:215], v[132:135], v[156:159]\n"
"ds_read_b128 a[212:215], v208 offset:4608\n"
"ds_read_b64_tr_b16 a[236:237], v214 offset:512\n"
"v_mfma_f32_16x16x32_bf16 v[156:159], a[216:219], v[136:139], v[156:159]\n"
"ds_read_b128 a[216:219], v208 offset:5120\n"
"ds_read_b64_tr_b16 a[238:239], v214 offset:4608\n"
"s_add_u32 m0, s77, 0x22000\n"
"v_mfma_f32_16x16x32_bf16 v[156:159], a[220:223], v[140:143], v[156:159]\n"
"buffer_load_dwordx4 v220, s[48:51], s79 offen nt lds\n"
"v_mfma_f32_16x16x32_bf16 v[196:199], v[204:207], v[144:147], v[196:199]\n"
"ds_read_b128 a[220:223], v208 offset:5632\n"
"ds_read_b64_tr_b16 a[240:241], v211 offset:1024\n"
"v_mfma_f32_16x16x32_bf16 v[4:7], a[224:227], v[144:147], v[4:7]\n"
"v_exp_f32 v152, v152\n"
"v_mfma_f32_16x16x32_bf16 v[16:19], a[228:231], v[144:147], v[16:19]\n"
"v_exp_f32 v153, v153\n"
"v_mfma_f32_16x16x32_bf16 v[28:31], a[232:235], v[144:147], v[28:31]\n"
"v_exp_f32 v154, v154\n"
"v_mfma_f32_16x16x32_bf16 v[40:43], v[172:175], v[144:147], v[40:43]\n"
"v_exp_f32 v155, v155\n"
"v_mfma_f32_16x16x32_bf16 v[52:55], v[176:179], v[144:147], v[52:55]\n"
"v_exp_f32 v156, v156\n"
"v_mfma_f32_16x16x32_bf16 v[64:67], v[180:183], v[144:147], v[64:67]\n"
"v_exp_f32 v157, v157\n"
"v_mfma_f32_16x16x32_bf16 v[76:79], v[184:187], v[144:147], v[76:79]\n"
"v_exp_f32 v158, v158\n"
"v_mfma_f32_16x16x32_bf16 v[88:91], v[188:191], v[144:147], v[88:91]\n"
"v_exp_f32 v159, v159\n"
"s_waitcnt lgkmcnt(0)\n"
"v_mfma_f32_16x16x32_bf16 v[144:147], a[192:195], v[96:99], v[160:163]\n"
"v_cvt_pk_bf16_f32 v152, v152, v153\n"
"v_cvt_pk_bf16_f32 v153, v154, v155\n"
"v_mfma_f32_16x16x32_bf16 v[144:147], a[196:199], v[100:103], v[144:147]\n"
"v_cvt_pk_bf16_f32 v154, v156, v157\n"
"v_cvt_pk_bf16_f32 v155, v158, v159\n"
"v_mfma_f32_16x16x32_bf16 v[144:147], a[200:203], v[104:107], v[144:147]\n"
"ds_read_b64_tr_b16 a[242:243], v211 offset:5120\n"
"ds_read_b64_tr_b16 a[244:245], v214 offset:1024\n"
"v_mfma_f32_16x16x32_bf16 v[144:147], a[204:207], v[108:111], v[144:147]\n"
"ds_read_b64_tr_b16 a[246:247], v214 offset:5120\n"
"ds_read_b64_tr_b16 a[248:249], v211 offset:1536\n"
"v_mfma_f32_16x16x32_bf16 v[148:151], a[208:211], v[96:99], v[160:163]\n"
"buffer_load_dwordx4 v220, s[48:51], s79 offen offset:2048 nt lds\n"
"s_xor_b32 s79, s100, 0x12000\n"
"v_mfma_f32_16x16x32_bf16 v[148:151], a[212:215], v[100:103], v[148:151]\n"
"ds_read_b64_tr_b16 a[250:251], v211 offset:5632\n"
"ds_read_b64_tr_b16 a[252:253], v214 offset:1536\n"
"v_mfma_f32_16x16x32_bf16 v[148:151], a[216:219], v[104:107], v[148:151]\n"
"ds_read_b64_tr_b16 a[254:255], v214 offset:5632\n"
"v_mfma_f32_16x16x32_bf16 v[148:151], a[220:223], v[108:111], v[148:151]\n"
"v_mfma_f32_16x16x32_bf16 v[200:203], v[204:207], v[152:155], v[200:203]\n"
"v_exp_f32 v144, v144\n"
"v_mfma_f32_16x16x32_bf16 v[8:11], a[224:227], v[152:155], v[8:11]\n"
"ds_read_b64_tr_b16 a[224:225], v211 offset:0\n"
"ds_read_b64_tr_b16 a[226:227], v211 offset:4096\n"
"v_mfma_f32_16x16x32_bf16 v[20:23], a[228:231], v[152:155], v[20:23]\n"
"ds_read_b64_tr_b16 a[228:229], v214 offset:0\n"
"ds_read_b64_tr_b16 a[230:231], v214 offset:4096\n"
"v_mfma_f32_16x16x32_bf16 v[32:35], a[232:235], v[152:155], v[32:35]\n"
"ds_read_b64_tr_b16 a[232:233], v211 offset:512\n"
"ds_read_b64_tr_b16 a[234:235], v211 offset:4608\n"
"v_mfma_f32_16x16x32_bf16 v[44:47], v[172:175], v[152:155], v[44:47]\n"
"v_exp_f32 v145, v145\n"
"v_mfma_f32_16x16x32_bf16 v[56:59], v[176:179], v[152:155], v[56:59]\n"
"v_exp_f32 v146, v146\n"
"v_mfma_f32_16x16x32_bf16 v[68:71], v[180:183], v[152:155], v[68:71]\n"
"v_exp_f32 v147, v147\n"
"v_mfma_f32_16x16x32_bf16 v[80:83], v[184:187], v[152:155], v[80:83]\n"
"v_exp_f32 v148, v148\n"
"v_mfma_f32_16x16x32_bf16 v[92:95], v[188:191], v[152:155], v[92:95]\n"
"v_exp_f32 v149, v149\n"
"v_mfma_f32_16x16x32_bf16 v[152:155], a[192:195], v[112:115], v[164:167]\n"
"v_exp_f32 v150, v150\n"
"s_add_u32 m0, s77, 0x24000\n"
"v_mfma_f32_16x16x32_bf16 v[152:155], a[196:199], v[116:119], v[152:155]\n"
"buffer_load_dwordx4 v220, s[44:47], s79 offen nt lds\n"
"v_mfma_f32_16x16x32_bf16 v[152:155], a[200:203], v[120:123], v[152:155]\n"
"v_exp_f32 v151, v151\n"
"v_mfma_f32_16x16x32_bf16 v[152:155], a[204:207], v[124:127], v[152:155]\n"
"v_cvt_pk_bf16_f32 v144, v144, v145\n"
"v_cvt_pk_bf16_f32 v145, v146, v147\n"
"v_mfma_f32_16x16x32_bf16 v[156:159], a[208:211], v[112:115], v[164:167]\n"
"v_cvt_pk_bf16_f32 v146, v148, v149\n"
"v_cvt_pk_bf16_f32 v147, v150, v151\n"
"v_mfma_f32_16x16x32_bf16 v[156:159], a[212:215], v[116:119], v[156:159]\n"
"v_mfma_f32_16x16x32_bf16 v[156:159], a[216:219], v[120:123], v[156:159]\n"
"v_mfma_f32_16x16x32_bf16 v[156:159], a[220:223], v[124:127], v[156:159]\n"
"v_mfma_f32_16x16x32_bf16 v[192:195], v[204:207], v[144:147], v[192:195]\n"
"v_exp_f32 v152, v152\n"
"s_waitcnt lgkmcnt(0)\n"
"v_mfma_f32_16x16x32_bf16 v[0:3], a[224:227], v[144:147], v[0:3]\n"
"v_exp_f32 v153, v153\n"
"v_mfma_f32_16x16x32_bf16 v[12:15], a[228:231], v[144:147], v[12:15]\n"
"v_exp_f32 v154, v154\n"
"v_mfma_f32_16x16x32_bf16 v[24:27], a[232:235], v[144:147], v[24:27]\n"
"v_exp_f32 v155, v155\n"
"v_mfma_f32_16x16x32_bf16 v[36:39], a[236:239], v[144:147], v[36:39]\n"
"v_exp_f32 v156, v156\n"
"v_mfma_f32_16x16x32_bf16 v[48:51], a[240:243], v[144:147], v[48:51]\n"
"v_exp_f32 v157, v157\n"
"v_mfma_f32_16x16x32_bf16 v[60:63], a[244:247], v[144:147], v[60:63]\n"
"v_exp_f32 v158, v158\n"
"v_mfma_f32_16x16x32_bf16 v[72:75], a[248:251], v[144:147], v[72:75]\n"
"buffer_load_dwordx4 v220, s[44:47], s79 offen offset:2048 nt lds\n"
"v_cvt_pk_bf16_f32 v152, v152, v153\n"
"v_mfma_f32_16x16x32_bf16 v[84:87], a[252:255], v[144:147], v[84:87]\n"
"s_waitcnt vmcnt(14)\n"
"s_barrier\n"
"v_mfma_f32_16x16x32_bf16 v[144:147], a[192:195], v[128:131], v[168:171]\n"
"v_exp_f32 v159, v159\n"
"v_mfma_f32_16x16x32_bf16 v[144:147], a[196:199], v[132:135], v[144:147]\n"
"v_cvt_pk_bf16_f32 v153, v154, v155\n"
"v_cvt_pk_bf16_f32 v154, v156, v157\n"
"v_mfma_f32_16x16x32_bf16 v[144:147], a[200:203], v[136:139], v[144:147]\n"
"v_cvt_pk_bf16_f32 v155, v158, v159\n"
"ds_read_b128 a[192:195], v208 offset:32768\n"
"v_mfma_f32_16x16x32_bf16 v[144:147], a[204:207], v[140:143], v[144:147]\n"
"ds_read_b128 a[196:199], v208 offset:33280\n"
"ds_read_b128 a[200:203], v208 offset:33792\n"
"v_mfma_f32_16x16x32_bf16 v[148:151], a[208:211], v[128:131], v[168:171]\n"
"ds_read_b128 a[204:207], v208 offset:34304\n"
"ds_read_b128 a[208:211], v208 offset:36864\n"
"v_mfma_f32_16x16x32_bf16 v[148:151], a[212:215], v[132:135], v[148:151]\n"
"ds_read_b128 a[212:215], v208 offset:37376\n"
"ds_read_b64_tr_b16 v[172:173], v214 offset:33280\n"
"v_mfma_f32_16x16x32_bf16 v[148:151], a[216:219], v[136:139], v[148:151]\n"
"ds_read_b128 a[216:219], v208 offset:37888\n"
"ds_read_b64_tr_b16 v[174:175], v214 offset:37376\n"
"v_mfma_f32_16x16x32_bf16 v[148:151], a[220:223], v[140:143], v[148:151]\n"
"ds_read_b128 a[220:223], v208 offset:38400\n"
"ds_read_b64_tr_b16 v[176:177], v211 offset:33792\n"
"v_mfma_f32_16x16x32_bf16 v[196:199], v[204:207], v[152:155], v[196:199]\n"
"v_exp_f32 v144, v144\n"
"v_mfma_f32_16x16x32_bf16 v[4:7], a[224:227], v[152:155], v[4:7]\n"
"v_exp_f32 v145, v145\n"
"v_mfma_f32_16x16x32_bf16 v[16:19], a[228:231], v[152:155], v[16:19]\n"
"v_exp_f32 v146, v146\n"
"v_mfma_f32_16x16x32_bf16 v[28:31], a[232:235], v[152:155], v[28:31]\n"
"v_exp_f32 v147, v147\n"
"s_add_u32 m0, s77, 0x26000\n"
"v_mfma_f32_16x16x32_bf16 v[40:43], a[236:239], v[152:155], v[40:43]\n"
"buffer_load_dwordx4 v220, s[48:51], s79 offen nt lds\n"
"v_mfma_f32_16x16x32_bf16 v[52:55], a[240:243], v[152:155], v[52:55]\n"
"v_exp_f32 v148, v148\n"
"v_mfma_f32_16x16x32_bf16 v[64:67], a[244:247], v[152:155], v[64:67]\n"
"v_exp_f32 v149, v149\n"
"v_mfma_f32_16x16x32_bf16 v[76:79], a[248:251], v[152:155], v[76:79]\n"
"v_exp_f32 v150, v150\n"
"v_mfma_f32_16x16x32_bf16 v[88:91], a[252:255], v[152:155], v[88:91]\n"
"v_exp_f32 v151, v151\n"
"s_waitcnt lgkmcnt(0)\n"
"v_mfma_f32_16x16x32_bf16 v[152:155], a[192:195], v[96:99], v[160:163]\n"
"v_cvt_pk_bf16_f32 v144, v144, v145\n"
"v_cvt_pk_bf16_f32 v145, v146, v147\n"
"v_mfma_f32_16x16x32_bf16 v[152:155], a[196:199], v[100:103], v[152:155]\n"
"v_cvt_pk_bf16_f32 v146, v148, v149\n"
"v_cvt_pk_bf16_f32 v147, v150, v151\n"
"v_mfma_f32_16x16x32_bf16 v[152:155], a[200:203], v[104:107], v[152:155]\n"
"ds_read_b64_tr_b16 v[178:179], v211 offset:37888\n"
"ds_read_b64_tr_b16 v[180:181], v214 offset:33792\n"
"v_mfma_f32_16x16x32_bf16 v[152:155], a[204:207], v[108:111], v[152:155]\n"
"ds_read_b64_tr_b16 v[182:183], v214 offset:37888\n"
"ds_read_b64_tr_b16 v[184:185], v211 offset:34304\n"
"v_mfma_f32_16x16x32_bf16 v[156:159], a[208:211], v[96:99], v[160:163]\n"
"ds_read_b64_tr_b16 v[186:187], v211 offset:38400\n"
"ds_read_b64_tr_b16 v[188:189], v214 offset:34304\n"
"v_mfma_f32_16x16x32_bf16 v[156:159], a[212:215], v[100:103], v[156:159]\n"
"ds_read_b64_tr_b16 v[190:191], v214 offset:38400\n"
"v_mfma_f32_16x16x32_bf16 v[156:159], a[216:219], v[104:107], v[156:159]\n"
"v_mfma_f32_16x16x32_bf16 v[156:159], a[220:223], v[108:111], v[156:159]\n"
"v_mfma_f32_16x16x32_bf16 v[200:203], v[204:207], v[144:147], v[200:203]\n"
"v_exp_f32 v152, v152\n"
"v_mfma_f32_16x16x32_bf16 v[8:11], a[224:227], v[144:147], v[8:11]\n"
"buffer_load_dwordx4 v220, s[48:51], s79 offen offset:2048 nt lds\n"
"s_xor_b32 s79, s100, 0x14000\n"
"v_mfma_f32_16x16x32_bf16 v[20:23], a[228:231], v[144:147], v[20:23]\n"
"ds_read_b64_tr_b16 a[224:225], v211 offset:32768\n"
"ds_read_b64_tr_b16 a[226:227], v211 offset:36864\n"
"v_mfma_f32_16x16x32_bf16 v[32:35], a[232:235], v[144:147], v[32:35]\n"
"ds_read_b64_tr_b16 a[228:229], v214 offset:32768\n"
"ds_read_b64_tr_b16 a[230:231], v214 offset:36864\n"
"v_mfma_f32_16x16x32_bf16 v[44:47], a[236:239], v[144:147], v[44:47]\n"
"ds_read_b64_tr_b16 a[232:233], v211 offset:33280\n"
"ds_read_b64_tr_b16 a[234:235], v211 offset:37376\n"
"v_mfma_f32_16x16x32_bf16 v[56:59], a[240:243], v[144:147], v[56:59]\n"
"v_exp_f32 v153, v153\n"
"v_mfma_f32_16x16x32_bf16 v[68:71], a[244:247], v[144:147], v[68:71]\n"
"v_exp_f32 v154, v154\n"
"v_mfma_f32_16x16x32_bf16 v[80:83], a[248:251], v[144:147], v[80:83]\n"
"v_exp_f32 v155, v155\n"
"v_mfma_f32_16x16x32_bf16 v[92:95], a[252:255], v[144:147], v[92:95]\n"
"v_exp_f32 v156, v156\n"
"v_mfma_f32_16x16x32_bf16 v[144:147], a[192:195], v[112:115], v[164:167]\n"
"v_exp_f32 v157, v157\n"
"v_mfma_f32_16x16x32_bf16 v[144:147], a[196:199], v[116:119], v[144:147]\n"
"v_exp_f32 v158, v158\n"
"v_mfma_f32_16x16x32_bf16 v[144:147], a[200:203], v[120:123], v[144:147]\n"
"v_exp_f32 v159, v159\n"
"v_mfma_f32_16x16x32_bf16 v[144:147], a[204:207], v[124:127], v[144:147]\n"
"v_cvt_pk_bf16_f32 v152, v152, v153\n"
"v_cvt_pk_bf16_f32 v153, v154, v155\n"
"v_mfma_f32_16x16x32_bf16 v[148:151], a[208:211], v[112:115], v[164:167]\n"
"v_cvt_pk_bf16_f32 v154, v156, v157\n"
"v_cvt_pk_bf16_f32 v155, v158, v159\n"
"v_mfma_f32_16x16x32_bf16 v[148:151], a[212:215], v[116:119], v[148:151]\n"
"s_add_u32 m0, s77, 0x14000\n"
"v_mfma_f32_16x16x32_bf16 v[148:151], a[216:219], v[120:123], v[148:151]\n"
"buffer_load_dwordx4 v220, s[44:47], s79 offen nt lds\n"
"v_mfma_f32_16x16x32_bf16 v[148:151], a[220:223], v[124:127], v[148:151]\n"
"v_mfma_f32_16x16x32_bf16 v[192:195], v[204:207], v[152:155], v[192:195]\n"
"v_exp_f32 v144, v144\n"
"s_waitcnt lgkmcnt(0)\n"
"v_mfma_f32_16x16x32_bf16 v[0:3], a[224:227], v[152:155], v[0:3]\n"
"v_exp_f32 v145, v145\n"
"v_mfma_f32_16x16x32_bf16 v[12:15], a[228:231], v[152:155], v[12:15]\n"
"v_exp_f32 v146, v146\n"
"v_mfma_f32_16x16x32_bf16 v[24:27], a[232:235], v[152:155], v[24:27]\n"
"v_exp_f32 v147, v147\n"
"v_mfma_f32_16x16x32_bf16 v[36:39], v[172:175], v[152:155], v[36:39]\n"
"v_exp_f32 v148, v148\n"
"v_mfma_f32_16x16x32_bf16 v[48:51], v[176:179], v[152:155], v[48:51]\n"
"v_exp_f32 v149, v149\n"
"v_mfma_f32_16x16x32_bf16 v[60:63], v[180:183], v[152:155], v[60:63]\n"
"v_exp_f32 v150, v150\n"
"v_mfma_f32_16x16x32_bf16 v[72:75], v[184:187], v[152:155], v[72:75]\n"
"v_exp_f32 v151, v151\n"
"v_mfma_f32_16x16x32_bf16 v[84:87], v[188:191], v[152:155], v[84:87]\n"
"s_waitcnt vmcnt(13)\n"
"s_barrier\n"
"v_mfma_f32_16x16x32_bf16 v[152:155], a[192:195], v[128:131], v[168:171]\n"
"v_cvt_pk_bf16_f32 v144, v144, v145\n"
"v_cvt_pk_bf16_f32 v145, v146, v147\n"
"v_mfma_f32_16x16x32_bf16 v[152:155], a[196:199], v[132:135], v[152:155]\n"
"v_cvt_pk_bf16_f32 v146, v148, v149\n"
"v_cvt_pk_bf16_f32 v147, v150, v151\n"
"v_mfma_f32_16x16x32_bf16 v[152:155], a[200:203], v[136:139], v[152:155]\n"
"ds_read_b128 a[192:195], v208 offset:49152\n"
"ds_read_b128 a[196:199], v208 offset:49664\n"
"v_mfma_f32_16x16x32_bf16 v[152:155], a[204:207], v[140:143], v[152:155]\n"
"buffer_load_dwordx4 v220, s[44:47], s79 offen offset:2048 nt lds\n"
"ds_read_b128 a[200:203], v208 offset:50176\n"
"v_mfma_f32_16x16x32_bf16 v[156:159], a[208:211], v[128:131], v[168:171]\n"
"ds_read_b128 a[204:207], v208 offset:50688\n"
"ds_read_b128 a[208:211], v208 offset:53248\n"
"v_mfma_f32_16x16x32_bf16 v[156:159], a[212:215], v[132:135], v[156:159]\n"
"ds_read_b128 a[212:215], v208 offset:53760\n"
"ds_read_b64_tr_b16 a[236:237], v214 offset:49664\n"
"v_mfma_f32_16x16x32_bf16 v[156:159], a[216:219], v[136:139], v[156:159]\n"
"ds_read_b128 a[216:219], v208 offset:54272\n"
"ds_read_b64_tr_b16 a[238:239], v214 offset:53760\n"
"v_mfma_f32_16x16x32_bf16 v[156:159], a[220:223], v[140:143], v[156:159]\n"
"ds_read_b128 a[220:223], v208 offset:54784\n"
"ds_read_b64_tr_b16 a[240:241], v211 offset:50176\n"
"v_mfma_f32_16x16x32_bf16 v[196:199], v[204:207], v[144:147], v[196:199]\n"
"v_exp_f32 v152, v152\n"
"v_mfma_f32_16x16x32_bf16 v[4:7], a[224:227], v[144:147], v[4:7]\n"
"v_exp_f32 v153, v153\n"
"v_mfma_f32_16x16x32_bf16 v[16:19], a[228:231], v[144:147], v[16:19]\n"
"v_exp_f32 v154, v154\n"
"v_mfma_f32_16x16x32_bf16 v[28:31], a[232:235], v[144:147], v[28:31]\n"
"v_exp_f32 v155, v155\n"
"v_mfma_f32_16x16x32_bf16 v[40:43], v[172:175], v[144:147], v[40:43]\n"
"v_exp_f32 v156, v156\n"
"v_mfma_f32_16x16x32_bf16 v[52:55], v[176:179], v[144:147], v[52:55]\n"
"v_exp_f32 v157, v157\n"
"v_mfma_f32_16x16x32_bf16 v[64:67], v[180:183], v[144:147], v[64:67]\n"
"v_exp_f32 v158, v158\n"
"v_mfma_f32_16x16x32_bf16 v[76:79], v[184:187], v[144:147], v[76:79]\n"
"v_exp_f32 v159, v159\n"
"v_mfma_f32_16x16x32_bf16 v[88:91], v[188:191], v[144:147], v[88:91]\n"
"v_cvt_pk_bf16_f32 v152, v152, v153\n"
"v_cvt_pk_bf16_f32 v153, v154, v155\n"
"s_add_u32 m0, s77, 0x16000\n"
"s_waitcnt lgkmcnt(0)\n"
"v_mfma_f32_16x16x32_bf16 v[144:147], a[192:195], v[96:99], v[160:163]\n"
"buffer_load_dwordx4 v220, s[48:51], s79 offen nt lds\n"
"v_mfma_f32_16x16x32_bf16 v[144:147], a[196:199], v[100:103], v[144:147]\n"
"v_cvt_pk_bf16_f32 v154, v156, v157\n"
"v_cvt_pk_bf16_f32 v155, v158, v159\n"
"v_mfma_f32_16x16x32_bf16 v[144:147], a[200:203], v[104:107], v[144:147]\n"
"ds_read_b64_tr_b16 a[242:243], v211 offset:54272\n"
"ds_read_b64_tr_b16 a[244:245], v214 offset:50176\n"
"v_mfma_f32_16x16x32_bf16 v[144:147], a[204:207], v[108:111], v[144:147]\n"
"ds_read_b64_tr_b16 a[246:247], v214 offset:54272\n"
"ds_read_b64_tr_b16 a[248:249], v211 offset:50688\n"
"v_mfma_f32_16x16x32_bf16 v[148:151], a[208:211], v[96:99], v[160:163]\n"
"ds_read_b64_tr_b16 a[250:251], v211 offset:54784\n"
"ds_read_b64_tr_b16 a[252:253], v214 offset:50688\n"
"v_mfma_f32_16x16x32_bf16 v[148:151], a[212:215], v[100:103], v[148:151]\n"
"ds_read_b64_tr_b16 a[254:255], v214 offset:54784\n"
"v_mfma_f32_16x16x32_bf16 v[148:151], a[216:219], v[104:107], v[148:151]\n"
"v_mfma_f32_16x16x32_bf16 v[148:151], a[220:223], v[108:111], v[148:151]\n"
"v_mfma_f32_16x16x32_bf16 v[200:203], v[204:207], v[152:155], v[200:203]\n"
"v_exp_f32 v144, v144\n"
"v_mfma_f32_16x16x32_bf16 v[8:11], a[224:227], v[152:155], v[8:11]\n"
"ds_read_b64_tr_b16 a[224:225], v211 offset:49152\n"
"ds_read_b64_tr_b16 a[226:227], v211 offset:53248\n"
"v_mfma_f32_16x16x32_bf16 v[20:23], a[228:231], v[152:155], v[20:23]\n"
"ds_read_b64_tr_b16 a[228:229], v214 offset:49152\n"
"ds_read_b64_tr_b16 a[230:231], v214 offset:53248\n"
"v_mfma_f32_16x16x32_bf16 v[32:35], a[232:235], v[152:155], v[32:35]\n"
"ds_read_b64_tr_b16 a[232:233], v211 offset:49664\n"
"ds_read_b64_tr_b16 a[234:235], v211 offset:53760\n"
"v_mfma_f32_16x16x32_bf16 v[44:47], v[172:175], v[152:155], v[44:47]\n"
"v_exp_f32 v145, v145\n"
"v_mfma_f32_16x16x32_bf16 v[56:59], v[176:179], v[152:155], v[56:59]\n"
"v_exp_f32 v146, v146\n"
"v_mfma_f32_16x16x32_bf16 v[68:71], v[180:183], v[152:155], v[68:71]\n"
"buffer_load_dwordx4 v220, s[48:51], s79 offen offset:2048 nt lds\n"
"s_xor_b32 s79, s100, 0x16000\n"
"v_mfma_f32_16x16x32_bf16 v[80:83], v[184:187], v[152:155], v[80:83]\n"
"v_exp_f32 v147, v147\n"
"v_mfma_f32_16x16x32_bf16 v[92:95], v[188:191], v[152:155], v[92:95]\n"
"v_exp_f32 v148, v148\n"
"v_mfma_f32_16x16x32_bf16 v[152:155], a[192:195], v[112:115], v[164:167]\n"
"v_exp_f32 v149, v149\n"
"v_mfma_f32_16x16x32_bf16 v[152:155], a[196:199], v[116:119], v[152:155]\n"
"v_exp_f32 v150, v150\n"
"v_mfma_f32_16x16x32_bf16 v[152:155], a[200:203], v[120:123], v[152:155]\n"
"v_exp_f32 v151, v151\n"
"v_mfma_f32_16x16x32_bf16 v[152:155], a[204:207], v[124:127], v[152:155]\n"
"v_cvt_pk_bf16_f32 v144, v144, v145\n"
"v_cvt_pk_bf16_f32 v145, v146, v147\n"
"v_mfma_f32_16x16x32_bf16 v[156:159], a[208:211], v[112:115], v[164:167]\n"
"v_cvt_pk_bf16_f32 v146, v148, v149\n"
"v_cvt_pk_bf16_f32 v147, v150, v151\n"
"v_mfma_f32_16x16x32_bf16 v[156:159], a[212:215], v[116:119], v[156:159]\n"
"v_mfma_f32_16x16x32_bf16 v[156:159], a[216:219], v[120:123], v[156:159]\n"
"v_mfma_f32_16x16x32_bf16 v[156:159], a[220:223], v[124:127], v[156:159]\n"
"v_mfma_f32_16x16x32_bf16 v[192:195], v[204:207], v[144:147], v[192:195]\n"
"v_exp_f32 v152, v152\n"
"s_waitcnt lgkmcnt(0)\n"
"v_mfma_f32_16x16x32_bf16 v[0:3], a[224:227], v[144:147], v[0:3]\n"
"v_exp_f32 v153, v153\n"
"v_mfma_f32_16x16x32_bf16 v[12:15], a[228:231], v[144:147], v[12:15]\n"
"v_exp_f32 v154, v154\n"
"s_add_u32 m0, s77, 0x18000\n"
"v_mfma_f32_16x16x32_bf16 v[24:27], a[232:235], v[144:147], v[24:27]\n"
"buffer_load_dwordx4 v220, s[44:47], s79 offen nt lds\n"
"v_mfma_f32_16x16x32_bf16 v[36:39], a[236:239], v[144:147], v[36:39]\n"
"v_exp_f32 v155, v155\n"
"v_mfma_f32_16x16x32_bf16 v[48:51], a[240:243], v[144:147], v[48:51]\n"
"v_exp_f32 v156, v156\n"
"v_mfma_f32_16x16x32_bf16 v[60:63], a[244:247], v[144:147], v[60:63]\n"
"v_exp_f32 v157, v157\n"
"v_mfma_f32_16x16x32_bf16 v[72:75], a[248:251], v[144:147], v[72:75]\n"
"v_exp_f32 v158, v158\n"
"v_mfma_f32_16x16x32_bf16 v[84:87], a[252:255], v[144:147], v[84:87]\n"
"s_waitcnt vmcnt(13)\n"
"s_barrier\n"
"v_mfma_f32_16x16x32_bf16 v[144:147], a[192:195], v[128:131], v[168:171]\n"
"v_exp_f32 v159, v159\n"
"v_mfma_f32_16x16x32_bf16 v[144:147], a[196:199], v[132:135], v[144:147]\n"
"v_cvt_pk_bf16_f32 v152, v152, v153\n"
"v_cvt_pk_bf16_f32 v153, v154, v155\n"
"v_mfma_f32_16x16x32_bf16 v[144:147], a[200:203], v[136:139], v[144:147]\n"
"v_cvt_pk_bf16_f32 v154, v156, v157\n"
"v_cvt_pk_bf16_f32 v155, v158, v159\n"
"v_mfma_f32_16x16x32_bf16 v[144:147], a[204:207], v[140:143], v[144:147]\n"
"ds_read_b128 a[192:195], v209 offset:49152\n"
"ds_read_b128 a[196:199], v209 offset:49664\n"
"v_mfma_f32_16x16x32_bf16 v[148:151], a[208:211], v[128:131], v[168:171]\n"
"ds_read_b128 a[200:203], v209 offset:50176\n"
"ds_read_b128 a[204:207], v209 offset:50688\n"
"v_mfma_f32_16x16x32_bf16 v[148:151], a[212:215], v[132:135], v[148:151]\n"
"ds_read_b128 a[208:211], v209 offset:53248\n"
"ds_read_b128 a[212:215], v209 offset:53760\n"
"v_mfma_f32_16x16x32_bf16 v[148:151], a[216:219], v[136:139], v[148:151]\n"
"ds_read_b128 a[216:219], v209 offset:54272\n"
"ds_read_b64_tr_b16 v[172:173], v215 offset:49664\n"
"v_mfma_f32_16x16x32_bf16 v[148:151], a[220:223], v[140:143], v[148:151]\n"
"ds_read_b128 a[220:223], v209 offset:54784\n"
"ds_read_b64_tr_b16 v[174:175], v215 offset:53760\n"
"v_mfma_f32_16x16x32_bf16 v[196:199], v[204:207], v[152:155], v[196:199]\n"
"buffer_load_dwordx4 v220, s[44:47], s79 offen offset:2048 nt lds\n"
"ds_read_b64_tr_b16 v[176:177], v212 offset:50176\n"
"v_mfma_f32_16x16x32_bf16 v[4:7], a[224:227], v[152:155], v[4:7]\n"
"v_exp_f32 v144, v144\n"
"v_mfma_f32_16x16x32_bf16 v[16:19], a[228:231], v[152:155], v[16:19]\n"
"v_exp_f32 v145, v145\n"
"v_mfma_f32_16x16x32_bf16 v[28:31], a[232:235], v[152:155], v[28:31]\n"
"v_exp_f32 v146, v146\n"
"v_mfma_f32_16x16x32_bf16 v[40:43], a[236:239], v[152:155], v[40:43]\n"
"v_exp_f32 v147, v147\n"
"v_mfma_f32_16x16x32_bf16 v[52:55], a[240:243], v[152:155], v[52:55]\n"
"v_exp_f32 v148, v148\n"
"v_mfma_f32_16x16x32_bf16 v[64:67], a[244:247], v[152:155], v[64:67]\n"
"v_exp_f32 v149, v149\n"
"v_mfma_f32_16x16x32_bf16 v[76:79], a[248:251], v[152:155], v[76:79]\n"
"v_exp_f32 v150, v150\n"
"v_mfma_f32_16x16x32_bf16 v[88:91], a[252:255], v[152:155], v[88:91]\n"
"v_exp_f32 v151, v151\n"
"s_waitcnt lgkmcnt(0)\n"
"v_mfma_f32_16x16x32_bf16 v[152:155], a[192:195], v[96:99], v[160:163]\n"
"v_cvt_pk_bf16_f32 v144, v144, v145\n"
"v_cvt_pk_bf16_f32 v145, v146, v147\n"
"v_mfma_f32_16x16x32_bf16 v[152:155], a[196:199], v[100:103], v[152:155]\n"
"v_cvt_pk_bf16_f32 v146, v148, v149\n"
"v_cvt_pk_bf16_f32 v147, v150, v151\n"
"v_mfma_f32_16x16x32_bf16 v[152:155], a[200:203], v[104:107], v[152:155]\n"
"ds_read_b64_tr_b16 v[178:179], v212 offset:54272\n"
"ds_read_b64_tr_b16 v[180:181], v215 offset:50176\n"
"v_mfma_f32_16x16x32_bf16 v[152:155], a[204:207], v[108:111], v[152:155]\n"
"ds_read_b64_tr_b16 v[182:183], v215 offset:54272\n"
"ds_read_b64_tr_b16 v[184:185], v212 offset:50688\n"
"v_mfma_f32_16x16x32_bf16 v[156:159], a[208:211], v[96:99], v[160:163]\n"
"ds_read_b64_tr_b16 v[186:187], v212 offset:54784\n"
"ds_read_b64_tr_b16 v[188:189], v215 offset:50688\n"
"s_add_u32 m0, s77, 0x1a000\n"
"v_mfma_f32_16x16x32_bf16 v[156:159], a[212:215], v[100:103], v[156:159]\n"
"buffer_load_dwordx4 v220, s[48:51], s79 offen nt lds\n"
"v_mfma_f32_16x16x32_bf16 v[156:159], a[216:219], v[104:107], v[156:159]\n"
"ds_read_b64_tr_b16 v[190:191], v215 offset:54784\n"
"v_mfma_f32_16x16x32_bf16 v[156:159], a[220:223], v[108:111], v[156:159]\n"
"v_mfma_f32_16x16x32_bf16 v[200:203], v[204:207], v[144:147], v[200:203]\n"
"v_exp_f32 v152, v152\n"
"v_mfma_f32_16x16x32_bf16 v[8:11], a[224:227], v[144:147], v[8:11]\n"
"ds_read_b64_tr_b16 a[224:225], v212 offset:49152\n"
"ds_read_b64_tr_b16 a[226:227], v212 offset:53248\n"
"v_mfma_f32_16x16x32_bf16 v[20:23], a[228:231], v[144:147], v[20:23]\n"
"ds_read_b64_tr_b16 a[228:229], v215 offset:49152\n"
"ds_read_b64_tr_b16 a[230:231], v215 offset:53248\n"
"v_mfma_f32_16x16x32_bf16 v[32:35], a[232:235], v[144:147], v[32:35]\n"
"ds_read_b64_tr_b16 a[232:233], v212 offset:49664\n"
"ds_read_b64_tr_b16 a[234:235], v212 offset:53760\n"
"v_mfma_f32_16x16x32_bf16 v[44:47], a[236:239], v[144:147], v[44:47]\n"
"v_exp_f32 v153, v153\n"
"v_mfma_f32_16x16x32_bf16 v[56:59], a[240:243], v[144:147], v[56:59]\n"
"v_exp_f32 v154, v154\n"
"v_mfma_f32_16x16x32_bf16 v[68:71], a[244:247], v[144:147], v[68:71]\n"
"v_exp_f32 v155, v155\n"
"v_mfma_f32_16x16x32_bf16 v[80:83], a[248:251], v[144:147], v[80:83]\n"
"v_exp_f32 v156, v156\n"
"v_mfma_f32_16x16x32_bf16 v[92:95], a[252:255], v[144:147], v[92:95]\n"
"v_exp_f32 v157, v157\n"
"v_mfma_f32_16x16x32_bf16 v[144:147], a[192:195], v[112:115], v[164:167]\n"
"v_exp_f32 v158, v158\n"
"v_mfma_f32_16x16x32_bf16 v[144:147], a[196:199], v[116:119], v[144:147]\n"
"v_exp_f32 v159, v159\n"
"v_mfma_f32_16x16x32_bf16 v[144:147], a[200:203], v[120:123], v[144:147]\n"
"buffer_load_dwordx4 v220, s[48:51], s79 offen offset:2048 nt lds\n"
"s_xor_b32 s79, s100, 0x18000\n"
"v_mfma_f32_16x16x32_bf16 v[144:147], a[204:207], v[124:127], v[144:147]\n"
"v_cvt_pk_bf16_f32 v152, v152, v153\n"
"v_cvt_pk_bf16_f32 v153, v154, v155\n"
"v_mfma_f32_16x16x32_bf16 v[148:151], a[208:211], v[112:115], v[164:167]\n"
"v_cvt_pk_bf16_f32 v154, v156, v157\n"
"v_cvt_pk_bf16_f32 v155, v158, v159\n"
"v_mfma_f32_16x16x32_bf16 v[148:151], a[212:215], v[116:119], v[148:151]\n"
"v_mfma_f32_16x16x32_bf16 v[148:151], a[216:219], v[120:123], v[148:151]\n"
"v_mfma_f32_16x16x32_bf16 v[148:151], a[220:223], v[124:127], v[148:151]\n"
"v_mfma_f32_16x16x32_bf16 v[192:195], v[204:207], v[152:155], v[192:195]\n"
"v_exp_f32 v144, v144\n"
"s_waitcnt lgkmcnt(0)\n"
"v_mfma_f32_16x16x32_bf16 v[0:3], a[224:227], v[152:155], v[0:3]\n"
"v_exp_f32 v145, v145\n"
"v_mfma_f32_16x16x32_bf16 v[12:15], a[228:231], v[152:155], v[12:15]\n"
"v_exp_f32 v146, v146\n"
"v_mfma_f32_16x16x32_bf16 v[24:27], a[232:235], v[152:155], v[24:27]\n"
"v_exp_f32 v147, v147\n"
"v_mfma_f32_16x16x32_bf16 v[36:39], v[172:175], v[152:155], v[36:39]\n"
"v_exp_f32 v148, v148\n"
"v_mfma_f32_16x16x32_bf16 v[48:51], v[176:179], v[152:155], v[48:51]\n"
"v_exp_f32 v149, v149\n"
"v_mfma_f32_16x16x32_bf16 v[60:63], v[180:183], v[152:155], v[60:63]\n"
"v_exp_f32 v150, v150\n"
"v_mfma_f32_16x16x32_bf16 v[72:75], v[184:187], v[152:155], v[72:75]\n"
"v_exp_f32 v151, v151\n"
"s_add_u32 m0, s77, 0x4000\n"
"v_mfma_f32_16x16x32_bf16 v[84:87], v[188:191], v[152:155], v[84:87]\n"
"buffer_load_dwordx4 v220, s[44:47], s79 offen nt lds\n"
"s_waitcnt vmcnt(13)\n"
"s_barrier\n"
"v_mfma_f32_16x16x32_bf16 v[152:155], a[192:195], v[128:131], v[168:171]\n"
"v_cvt_pk_bf16_f32 v144, v144, v145\n"
"v_cvt_pk_bf16_f32 v145, v146, v147\n"
"v_mfma_f32_16x16x32_bf16 v[152:155], a[196:199], v[132:135], v[152:155]\n"
"v_cvt_pk_bf16_f32 v146, v148, v149\n"
"v_cvt_pk_bf16_f32 v147, v150, v151\n"
"v_mfma_f32_16x16x32_bf16 v[152:155], a[200:203], v[136:139], v[152:155]\n"
"ds_read_b128 a[192:195], v210 offset:0\n"
"ds_read_b128 a[196:199], v210 offset:512\n"
"v_mfma_f32_16x16x32_bf16 v[152:155], a[204:207], v[140:143], v[152:155]\n"
"ds_read_b128 a[200:203], v210 offset:1024\n"
"ds_read_b128 a[204:207], v210 offset:1536\n"
"v_mfma_f32_16x16x32_bf16 v[156:159], a[208:211], v[128:131], v[168:171]\n"
"ds_read_b128 a[208:211], v210 offset:4096\n"
"ds_read_b64_tr_b16 a[236:237], v216 offset:512\n"
"v_mfma_f32_16x16x32_bf16 v[156:159], a[212:215], v[132:135], v[156:159]\n"
"ds_read_b128 a[212:215], v210 offset:4608\n"
"ds_read_b64_tr_b16 a[238:239], v216 offset:4608\n"
"v_mfma_f32_16x16x32_bf16 v[156:159], a[216:219], v[136:139], v[156:159]\n"
"ds_read_b128 a[216:219], v210 offset:5120\n"
"ds_read_b64_tr_b16 a[240:241], v213 offset:1024\n"
"v_mfma_f32_16x16x32_bf16 v[156:159], a[220:223], v[140:143], v[156:159]\n"
"ds_read_b128 a[220:223], v210 offset:5632\n"
"ds_read_b64_tr_b16 a[242:243], v213 offset:5120\n"
"v_mfma_f32_16x16x32_bf16 v[196:199], v[204:207], v[144:147], v[196:199]\n"
"v_exp_f32 v152, v152\n"
"v_mfma_f32_16x16x32_bf16 v[4:7], a[224:227], v[144:147], v[4:7]\n"
"v_exp_f32 v153, v153\n"
"v_mfma_f32_16x16x32_bf16 v[16:19], a[228:231], v[144:147], v[16:19]\n"
"v_exp_f32 v154, v154\n"
"v_mfma_f32_16x16x32_bf16 v[28:31], a[232:235], v[144:147], v[28:31]\n"
"v_exp_f32 v155, v155\n"
"v_mfma_f32_16x16x32_bf16 v[40:43], v[172:175], v[144:147], v[40:43]\n"
"v_exp_f32 v156, v156\n"
"v_mfma_f32_16x16x32_bf16 v[52:55], v[176:179], v[144:147], v[52:55]\n"
"buffer_load_dwordx4 v220, s[44:47], s79 offen offset:2048 nt lds\n"
"v_cvt_pk_bf16_f32 v152, v152, v153\n"
"v_mfma_f32_16x16x32_bf16 v[64:67], v[180:183], v[144:147], v[64:67]\n"
"v_exp_f32 v157, v157\n"
"v_mfma_f32_16x16x32_bf16 v[76:79], v[184:187], v[144:147], v[76:79]\n"
"v_exp_f32 v158, v158\n"
"v_mfma_f32_16x16x32_bf16 v[88:91], v[188:191], v[144:147], v[88:91]\n"
"v_exp_f32 v159, v159\n"
"s_waitcnt lgkmcnt(0)\n"
"v_mfma_f32_16x16x32_bf16 v[144:147], a[192:195], v[96:99], v[160:163]\n"
"v_cvt_pk_bf16_f32 v153, v154, v155\n"
"v_cvt_pk_bf16_f32 v154, v156, v157\n"
"v_mfma_f32_16x16x32_bf16 v[144:147], a[196:199], v[100:103], v[144:147]\n"
"v_cvt_pk_bf16_f32 v155, v158, v159\n"
"ds_read_b64_tr_b16 a[244:245], v216 offset:1024\n"
"v_mfma_f32_16x16x32_bf16 v[144:147], a[200:203], v[104:107], v[144:147]\n"
"ds_read_b64_tr_b16 a[246:247], v216 offset:5120\n"
"ds_read_b64_tr_b16 a[248:249], v213 offset:1536\n"
"v_mfma_f32_16x16x32_bf16 v[144:147], a[204:207], v[108:111], v[144:147]\n"
"ds_read_b64_tr_b16 a[250:251], v213 offset:5632\n"
"ds_read_b64_tr_b16 a[252:253], v216 offset:1536\n"
"v_mfma_f32_16x16x32_bf16 v[148:151], a[208:211], v[96:99], v[160:163]\n"
"ds_read_b64_tr_b16 a[254:255], v216 offset:5632\n"
"v_mfma_f32_16x16x32_bf16 v[148:151], a[212:215], v[100:103], v[148:151]\n"
"v_mfma_f32_16x16x32_bf16 v[148:151], a[216:219], v[104:107], v[148:151]\n"
"v_mfma_f32_16x16x32_bf16 v[148:151], a[220:223], v[108:111], v[148:151]\n"
"v_mfma_f32_16x16x32_bf16 v[200:203], v[204:207], v[152:155], v[200:203]\n"
"v_exp_f32 v144, v144\n"
"v_mfma_f32_16x16x32_bf16 v[8:11], a[224:227], v[152:155], v[8:11]\n"
"ds_read_b64_tr_b16 a[224:225], v213 offset:0\n"
"ds_read_b64_tr_b16 a[226:227], v213 offset:4096\n"
"s_add_u32 m0, s77, 0x6000\n"
"v_mfma_f32_16x16x32_bf16 v[20:23], a[228:231], v[152:155], v[20:23]\n"
"buffer_load_dwordx4 v220, s[48:51], s79 offen nt lds\n"
"v_mfma_f32_16x16x32_bf16 v[32:35], a[232:235], v[152:155], v[32:35]\n"
"ds_read_b64_tr_b16 a[228:229], v216 offset:0\n"
"ds_read_b64_tr_b16 a[230:231], v216 offset:4096\n"
"v_mfma_f32_16x16x32_bf16 v[44:47], v[172:175], v[152:155], v[44:47]\n"
"ds_read_b64_tr_b16 a[232:233], v213 offset:512\n"
"ds_read_b64_tr_b16 a[234:235], v213 offset:4608\n"
"v_mfma_f32_16x16x32_bf16 v[56:59], v[176:179], v[152:155], v[56:59]\n"
"v_exp_f32 v145, v145\n"
"v_mfma_f32_16x16x32_bf16 v[68:71], v[180:183], v[152:155], v[68:71]\n"
"v_exp_f32 v146, v146\n"
"v_mfma_f32_16x16x32_bf16 v[80:83], v[184:187], v[152:155], v[80:83]\n"
"v_exp_f32 v147, v147\n"
"v_mfma_f32_16x16x32_bf16 v[92:95], v[188:191], v[152:155], v[92:95]\n"
"v_exp_f32 v148, v148\n"
"v_mfma_f32_16x16x32_bf16 v[152:155], a[192:195], v[112:115], v[164:167]\n"
"v_exp_f32 v149, v149\n"
"v_mfma_f32_16x16x32_bf16 v[152:155], a[196:199], v[116:119], v[152:155]\n"
"v_exp_f32 v150, v150\n"
"v_mfma_f32_16x16x32_bf16 v[152:155], a[200:203], v[120:123], v[152:155]\n"
"v_exp_f32 v151, v151\n"
"v_mfma_f32_16x16x32_bf16 v[152:155], a[204:207], v[124:127], v[152:155]\n"
"v_cvt_pk_bf16_f32 v144, v144, v145\n"
"v_cvt_pk_bf16_f32 v145, v146, v147\n"
"v_mfma_f32_16x16x32_bf16 v[156:159], a[208:211], v[112:115], v[164:167]\n"
"v_cvt_pk_bf16_f32 v146, v148, v149\n"
"v_cvt_pk_bf16_f32 v147, v150, v151\n"
"v_mfma_f32_16x16x32_bf16 v[156:159], a[212:215], v[116:119], v[156:159]\n"
"v_mfma_f32_16x16x32_bf16 v[156:159], a[216:219], v[120:123], v[156:159]\n"
"v_mfma_f32_16x16x32_bf16 v[156:159], a[220:223], v[124:127], v[156:159]\n"
"buffer_load_dwordx4 v220, s[48:51], s79 offen offset:2048 nt lds\n"
"s_xor_b32 s79, s100, 0x1a000\n"
"v_mfma_f32_16x16x32_bf16 v[192:195], v[204:207], v[144:147], v[192:195]\n"
"v_exp_f32 v152, v152\n"
"s_waitcnt lgkmcnt(0)\n"
"v_mfma_f32_16x16x32_bf16 v[0:3], a[224:227], v[144:147], v[0:3]\n"
"v_exp_f32 v153, v153\n"
"v_mfma_f32_16x16x32_bf16 v[12:15], a[228:231], v[144:147], v[12:15]\n"
"v_exp_f32 v154, v154\n"
"v_mfma_f32_16x16x32_bf16 v[24:27], a[232:235], v[144:147], v[24:27]\n"
"v_exp_f32 v155, v155\n"
"v_mfma_f32_16x16x32_bf16 v[36:39], a[236:239], v[144:147], v[36:39]\n"
"v_exp_f32 v156, v156\n"
"v_mfma_f32_16x16x32_bf16 v[48:51], a[240:243], v[144:147], v[48:51]\n"
"v_exp_f32 v157, v157\n"
"v_mfma_f32_16x16x32_bf16 v[60:63], a[244:247], v[144:147], v[60:63]\n"
"v_exp_f32 v158, v158\n"
"v_mfma_f32_16x16x32_bf16 v[72:75], a[248:251], v[144:147], v[72:75]\n"
"v_exp_f32 v159, v159\n"
"v_mfma_f32_16x16x32_bf16 v[84:87], a[252:255], v[144:147], v[84:87]\n"
"s_waitcnt vmcnt(12)\n"
"s_barrier\n"
"v_mfma_f32_16x16x32_bf16 v[144:147], a[192:195], v[128:131], v[168:171]\n"
"v_cvt_pk_bf16_f32 v152, v152, v153\n"
"v_cvt_pk_bf16_f32 v153, v154, v155\n"
"v_mfma_f32_16x16x32_bf16 v[144:147], a[196:199], v[132:135], v[144:147]\n"
"v_cvt_pk_bf16_f32 v154, v156, v157\n"
"v_cvt_pk_bf16_f32 v155, v158, v159\n"
"v_mfma_f32_16x16x32_bf16 v[144:147], a[200:203], v[136:139], v[144:147]\n"
"ds_read_b128 a[192:195], v210 offset:16384\n"
"ds_read_b128 a[196:199], v210 offset:16896\n"
"v_mfma_f32_16x16x32_bf16 v[144:147], a[204:207], v[140:143], v[144:147]\n"
"ds_read_b128 a[200:203], v210 offset:17408\n"
"ds_read_b128 a[204:207], v210 offset:17920\n"
"s_add_u32 m0, s77, 0x10000\n"
"v_mfma_f32_16x16x32_bf16 v[148:151], a[208:211], v[128:131], v[168:171]\n"
"buffer_load_dwordx4 v220, s[44:47], s79 offen nt lds\n"
"v_mfma_f32_16x16x32_bf16 v[148:151], a[212:215], v[132:135], v[148:151]\n"
"ds_read_b128 a[208:211], v210 offset:20480\n"
"ds_read_b128 a[212:215], v210 offset:20992\n"
"v_mfma_f32_16x16x32_bf16 v[148:151], a[216:219], v[136:139], v[148:151]\n"
"ds_read_b128 a[216:219], v210 offset:21504\n"
"ds_read_b64_tr_b16 v[172:173], v216 offset:16896\n"
"v_mfma_f32_16x16x32_bf16 v[148:151], a[220:223], v[140:143], v[148:151]\n"
"ds_read_b128 a[220:223], v210 offset:22016\n"
"ds_read_b64_tr_b16 v[174:175], v216 offset:20992\n"
"v_mfma_f32_16x16x32_bf16 v[196:199], v[204:207], v[152:155], v[196:199]\n"
"v_exp_f32 v144, v144\n"
"v_mfma_f32_16x16x32_bf16 v[4:7], a[224:227], v[152:155], v[4:7]\n"
"v_exp_f32 v145, v145\n"
"v_mfma_f32_16x16x32_bf16 v[16:19], a[228:231], v[152:155], v[16:19]\n"
"v_exp_f32 v146, v146\n"
"v_mfma_f32_16x16x32_bf16 v[28:31], a[232:235], v[152:155], v[28:31]\n"
"v_exp_f32 v147, v147\n"
"v_mfma_f32_16x16x32_bf16 v[40:43], a[236:239], v[152:155], v[40:43]\n"
"v_exp_f32 v148, v148\n"
"v_mfma_f32_16x16x32_bf16 v[52:55], a[240:243], v[152:155], v[52:55]\n"
"v_exp_f32 v149, v149\n"
"v_mfma_f32_16x16x32_bf16 v[64:67], a[244:247], v[152:155], v[64:67]\n"
"v_exp_f32 v150, v150\n"
"v_mfma_f32_16x16x32_bf16 v[76:79], a[248:251], v[152:155], v[76:79]\n"
"v_exp_f32 v151, v151\n"
"v_mfma_f32_16x16x32_bf16 v[88:91], a[252:255], v[152:155], v[88:91]\n"
"v_cvt_pk_bf16_f32 v144, v144, v145\n"
"v_cvt_pk_bf16_f32 v145, v146, v147\n"
"s_waitcnt lgkmcnt(0)\n"
"v_mfma_f32_16x16x32_bf16 v[152:155], a[192:195], v[96:99], v[160:163]\n"
"v_cvt_pk_bf16_f32 v146, v148, v149\n"
"v_cvt_pk_bf16_f32 v147, v150, v151\n"
"v_mfma_f32_16x16x32_bf16 v[152:155], a[196:199], v[100:103], v[152:155]\n"
"buffer_load_dwordx4 v220, s[44:47], s79 offen offset:2048 nt lds\n"
"ds_read_b64_tr_b16 v[176:177], v213 offset:17408\n"
"v_mfma_f32_16x16x32_bf16 v[152:155], a[200:203], v[104:107], v[152:155]\n"
"ds_read_b64_tr_b16 v[178:179], v213 offset:21504\n"
"ds_read_b64_tr_b16 v[180:181], v216 offset:17408\n"
"v_mfma_f32_16x16x32_bf16 v[152:155], a[204:207], v[108:111], v[152:155]\n"
"ds_read_b64_tr_b16 v[182:183], v216 offset:21504\n"
"ds_read_b64_tr_b16 v[184:185], v213 offset:17920\n"
"v_mfma_f32_16x16x32_bf16 v[156:159], a[208:211], v[96:99], v[160:163]\n"
"ds_read_b64_tr_b16 v[186:187], v213 offset:22016\n"
"ds_read_b64_tr_b16 v[188:189], v216 offset:17920\n"
"v_mfma_f32_16x16x32_bf16 v[156:159], a[212:215], v[100:103], v[156:159]\n"
"ds_read_b64_tr_b16 v[190:191], v216 offset:22016\n"
"v_mfma_f32_16x16x32_bf16 v[156:159], a[216:219], v[104:107], v[156:159]\n"
"v_mfma_f32_16x16x32_bf16 v[156:159], a[220:223], v[108:111], v[156:159]\n"
"v_mfma_f32_16x16x32_bf16 v[200:203], v[204:207], v[144:147], v[200:203]\n"
"v_exp_f32 v152, v152\n"
"v_mfma_f32_16x16x32_bf16 v[8:11], a[224:227], v[144:147], v[8:11]\n"
"ds_read_b64_tr_b16 a[224:225], v213 offset:16384\n"
"ds_read_b64_tr_b16 a[226:227], v213 offset:20480\n"
"v_mfma_f32_16x16x32_bf16 v[20:23], a[228:231], v[144:147], v[20:23]\n"
"ds_read_b64_tr_b16 a[228:229], v216 offset:16384\n"
"ds_read_b64_tr_b16 a[230:231], v216 offset:20480\n"
"v_mfma_f32_16x16x32_bf16 v[32:35], a[232:235], v[144:147], v[32:35]\n"
"ds_read_b64_tr_b16 a[232:233], v213 offset:16896\n"
"ds_read_b64_tr_b16 a[234:235], v213 offset:20992\n"
"v_mfma_f32_16x16x32_bf16 v[44:47], a[236:239], v[144:147], v[44:47]\n"
"v_exp_f32 v153, v153\n"
"v_mfma_f32_16x16x32_bf16 v[56:59], a[240:243], v[144:147], v[56:59]\n"
"v_exp_f32 v154, v154\n"
"v_mfma_f32_16x16x32_bf16 v[68:71], a[244:247], v[144:147], v[68:71]\n"
"v_exp_f32 v155, v155\n"
"s_add_u32 m0, s77, 0x12000\n"
"v_mfma_f32_16x16x32_bf16 v[80:83], a[248:251], v[144:147], v[80:83]\n"
"buffer_load_dwordx4 v220, s[48:51], s79 offen nt lds\n"
"v_mfma_f32_16x16x32_bf16 v[92:95], a[252:255], v[144:147], v[92:95]\n"
"v_exp_f32 v156, v156\n"
"v_mfma_f32_16x16x32_bf16 v[144:147], a[192:195], v[112:115], v[164:167]\n"
"v_exp_f32 v157, v157\n"
"v_mfma_f32_16x16x32_bf16 v[144:147], a[196:199], v[116:119], v[144:147]\n"
"v_exp_f32 v158, v158\n"
"v_mfma_f32_16x16x32_bf16 v[144:147], a[200:203], v[120:123], v[144:147]\n"
"v_exp_f32 v159, v159\n"
"v_mfma_f32_16x16x32_bf16 v[144:147], a[204:207], v[124:127], v[144:147]\n"
"v_cvt_pk_bf16_f32 v152, v152, v153\n"
"v_cvt_pk_bf16_f32 v153, v154, v155\n"
"v_mfma_f32_16x16x32_bf16 v[148:151], a[208:211], v[112:115], v[164:167]\n"
"v_cvt_pk_bf16_f32 v154, v156, v157\n"
"v_cvt_pk_bf16_f32 v155, v158, v159\n"
"v_mfma_f32_16x16x32_bf16 v[148:151], a[212:215], v[116:119], v[148:151]\n"
"v_mfma_f32_16x16x32_bf16 v[148:151], a[216:219], v[120:123], v[148:151]\n"
"v_mfma_f32_16x16x32_bf16 v[148:151], a[220:223], v[124:127], v[148:151]\n"
"v_mfma_f32_16x16x32_bf16 v[192:195], v[204:207], v[152:155], v[192:195]\n"
"v_exp_f32 v144, v144\n"
"s_waitcnt lgkmcnt(0)\n"
"v_mfma_f32_16x16x32_bf16 v[0:3], a[224:227], v[152:155], v[0:3]\n"
"v_exp_f32 v145, v145\n"
"v_mfma_f32_16x16x32_bf16 v[12:15], a[228:231], v[152:155], v[12:15]\n"
"v_exp_f32 v146, v146\n"
"v_mfma_f32_16x16x32_bf16 v[24:27], a[232:235], v[152:155], v[24:27]\n"
"v_exp_f32 v147, v147\n"
"v_mfma_f32_16x16x32_bf16 v[36:39], v[172:175], v[152:155], v[36:39]\n"
"buffer_load_dwordx4 v220, s[48:51], s79 offen offset:2048 nt lds\n"
"s_xor_b32 s79, s100, 0x1c000\n"
"v_mfma_f32_16x16x32_bf16 v[48:51], v[176:179], v[152:155], v[48:51]\n"
"v_exp_f32 v148, v148\n"
"v_mfma_f32_16x16x32_bf16 v[60:63], v[180:183], v[152:155], v[60:63]\n"
"v_exp_f32 v149, v149\n"
"v_mfma_f32_16x16x32_bf16 v[72:75], v[184:187], v[152:155], v[72:75]\n"
"v_exp_f32 v150, v150\n"
"v_mfma_f32_16x16x32_bf16 v[84:87], v[188:191], v[152:155], v[84:87]\n"
"s_waitcnt vmcnt(12)\n"
"s_barrier\n"
"v_mfma_f32_16x16x32_bf16 v[152:155], a[192:195], v[128:131], v[168:171]\n"
"v_exp_f32 v151, v151\n"
"v_mfma_f32_16x16x32_bf16 v[152:155], a[196:199], v[132:135], v[152:155]\n"
"v_cvt_pk_bf16_f32 v144, v144, v145\n"
"v_cvt_pk_bf16_f32 v145, v146, v147\n"
"v_mfma_f32_16x16x32_bf16 v[152:155], a[200:203], v[136:139], v[152:155]\n"
"v_cvt_pk_bf16_f32 v146, v148, v149\n"
"v_cvt_pk_bf16_f32 v147, v150, v151\n"
"v_mfma_f32_16x16x32_bf16 v[152:155], a[204:207], v[140:143], v[152:155]\n"
"ds_read_b128 a[192:195], v209 offset:16384\n"
"ds_read_b128 a[196:199], v209 offset:16896\n"
"v_mfma_f32_16x16x32_bf16 v[156:159], a[208:211], v[128:131], v[168:171]\n"
"ds_read_b128 a[200:203], v209 offset:17408\n"
"ds_read_b128 a[204:207], v209 offset:17920\n"
"v_mfma_f32_16x16x32_bf16 v[156:159], a[212:215], v[132:135], v[156:159]\n"
"ds_read_b128 a[208:211], v209 offset:20480\n"
"ds_read_b128 a[212:215], v209 offset:20992\n"
"v_mfma_f32_16x16x32_bf16 v[156:159], a[216:219], v[136:139], v[156:159]\n"
"ds_read_b128 a[216:219], v209 offset:21504\n"
"ds_read_b64_tr_b16 a[236:237], v215 offset:16896\n"
"v_mfma_f32_16x16x32_bf16 v[156:159], a[220:223], v[140:143], v[156:159]\n"
"ds_read_b128 a[220:223], v209 offset:22016\n"
"ds_read_b64_tr_b16 a[238:239], v215 offset:20992\n"
"v_mfma_f32_16x16x32_bf16 v[196:199], v[204:207], v[144:147], v[196:199]\n"
"v_exp_f32 v152, v152\n"
"s_add_u32 m0, s77, 0x0\n"
"v_mfma_f32_16x16x32_bf16 v[4:7], a[224:227], v[144:147], v[4:7]\n"
"buffer_load_dwordx4 v220, s[44:47], s79 offen nt lds\n"
"v_mfma_f32_16x16x32_bf16 v[16:19], a[228:231], v[144:147], v[16:19]\n"
"v_exp_f32 v153, v153\n"
"v_mfma_f32_16x16x32_bf16 v[28:31], a[232:235], v[144:147], v[28:31]\n"
"v_exp_f32 v154, v154\n"
"v_mfma_f32_16x16x32_bf16 v[40:43], v[172:175], v[144:147], v[40:43]\n"
"v_exp_f32 v155, v155\n"
"v_mfma_f32_16x16x32_bf16 v[52:55], v[176:179], v[144:147], v[52:55]\n"
"v_exp_f32 v156, v156\n"
"v_mfma_f32_16x16x32_bf16 v[64:67], v[180:183], v[144:147], v[64:67]\n"
"v_exp_f32 v157, v157\n"
"v_mfma_f32_16x16x32_bf16 v[76:79], v[184:187], v[144:147], v[76:79]\n"
"v_exp_f32 v158, v158\n"
"v_mfma_f32_16x16x32_bf16 v[88:91], v[188:191], v[144:147], v[88:91]\n"
"v_exp_f32 v159, v159\n"
"s_waitcnt lgkmcnt(0)\n"
"v_mfma_f32_16x16x32_bf16 v[144:147], a[192:195], v[96:99], v[160:163]\n"
"v_cvt_pk_bf16_f32 v152, v152, v153\n"
"v_cvt_pk_bf16_f32 v153, v154, v155\n"
"v_mfma_f32_16x16x32_bf16 v[144:147], a[196:199], v[100:103], v[144:147]\n"
"v_cvt_pk_bf16_f32 v154, v156, v157\n"
"v_cvt_pk_bf16_f32 v155, v158, v159\n"
"v_mfma_f32_16x16x32_bf16 v[144:147], a[200:203], v[104:107], v[144:147]\n"
"ds_read_b64_tr_b16 a[240:241], v212 offset:17408\n"
"ds_read_b64_tr_b16 a[242:243], v212 offset:21504\n"
"v_mfma_f32_16x16x32_bf16 v[144:147], a[204:207], v[108:111], v[144:147]\n"
"ds_read_b64_tr_b16 a[244:245], v215 offset:17408\n"
"ds_read_b64_tr_b16 a[246:247], v215 offset:21504\n"
"v_mfma_f32_16x16x32_bf16 v[148:151], a[208:211], v[96:99], v[160:163]\n"
"ds_read_b64_tr_b16 a[248:249], v212 offset:17920\n"
"ds_read_b64_tr_b16 a[250:251], v212 offset:22016\n"
"v_mfma_f32_16x16x32_bf16 v[148:151], a[212:215], v[100:103], v[148:151]\n"
"ds_read_b64_tr_b16 a[252:253], v215 offset:17920\n"
"ds_read_b64_tr_b16 a[254:255], v215 offset:22016\n"
"v_mfma_f32_16x16x32_bf16 v[148:151], a[216:219], v[104:107], v[148:151]\n"
"buffer_load_dwordx4 v220, s[44:47], s79 offen offset:2048 nt lds\n"
"v_mfma_f32_16x16x32_bf16 v[148:151], a[220:223], v[108:111], v[148:151]\n"
"v_mfma_f32_16x16x32_bf16 v[200:203], v[204:207], v[152:155], v[200:203]\n"
"v_exp_f32 v144, v144\n"
"v_mfma_f32_16x16x32_bf16 v[8:11], a[224:227], v[152:155], v[8:11]\n"
"ds_read_b64_tr_b16 a[224:225], v212 offset:16384\n"
"ds_read_b64_tr_b16 a[226:227], v212 offset:20480\n"
"v_mfma_f32_16x16x32_bf16 v[20:23], a[228:231], v[152:155], v[20:23]\n"
"ds_read_b64_tr_b16 a[228:229], v215 offset:16384\n"
"ds_read_b64_tr_b16 a[230:231], v215 offset:20480\n"
"v_mfma_f32_16x16x32_bf16 v[32:35], a[232:235], v[152:155], v[32:35]\n"
"ds_read_b64_tr_b16 a[232:233], v212 offset:16896\n"
"ds_read_b64_tr_b16 a[234:235], v212 offset:20992\n"
"v_mfma_f32_16x16x32_bf16 v[44:47], v[172:175], v[152:155], v[44:47]\n"
"v_exp_f32 v145, v145\n"
"v_mfma_f32_16x16x32_bf16 v[56:59], v[176:179], v[152:155], v[56:59]\n"
"v_exp_f32 v146, v146\n"
"v_mfma_f32_16x16x32_bf16 v[68:71], v[180:183], v[152:155], v[68:71]\n"
"v_exp_f32 v147, v147\n"
"v_mfma_f32_16x16x32_bf16 v[80:83], v[184:187], v[152:155], v[80:83]\n"
"v_exp_f32 v148, v148\n"
"v_mfma_f32_16x16x32_bf16 v[92:95], v[188:191], v[152:155], v[92:95]\n"
"v_exp_f32 v149, v149\n"
"v_mfma_f32_16x16x32_bf16 v[152:155], a[192:195], v[112:115], v[164:167]\n"
"v_exp_f32 v150, v150\n"
"v_mfma_f32_16x16x32_bf16 v[152:155], a[196:199], v[116:119], v[152:155]\n"
"v_exp_f32 v151, v151\n"
"v_mfma_f32_16x16x32_bf16 v[152:155], a[200:203], v[120:123], v[152:155]\n"
"v_cvt_pk_bf16_f32 v144, v144, v145\n"
"v_cvt_pk_bf16_f32 v145, v146, v147\n"
"s_add_u32 m0, s77, 0x2000\n"
"v_mfma_f32_16x16x32_bf16 v[152:155], a[204:207], v[124:127], v[152:155]\n"
"buffer_load_dwordx4 v220, s[48:51], s79 offen nt lds\n"
"v_mfma_f32_16x16x32_bf16 v[156:159], a[208:211], v[112:115], v[164:167]\n"
"v_cvt_pk_bf16_f32 v146, v148, v149\n"
"v_cvt_pk_bf16_f32 v147, v150, v151\n"
"v_mfma_f32_16x16x32_bf16 v[156:159], a[212:215], v[116:119], v[156:159]\n"
"v_mfma_f32_16x16x32_bf16 v[156:159], a[216:219], v[120:123], v[156:159]\n"
"v_mfma_f32_16x16x32_bf16 v[156:159], a[220:223], v[124:127], v[156:159]\n"
"v_mfma_f32_16x16x32_bf16 v[192:195], v[204:207], v[144:147], v[192:195]\n"
"v_exp_f32 v152, v152\n"
"s_waitcnt lgkmcnt(0)\n"
"v_mfma_f32_16x16x32_bf16 v[0:3], a[224:227], v[144:147], v[0:3]\n"
"v_exp_f32 v153, v153\n"
"v_mfma_f32_16x16x32_bf16 v[12:15], a[228:231], v[144:147], v[12:15]\n"
"v_exp_f32 v154, v154\n"
"v_mfma_f32_16x16x32_bf16 v[24:27], a[232:235], v[144:147], v[24:27]\n"
"v_exp_f32 v155, v155\n"
"v_mfma_f32_16x16x32_bf16 v[36:39], a[236:239], v[144:147], v[36:39]\n"
"v_exp_f32 v156, v156\n"
"v_mfma_f32_16x16x32_bf16 v[48:51], a[240:243], v[144:147], v[48:51]\n"
"v_exp_f32 v157, v157\n"
"v_mfma_f32_16x16x32_bf16 v[60:63], a[244:247], v[144:147], v[60:63]\n"
"v_exp_f32 v158, v158\n"
"v_mfma_f32_16x16x32_bf16 v[72:75], a[248:251], v[144:147], v[72:75]\n"
"v_exp_f32 v159, v159\n"
"v_mfma_f32_16x16x32_bf16 v[84:87], a[252:255], v[144:147], v[84:87]\n"
"s_waitcnt vmcnt(11)\n"
"s_barrier\n"
"v_mfma_f32_16x16x32_bf16 v[144:147], a[192:195], v[128:131], v[168:171]\n"
"buffer_load_dwordx4 v220, s[48:51], s79 offen offset:2048 nt lds\n"
"s_xor_b32 s80, s88, 0x1000\n"
"v_mfma_f32_16x16x32_bf16 v[144:147], a[196:199], v[132:135], v[144:147]\n"
"s_xor_b32 s79, s100, 0x1e000\n"
"v_cvt_pk_bf16_f32 v152, v152, v153\n"
"v_mfma_f32_16x16x32_bf16 v[144:147], a[200:203], v[136:139], v[144:147]\n"
"v_cvt_pk_bf16_f32 v153, v154, v155\n"
"v_cvt_pk_bf16_f32 v154, v156, v157\n"
"v_mfma_f32_16x16x32_bf16 v[144:147], a[204:207], v[140:143], v[144:147]\n"
"v_cvt_pk_bf16_f32 v155, v158, v159\n"
"ds_read_b128 a[192:195], v209 offset:32768\n"
"v_mfma_f32_16x16x32_bf16 v[148:151], a[208:211], v[128:131], v[168:171]\n"
"ds_read_b128 a[196:199], v209 offset:33280\n"
"ds_read_b128 a[200:203], v209 offset:33792\n"
"v_mfma_f32_16x16x32_bf16 v[148:151], a[212:215], v[132:135], v[148:151]\n"
"ds_read_b128 a[204:207], v209 offset:34304\n"
"ds_read_b128 a[208:211], v209 offset:36864\n"
"v_mfma_f32_16x16x32_bf16 v[148:151], a[216:219], v[136:139], v[148:151]\n"
"ds_read_b128 a[212:215], v209 offset:37376\n"
"ds_read_b128 a[216:219], v209 offset:37888\n"
"v_mfma_f32_16x16x32_bf16 v[148:151], a[220:223], v[140:143], v[148:151]\n"
"ds_read_b128 a[220:223], v209 offset:38400\n"
"ds_read_b64_tr_b16 v[172:173], v215 offset:33280\n"
"v_mfma_f32_16x16x32_bf16 v[196:199], v[204:207], v[152:155], v[196:199]\n"
"v_exp_f32 v144, v144\n"
"v_mfma_f32_16x16x32_bf16 v[4:7], a[224:227], v[152:155], v[4:7]\n"
"v_exp_f32 v145, v145\n"
"v_mfma_f32_16x16x32_bf16 v[16:19], a[228:231], v[152:155], v[16:19]\n"
"v_exp_f32 v146, v146\n"
"v_mfma_f32_16x16x32_bf16 v[28:31], a[232:235], v[152:155], v[28:31]\n"
"v_exp_f32 v147, v147\n"
"v_mfma_f32_16x16x32_bf16 v[40:43], a[236:239], v[152:155], v[40:43]\n"
"v_exp_f32 v148, v148\n"
"v_mfma_f32_16x16x32_bf16 v[52:55], a[240:243], v[152:155], v[52:55]\n"
"v_exp_f32 v149, v149\n"
"s_add_u32 m0, s77, 0x8000\n"
"v_mfma_f32_16x16x32_bf16 v[64:67], a[244:247], v[152:155], v[64:67]\n"
"buffer_load_dwordx4 v220, s[44:47], s79 offen nt lds\n"
"v_mfma_f32_16x16x32_bf16 v[76:79], a[248:251], v[152:155], v[76:79]\n"
"v_exp_f32 v150, v150\n"
"v_mfma_f32_16x16x32_bf16 v[88:91], a[252:255], v[152:155], v[88:91]\n"
"v_exp_f32 v151, v151\n"
"s_waitcnt lgkmcnt(0)\n"
"v_mfma_f32_16x16x32_bf16 v[152:155], a[192:195], v[96:99], v[160:163]\n"
"v_cvt_pk_bf16_f32 v144, v144, v145\n"
"v_cvt_pk_bf16_f32 v145, v146, v147\n"
"v_mfma_f32_16x16x32_bf16 v[152:155], a[196:199], v[100:103], v[152:155]\n"
"v_cvt_pk_bf16_f32 v146, v148, v149\n"
"v_cvt_pk_bf16_f32 v147, v150, v151\n"
"s_add_u32 m0, s78, 0x14000\n"
"v_mfma_f32_16x16x32_bf16 v[152:155], a[200:203], v[104:107], v[152:155]\n"
"buffer_load_dwordx4 v221, s[40:43], s80 offen lds\n"
"v_mfma_f32_16x16x32_bf16 v[152:155], a[204:207], v[108:111], v[152:155]\n"
"s_xor_b32 s80, s88, 0x21000\n"
"ds_read_b64_tr_b16 v[174:175], v215 offset:37376\n"
"v_mfma_f32_16x16x32_bf16 v[156:159], a[208:211], v[96:99], v[160:163]\n"
"ds_read_b64_tr_b16 v[176:177], v212 offset:33792\n"
"ds_read_b64_tr_b16 v[178:179], v212 offset:37888\n"
"v_mfma_f32_16x16x32_bf16 v[156:159], a[212:215], v[100:103], v[156:159]\n"
"ds_read_b64_tr_b16 v[180:181], v215 offset:33792\n"
"ds_read_b64_tr_b16 v[182:183], v215 offset:37888\n"
"s_add_u32 m0, s78, 0x15000\n"
"v_mfma_f32_16x16x32_bf16 v[156:159], a[216:219], v[104:107], v[156:159]\n"
"buffer_load_dwordx4 v221, s[40:43], s80 offen lds\n"
"v_mfma_f32_16x16x32_bf16 v[156:159], a[220:223], v[108:111], v[156:159]\n"
"s_xor_b32 s80, s88, 0x41000\n"
"ds_read_b64_tr_b16 v[184:185], v212 offset:34304\n"
"v_mfma_f32_16x16x32_bf16 v[200:203], v[204:207], v[144:147], v[200:203]\n"
"v_exp_f32 v152, v152\n"
"v_mfma_f32_16x16x32_bf16 v[8:11], a[224:227], v[144:147], v[8:11]\n"
"ds_read_b64_tr_b16 a[224:225], v212 offset:32768\n"
"ds_read_b64_tr_b16 a[226:227], v212 offset:36864\n"
"v_mfma_f32_16x16x32_bf16 v[20:23], a[228:231], v[144:147], v[20:23]\n"
"ds_read_b64_tr_b16 a[228:229], v215 offset:32768\n"
"ds_read_b64_tr_b16 a[230:231], v215 offset:36864\n"
"s_add_u32 m0, s77, 0x8000\n"
"v_mfma_f32_16x16x32_bf16 v[32:35], a[232:235], v[144:147], v[32:35]\n"
"buffer_load_dwordx4 v220, s[44:47], s79 offen offset:2048 nt lds\n"
"ds_read_b64_tr_b16 a[232:233], v212 offset:33280\n"
"v_mfma_f32_16x16x32_bf16 v[44:47], a[236:239], v[144:147], v[44:47]\n"
"ds_read_b64_tr_b16 a[234:235], v212 offset:37376\n"
"ds_read_b64_tr_b16 v[186:187], v212 offset:38400\n"
"s_add_u32 m0, s78, 0x16000\n"
"v_mfma_f32_16x16x32_bf16 v[56:59], a[240:243], v[144:147], v[56:59]\n"
"buffer_load_dwordx4 v221, s[40:43], s80 offen lds\n"
"v_mfma_f32_16x16x32_bf16 v[68:71], a[244:247], v[144:147], v[68:71]\n"
"s_xor_b32 s80, s88, 0x61000\n"
"ds_read_b64_tr_b16 v[188:189], v215 offset:34304\n"
"v_mfma_f32_16x16x32_bf16 v[80:83], a[248:251], v[144:147], v[80:83]\n"
"v_exp_f32 v153, v153\n"
"v_mfma_f32_16x16x32_bf16 v[92:95], a[252:255], v[144:147], v[92:95]\n"
"v_exp_f32 v154, v154\n"
"v_mfma_f32_16x16x32_bf16 v[144:147], a[192:195], v[112:115], v[164:167]\n"
"v_exp_f32 v155, v155\n"
"v_mfma_f32_16x16x32_bf16 v[144:147], a[196:199], v[116:119], v[144:147]\n"
"v_exp_f32 v156, v156\n"
"v_mfma_f32_16x16x32_bf16 v[144:147], a[200:203], v[120:123], v[144:147]\n"
"v_exp_f32 v157, v157\n"
"v_mfma_f32_16x16x32_bf16 v[144:147], a[204:207], v[124:127], v[144:147]\n"
"v_exp_f32 v158, v158\n"
"v_mfma_f32_16x16x32_bf16 v[148:151], a[208:211], v[112:115], v[164:167]\n"
"v_exp_f32 v159, v159\n"
"v_mfma_f32_16x16x32_bf16 v[148:151], a[212:215], v[116:119], v[148:151]\n"
"v_cvt_pk_bf16_f32 v152, v152, v153\n"
"v_cvt_pk_bf16_f32 v153, v154, v155\n"
"v_mfma_f32_16x16x32_bf16 v[148:151], a[216:219], v[120:123], v[148:151]\n"
"v_cvt_pk_bf16_f32 v154, v156, v157\n"
"v_cvt_pk_bf16_f32 v155, v158, v159\n"
"v_mfma_f32_16x16x32_bf16 v[148:151], a[220:223], v[124:127], v[148:151]\n"
"ds_read_b64_tr_b16 v[190:191], v215 offset:38400\n"
"s_add_u32 m0, s77, 0xa000\n"
"v_mfma_f32_16x16x32_bf16 v[192:195], v[204:207], v[152:155], v[192:195]\n"
"buffer_load_dwordx4 v220, s[48:51], s79 offen nt lds\n"
"s_add_u32 m0, s78, 0x17000\n"
"s_waitcnt lgkmcnt(1)\n"
"v_mfma_f32_16x16x32_bf16 v[0:3], a[224:227], v[152:155], v[0:3]\n"
"buffer_load_dwordx4 v221, s[40:43], s80 offen lds\n"
"v_mfma_f32_16x16x32_bf16 v[12:15], a[228:231], v[152:155], v[12:15]\n"
"v_exp_f32 v144, v144\n"
"v_mfma_f32_16x16x32_bf16 v[24:27], a[232:235], v[152:155], v[24:27]\n"
"v_exp_f32 v145, v145\n"
"v_mfma_f32_16x16x32_bf16 v[36:39], v[172:175], v[152:155], v[36:39]\n"
"v_exp_f32 v146, v146\n"
"v_mfma_f32_16x16x32_bf16 v[48:51], v[176:179], v[152:155], v[48:51]\n"
"v_exp_f32 v147, v147\n"
"v_mfma_f32_16x16x32_bf16 v[60:63], v[180:183], v[152:155], v[60:63]\n"
"v_exp_f32 v148, v148\n"
"v_mfma_f32_16x16x32_bf16 v[72:75], v[184:187], v[152:155], v[72:75]\n"
"v_exp_f32 v149, v149\n"
"s_waitcnt lgkmcnt(0)\n"
"v_mfma_f32_16x16x32_bf16 v[84:87], v[188:191], v[152:155], v[84:87]\n"
"s_waitcnt vmcnt(15)\n"
"s_barrier\n"
"v_mfma_f32_16x16x32_bf16 v[152:155], a[192:195], v[128:131], v[168:171]\n"
"s_xor_b32 s80, s88, 0x81000\n"
"v_cvt_pk_bf16_f32 v144, v144, v145\n"
"v_mfma_f32_16x16x32_bf16 v[152:155], a[196:199], v[132:135], v[152:155]\n"
"v_exp_f32 v150, v150\n"
"v_mfma_f32_16x16x32_bf16 v[152:155], a[200:203], v[136:139], v[152:155]\n"
"v_exp_f32 v151, v151\n"
"v_mfma_f32_16x16x32_bf16 v[152:155], a[204:207], v[140:143], v[152:155]\n"
"v_cvt_pk_bf16_f32 v145, v146, v147\n"
"v_cvt_pk_bf16_f32 v146, v148, v149\n"
"v_mfma_f32_16x16x32_bf16 v[156:159], a[208:211], v[128:131], v[168:171]\n"
"v_cvt_pk_bf16_f32 v147, v150, v151\n"
"ds_read_b128 a[192:195], v208 offset:16384\n"
"s_add_u32 m0, s77, 0xa000\n"
"v_mfma_f32_16x16x32_bf16 v[156:159], a[212:215], v[132:135], v[156:159]\n"
"buffer_load_dwordx4 v220, s[48:51], s79 offen offset:2048 nt lds\n"
"s_xor_b32 s79, s100, 0x200000\n"
"v_mfma_f32_16x16x32_bf16 v[156:159], a[216:219], v[136:139], v[156:159]\n"
"ds_read_b128 a[196:199], v208 offset:16896\n"
"ds_read_b128 a[200:203], v208 offset:17408\n"
"v_mfma_f32_16x16x32_bf16 v[156:159], a[220:223], v[140:143], v[156:159]\n"
"ds_read_b128 a[204:207], v208 offset:17920\n"
"ds_read_b128 a[208:211], v208 offset:20480\n"
"v_mfma_f32_16x16x32_bf16 v[196:199], v[204:207], v[144:147], v[196:199]\n"
"ds_read_b128 a[212:215], v208 offset:20992\n"
"ds_read_b128 a[216:219], v208 offset:21504\n"
"v_mfma_f32_16x16x32_bf16 v[4:7], a[224:227], v[144:147], v[4:7]\n"
"ds_read_b128 a[220:223], v208 offset:22016\n"
"ds_read_b64_tr_b16 a[236:237], v214 offset:16896\n"
"v_mfma_f32_16x16x32_bf16 v[16:19], a[228:231], v[144:147], v[16:19]\n"
"v_exp_f32 v152, v152\n"
"v_mfma_f32_16x16x32_bf16 v[28:31], a[232:235], v[144:147], v[28:31]\n"
"v_exp_f32 v153, v153\n"
"v_mfma_f32_16x16x32_bf16 v[40:43], v[172:175], v[144:147], v[40:43]\n"
"v_exp_f32 v154, v154\n"
"v_mfma_f32_16x16x32_bf16 v[52:55], v[176:179], v[144:147], v[52:55]\n"
"v_exp_f32 v155, v155\n"
"v_mfma_f32_16x16x32_bf16 v[64:67], v[180:183], v[144:147], v[64:67]\n"
"v_exp_f32 v156, v156\n"
"v_mfma_f32_16x16x32_bf16 v[76:79], v[184:187], v[144:147], v[76:79]\n"
"v_exp_f32 v157, v157\n"
"v_mfma_f32_16x16x32_bf16 v[88:91], v[188:191], v[144:147], v[88:91]\n"
"v_exp_f32 v158, v158\n"
"s_waitcnt lgkmcnt(0)\n"
"v_mfma_f32_16x16x32_bf16 v[144:147], a[192:195], v[96:99], v[160:163]\n"
"v_exp_f32 v159, v159\n"
"v_mfma_f32_16x16x32_bf16 v[144:147], a[196:199], v[100:103], v[144:147]\n"
"v_cvt_pk_bf16_f32 v152, v152, v153\n"
"v_cvt_pk_bf16_f32 v153, v154, v155\n"
"v_mfma_f32_16x16x32_bf16 v[144:147], a[200:203], v[104:107], v[144:147]\n"
"v_cvt_pk_bf16_f32 v154, v156, v157\n"
"v_cvt_pk_bf16_f32 v155, v158, v159\n"
"v_mfma_f32_16x16x32_bf16 v[144:147], a[204:207], v[108:111], v[144:147]\n"
"ds_read_b64_tr_b16 a[238:239], v214 offset:20992\n"
"ds_read_b64_tr_b16 a[240:241], v211 offset:17408\n"
"v_mfma_f32_16x16x32_bf16 v[148:151], a[208:211], v[96:99], v[160:163]\n"
"ds_read_b64_tr_b16 a[242:243], v211 offset:21504\n"
"ds_read_b64_tr_b16 a[244:245], v214 offset:17408\n"
"v_mfma_f32_16x16x32_bf16 v[148:151], a[212:215], v[100:103], v[148:151]\n"
"ds_read_b64_tr_b16 a[246:247], v214 offset:21504\n"
"ds_read_b64_tr_b16 a[248:249], v211 offset:17920\n"
"v_mfma_f32_16x16x32_bf16 v[148:151], a[216:219], v[104:107], v[148:151]\n"
"ds_read_b64_tr_b16 a[250:251], v211 offset:22016\n"
"ds_read_b64_tr_b16 a[252:253], v214 offset:17920\n"
"v_mfma_f32_16x16x32_bf16 v[148:151], a[220:223], v[108:111], v[148:151]\n"
"ds_read_b64_tr_b16 a[254:255], v214 offset:22016\n"
"v_mfma_f32_16x16x32_bf16 v[200:203], v[204:207], v[152:155], v[200:203]\n"
"v_exp_f32 v144, v144\n"
"v_mfma_f32_16x16x32_bf16 v[8:11], a[224:227], v[152:155], v[8:11]\n"
"ds_read_b64_tr_b16 a[224:225], v211 offset:16384\n"
"ds_read_b64_tr_b16 a[226:227], v211 offset:20480\n"
"v_mfma_f32_16x16x32_bf16 v[20:23], a[228:231], v[152:155], v[20:23]\n"
"ds_read_b64_tr_b16 a[228:229], v214 offset:16384\n"
"ds_read_b64_tr_b16 a[230:231], v214 offset:20480\n"
"v_mfma_f32_16x16x32_bf16 v[32:35], a[232:235], v[152:155], v[32:35]\n"
"ds_read_b64_tr_b16 a[232:233], v211 offset:16896\n"
"ds_read_b64_tr_b16 a[234:235], v211 offset:20992\n"
"v_mfma_f32_16x16x32_bf16 v[44:47], v[172:175], v[152:155], v[44:47]\n"
"v_exp_f32 v145, v145\n"
"v_mfma_f32_16x16x32_bf16 v[56:59], v[176:179], v[152:155], v[56:59]\n"
"v_exp_f32 v146, v146\n"
"v_mfma_f32_16x16x32_bf16 v[68:71], v[180:183], v[152:155], v[68:71]\n"
"v_exp_f32 v147, v147\n"
"v_mfma_f32_16x16x32_bf16 v[80:83], v[184:187], v[152:155], v[80:83]\n"
"v_exp_f32 v148, v148\n"
"v_mfma_f32_16x16x32_bf16 v[92:95], v[188:191], v[152:155], v[92:95]\n"
"v_exp_f32 v149, v149\n"
"v_mfma_f32_16x16x32_bf16 v[152:155], a[192:195], v[112:115], v[164:167]\n"
"v_exp_f32 v150, v150\n"
"v_mfma_f32_16x16x32_bf16 v[152:155], a[196:199], v[116:119], v[152:155]\n"
"v_exp_f32 v151, v151\n"
"v_mfma_f32_16x16x32_bf16 v[152:155], a[200:203], v[120:123], v[152:155]\n"
"v_cvt_pk_bf16_f32 v144, v144, v145\n"
"v_cvt_pk_bf16_f32 v145, v146, v147\n"
"v_mfma_f32_16x16x32_bf16 v[152:155], a[204:207], v[124:127], v[152:155]\n"
"v_cvt_pk_bf16_f32 v146, v148, v149\n"
"v_cvt_pk_bf16_f32 v147, v150, v151\n"
"s_add_u32 m0, s77, 0xc000\n"
"v_mfma_f32_16x16x32_bf16 v[156:159], a[208:211], v[112:115], v[164:167]\n"
"buffer_load_dwordx4 v220, s[44:47], s79 offen nt lds\n"
"s_add_u32 m0, s78, 0x18000\n"
"v_mfma_f32_16x16x32_bf16 v[156:159], a[212:215], v[116:119], v[156:159]\n"
"buffer_load_dwordx4 v221, s[40:43], s80 offen lds\n"
"v_mfma_f32_16x16x32_bf16 v[156:159], a[216:219], v[120:123], v[156:159]\n"
"s_xor_b32 s80, s88, 0xa1000\n"
"v_mfma_f32_16x16x32_bf16 v[156:159], a[220:223], v[124:127], v[156:159]\n"
"v_mfma_f32_16x16x32_bf16 v[192:195], v[204:207], v[144:147], v[192:195]\n"
"v_exp_f32 v152, v152\n"
"s_add_u32 m0, s78, 0x19000\n"
"s_waitcnt lgkmcnt(0)\n"
"v_mfma_f32_16x16x32_bf16 v[0:3], a[224:227], v[144:147], v[0:3]\n"
"buffer_load_dwordx4 v221, s[40:43], s80 offen lds\n"
"v_mfma_f32_16x16x32_bf16 v[12:15], a[228:231], v[144:147], v[12:15]\n"
"s_xor_b32 s80, s88, 0xc1000\n"
"v_mfma_f32_16x16x32_bf16 v[24:27], a[232:235], v[144:147], v[24:27]\n"
"v_exp_f32 v153, v153\n"
"v_mfma_f32_16x16x32_bf16 v[36:39], a[236:239], v[144:147], v[36:39]\n"
"v_exp_f32 v154, v154\n"
"v_mfma_f32_16x16x32_bf16 v[48:51], a[240:243], v[144:147], v[48:51]\n"
"v_exp_f32 v155, v155\n"
"v_mfma_f32_16x16x32_bf16 v[60:63], a[244:247], v[144:147], v[60:63]\n"
"v_exp_f32 v156, v156\n"
"v_mfma_f32_16x16x32_bf16 v[72:75], a[248:251], v[144:147], v[72:75]\n"
"v_exp_f32 v157, v157\n"
"v_mfma_f32_16x16x32_bf16 v[84:87], a[252:255], v[144:147], v[84:87]\n"
"s_waitcnt vmcnt(15)\n"
"s_barrier\n"
"v_mfma_f32_16x16x32_bf16 v[144:147], a[192:195], v[128:131], v[168:171]\n"
"v_exp_f32 v158, v158\n"
"v_mfma_f32_16x16x32_bf16 v[144:147], a[196:199], v[132:135], v[144:147]\n"
"v_exp_f32 v159, v159\n"
"v_mfma_f32_16x16x32_bf16 v[144:147], a[200:203], v[136:139], v[144:147]\n"
"v_cvt_pk_bf16_f32 v152, v152, v153\n"
"v_cvt_pk_bf16_f32 v153, v154, v155\n"
"v_mfma_f32_16x16x32_bf16 v[144:147], a[204:207], v[140:143], v[144:147]\n"
"v_cvt_pk_bf16_f32 v154, v156, v157\n"
"v_cvt_pk_bf16_f32 v155, v158, v159\n"
"s_add_u32 m0, s78, 0x1a000\n"
"v_mfma_f32_16x16x32_bf16 v[148:151], a[208:211], v[128:131], v[168:171]\n"
"buffer_load_dwordx4 v221, s[40:43], s80 offen lds\n"
"v_mfma_f32_16x16x32_bf16 v[148:151], a[212:215], v[132:135], v[148:151]\n"
"s_xor_b32 s80, s88, 0xe1000\n"
"ds_read_b128 a[192:195], v209 offset:0\n"
"v_mfma_f32_16x16x32_bf16 v[148:151], a[216:219], v[136:139], v[148:151]\n"
"ds_read_b128 a[196:199], v209 offset:512\n"
"ds_read_b128 a[200:203], v209 offset:1024\n"
"v_mfma_f32_16x16x32_bf16 v[148:151], a[220:223], v[140:143], v[148:151]\n"
"ds_read_b128 a[204:207], v209 offset:1536\n"
"ds_read_b128 a[208:211], v209 offset:4096\n"
"v_mfma_f32_16x16x32_bf16 v[196:199], v[204:207], v[152:155], v[196:199]\n"
"ds_read_b128 a[212:215], v209 offset:4608\n"
"ds_read_b128 a[216:219], v209 offset:5120\n"
"s_add_u32 m0, s77, 0xc000\n"
"v_mfma_f32_16x16x32_bf16 v[4:7], a[224:227], v[152:155], v[4:7]\n"
"buffer_load_dwordx4 v220, s[44:47], s79 offen offset:2048 nt lds\n"
"ds_read_b128 a[220:223], v209 offset:5632\n"
"s_add_u32 m0, s78, 0x1b000\n"
"v_mfma_f32_16x16x32_bf16 v[16:19], a[228:231], v[152:155], v[16:19]\n"
"buffer_load_dwordx4 v221, s[40:43], s80 offen lds\n"
"v_mfma_f32_16x16x32_bf16 v[28:31], a[232:235], v[152:155], v[28:31]\n"
"s_xor_b32 s80, s88, 0x101000\n"
"ds_read_b64_tr_b16 v[172:173], v215 offset:512\n"
"v_mfma_f32_16x16x32_bf16 v[40:43], a[236:239], v[152:155], v[40:43]\n"
"v_exp_f32 v144, v144\n"
"v_mfma_f32_16x16x32_bf16 v[52:55], a[240:243], v[152:155], v[52:55]\n"
"v_exp_f32 v145, v145\n"
"v_mfma_f32_16x16x32_bf16 v[64:67], a[244:247], v[152:155], v[64:67]\n"
"v_exp_f32 v146, v146\n"
"v_mfma_f32_16x16x32_bf16 v[76:79], a[248:251], v[152:155], v[76:79]\n"
"v_exp_f32 v147, v147\n"
"v_mfma_f32_16x16x32_bf16 v[88:91], a[252:255], v[152:155], v[88:91]\n"
"v_exp_f32 v148, v148\n"
"s_waitcnt lgkmcnt(0)\n"
"v_mfma_f32_16x16x32_bf16 v[152:155], a[192:195], v[96:99], v[160:163]\n"
"v_exp_f32 v149, v149\n"
"v_mfma_f32_16x16x32_bf16 v[152:155], a[196:199], v[100:103], v[152:155]\n"
"v_exp_f32 v150, v150\n"
"v_mfma_f32_16x16x32_bf16 v[152:155], a[200:203], v[104:107], v[152:155]\n"
"v_exp_f32 v151, v151\n"
"v_mfma_f32_16x16x32_bf16 v[152:155], a[204:207], v[108:111], v[152:155]\n"
"v_cvt_pk_bf16_f32 v144, v144, v145\n"
"v_cvt_pk_bf16_f32 v145, v146, v147\n"
"v_mfma_f32_16x16x32_bf16 v[156:159], a[208:211], v[96:99], v[160:163]\n"
"v_cvt_pk_bf16_f32 v146, v148, v149\n"
"v_cvt_pk_bf16_f32 v147, v150, v151\n"
"v_mfma_f32_16x16x32_bf16 v[156:159], a[212:215], v[100:103], v[156:159]\n"
"ds_read_b64_tr_b16 v[174:175], v215 offset:4608\n"
"ds_read_b64_tr_b16 v[176:177], v212 offset:1024\n"
"v_mfma_f32_16x16x32_bf16 v[156:159], a[216:219], v[104:107], v[156:159]\n"
"ds_read_b64_tr_b16 v[178:179], v212 offset:5120\n"
"ds_read_b64_tr_b16 v[180:181], v215 offset:1024\n"
"v_mfma_f32_16x16x32_bf16 v[156:159], a[220:223], v[108:111], v[156:159]\n"
"ds_read_b64_tr_b16 v[182:183], v215 offset:5120\n"
"ds_read_b64_tr_b16 v[184:185], v212 offset:1536\n"
"s_add_u32 m0, s77, 0xe000\n"
"v_mfma_f32_16x16x32_bf16 v[200:203], v[204:207], v[144:147], v[200:203]\n"
"buffer_load_dwordx4 v220, s[48:51], s79 offen nt lds\n"
"v_mfma_f32_16x16x32_bf16 v[8:11], a[224:227], v[144:147], v[8:11]\n"
"ds_read_b64_tr_b16 a[224:225], v212 offset:0\n"
"ds_read_b64_tr_b16 a[226:227], v212 offset:4096\n"
"v_mfma_f32_16x16x32_bf16 v[20:23], a[228:231], v[144:147], v[20:23]\n"
"ds_read_b64_tr_b16 a[228:229], v215 offset:0\n"
"ds_read_b64_tr_b16 a[230:231], v215 offset:4096\n"
"v_mfma_f32_16x16x32_bf16 v[32:35], a[232:235], v[144:147], v[32:35]\n"
"ds_read_b64_tr_b16 a[232:233], v212 offset:512\n"
"ds_read_b64_tr_b16 a[234:235], v212 offset:4608\n"
"v_mfma_f32_16x16x32_bf16 v[44:47], a[236:239], v[144:147], v[44:47]\n"
"v_exp_f32 v152, v152\n"
"v_mfma_f32_16x16x32_bf16 v[56:59], a[240:243], v[144:147], v[56:59]\n"
"v_exp_f32 v153, v153\n"
"v_mfma_f32_16x16x32_bf16 v[68:71], a[244:247], v[144:147], v[68:71]\n"
"v_exp_f32 v154, v154\n"
"v_mfma_f32_16x16x32_bf16 v[80:83], a[248:251], v[144:147], v[80:83]\n"
"v_exp_f32 v155, v155\n"
"v_mfma_f32_16x16x32_bf16 v[92:95], a[252:255], v[144:147], v[92:95]\n"
"v_exp_f32 v156, v156\n"
"v_mfma_f32_16x16x32_bf16 v[144:147], a[192:195], v[112:115], v[164:167]\n"
"v_exp_f32 v157, v157\n"
"v_mfma_f32_16x16x32_bf16 v[144:147], a[196:199], v[116:119], v[144:147]\n"
"v_exp_f32 v158, v158\n"
"v_mfma_f32_16x16x32_bf16 v[144:147], a[200:203], v[120:123], v[144:147]\n"
"v_exp_f32 v159, v159\n"
"v_mfma_f32_16x16x32_bf16 v[144:147], a[204:207], v[124:127], v[144:147]\n"
"ds_read_b64_tr_b16 v[186:187], v212 offset:5632\n"
"v_cvt_pk_bf16_f32 v152, v152, v153\n"
"v_mfma_f32_16x16x32_bf16 v[148:151], a[208:211], v[112:115], v[164:167]\n"
"v_cvt_pk_bf16_f32 v153, v154, v155\n"
"v_cvt_pk_bf16_f32 v154, v156, v157\n"
"v_mfma_f32_16x16x32_bf16 v[148:151], a[212:215], v[116:119], v[148:151]\n"
"v_cvt_pk_bf16_f32 v155, v158, v159\n"
"ds_read_b64_tr_b16 v[188:189], v215 offset:1536\n"
"v_mfma_f32_16x16x32_bf16 v[148:151], a[216:219], v[120:123], v[148:151]\n"
"ds_read_b64_tr_b16 v[190:191], v215 offset:5632\n"
"s_add_u32 m0, s78, 0x4000\n"
"v_mfma_f32_16x16x32_bf16 v[148:151], a[220:223], v[124:127], v[148:151]\n"
"buffer_load_dwordx4 v221, s[40:43], s80 offen lds\n"
"v_mfma_f32_16x16x32_bf16 v[192:195], v[204:207], v[152:155], v[192:195]\n"
"s_add_u32 m0, s77, 0xe000\n"
"s_xor_b32 s80, s88, 0x121000\n"
"buffer_load_dwordx4 v220, s[48:51], s79 offen offset:2048 nt lds\n"
"s_waitcnt lgkmcnt(2)\n"
"v_mfma_f32_16x16x32_bf16 v[0:3], a[224:227], v[152:155], v[0:3]\n"
"s_xor_b32 s79, s100, 0x202000\n"
"v_mfma_f32_16x16x32_bf16 v[12:15], a[228:231], v[152:155], v[12:15]\n"
"v_exp_f32 v144, v144\n"
"v_mfma_f32_16x16x32_bf16 v[24:27], a[232:235], v[152:155], v[24:27]\n"
"v_exp_f32 v145, v145\n"
"v_mfma_f32_16x16x32_bf16 v[36:39], v[172:175], v[152:155], v[36:39]\n"
"v_exp_f32 v146, v146\n"
"v_mfma_f32_16x16x32_bf16 v[48:51], v[176:179], v[152:155], v[48:51]\n"
"v_exp_f32 v147, v147\n"
"v_mfma_f32_16x16x32_bf16 v[60:63], v[180:183], v[152:155], v[60:63]\n"
"v_exp_f32 v148, v148\n"
"v_mfma_f32_16x16x32_bf16 v[72:75], v[184:187], v[152:155], v[72:75]\n"
"v_exp_f32 v149, v149\n"
"s_waitcnt lgkmcnt(0)\n"
"v_mfma_f32_16x16x32_bf16 v[84:87], v[188:191], v[152:155], v[84:87]\n"
"s_waitcnt vmcnt(17)\n"
"s_barrier\n"
"v_mfma_f32_16x16x32_bf16 v[152:155], a[192:195], v[128:131], v[168:171]\n"
"v_exp_f32 v150, v150\n"
"v_mfma_f32_16x16x32_bf16 v[152:155], a[196:199], v[132:135], v[152:155]\n"
"v_exp_f32 v151, v151\n"
"s_add_u32 m0, s78, 0x5000\n"
"v_mfma_f32_16x16x32_bf16 v[152:155], a[200:203], v[136:139], v[152:155]\n"
"buffer_load_dwordx4 v221, s[40:43], s80 offen lds\n"
"v_mfma_f32_16x16x32_bf16 v[152:155], a[204:207], v[140:143], v[152:155]\n"
"s_xor_b32 s80, s88, 0x141000\n"
"v_cvt_pk_bf16_f32 v144, v144, v145\n"
"v_mfma_f32_16x16x32_bf16 v[156:159], a[208:211], v[128:131], v[168:171]\n"
"v_cvt_pk_bf16_f32 v145, v146, v147\n"
"v_cvt_pk_bf16_f32 v146, v148, v149\n"
"v_mfma_f32_16x16x32_bf16 v[156:159], a[212:215], v[132:135], v[156:159]\n"
"v_cvt_pk_bf16_f32 v147, v150, v151\n"
"ds_read_b128 a[192:195], v208 offset:0\n"
"v_mfma_f32_16x16x32_bf16 v[156:159], a[216:219], v[136:139], v[156:159]\n"
"ds_read_b128 a[196:199], v208 offset:512\n"
"ds_read_b128 a[200:203], v208 offset:1024\n"
"v_mfma_f32_16x16x32_bf16 v[156:159], a[220:223], v[140:143], v[156:159]\n"
"ds_read_b128 a[204:207], v208 offset:1536\n"
"ds_read_b128 a[208:211], v208 offset:4096\n"
"v_mfma_f32_16x16x32_bf16 v[196:199], v[204:207], v[144:147], v[196:199]\n"
"ds_read_b128 a[212:215], v208 offset:4608\n"
"ds_read_b128 a[216:219], v208 offset:5120\n"
"v_mfma_f32_16x16x32_bf16 v[4:7], a[224:227], v[144:147], v[4:7]\n"
"ds_read_b128 a[220:223], v208 offset:5632\n"
"ds_read_b64_tr_b16 a[236:237], v214 offset:512\n"
"s_add_u32 m0, s78, 0x6000\n"
"v_mfma_f32_16x16x32_bf16 v[16:19], a[228:231], v[144:147], v[16:19]\n"
"buffer_load_dwordx4 v221, s[40:43], s80 offen lds\n"
"v_mfma_f32_16x16x32_bf16 v[28:31], a[232:235], v[144:147], v[28:31]\n"
"s_xor_b32 s80, s88, 0x161000\n"
"ds_read_b64_tr_b16 a[238:239], v214 offset:4608\n"
"s_add_u32 m0, s77, 0x1c000\n"
"v_mfma_f32_16x16x32_bf16 v[40:43], v[172:175], v[144:147], v[40:43]\n"
"buffer_load_dwordx4 v220, s[44:47], s79 offen nt lds\n"
"v_mfma_f32_16x16x32_bf16 v[52:55], v[176:179], v[144:147], v[52:55]\n"
"v_exp_f32 v152, v152\n"
"v_mfma_f32_16x16x32_bf16 v[64:67], v[180:183], v[144:147], v[64:67]\n"
"v_exp_f32 v153, v153\n"
"v_mfma_f32_16x16x32_bf16 v[76:79], v[184:187], v[144:147], v[76:79]\n"
"v_exp_f32 v154, v154\n"
"v_mfma_f32_16x16x32_bf16 v[88:91], v[188:191], v[144:147], v[88:91]\n"
"v_exp_f32 v155, v155\n"
"s_waitcnt lgkmcnt(0)\n"
"v_mfma_f32_16x16x32_bf16 v[144:147], a[192:195], v[96:99], v[160:163]\n"
"v_exp_f32 v156, v156\n"
"v_mfma_f32_16x16x32_bf16 v[144:147], a[196:199], v[100:103], v[144:147]\n"
"v_exp_f32 v157, v157\n"
"v_mfma_f32_16x16x32_bf16 v[144:147], a[200:203], v[104:107], v[144:147]\n"
"v_exp_f32 v158, v158\n"
"v_mfma_f32_16x16x32_bf16 v[144:147], a[204:207], v[108:111], v[144:147]\n"
"v_exp_f32 v159, v159\n"
"v_mfma_f32_16x16x32_bf16 v[148:151], a[208:211], v[96:99], v[160:163]\n"
"v_cvt_pk_bf16_f32 v152, v152, v153\n"
"v_cvt_pk_bf16_f32 v153, v154, v155\n"
"v_mfma_f32_16x16x32_bf16 v[148:151], a[212:215], v[100:103], v[148:151]\n"
"v_cvt_pk_bf16_f32 v154, v156, v157\n"
"v_cvt_pk_bf16_f32 v155, v158, v159\n"
"s_add_u32 m0, s78, 0x7000\n"
"v_mfma_f32_16x16x32_bf16 v[148:151], a[216:219], v[104:107], v[148:151]\n"
"buffer_load_dwordx4 v221, s[40:43], s80 offen lds\n"
"v_mfma_f32_16x16x32_bf16 v[148:151], a[220:223], v[108:111], v[148:151]\n"
"s_xor_b32 s80, s88, 0x181000\n"
"ds_read_b64_tr_b16 a[240:241], v211 offset:1024\n"
"v_mfma_f32_16x16x32_bf16 v[200:203], v[204:207], v[152:155], v[200:203]\n"
"ds_read_b64_tr_b16 a[242:243], v211 offset:5120\n"
"ds_read_b64_tr_b16 a[244:245], v214 offset:1024\n"
"v_mfma_f32_16x16x32_bf16 v[8:11], a[224:227], v[152:155], v[8:11]\n"
"ds_read_b64_tr_b16 a[224:225], v211 offset:0\n"
"ds_read_b64_tr_b16 a[226:227], v211 offset:4096\n"
"v_mfma_f32_16x16x32_bf16 v[20:23], a[228:231], v[152:155], v[20:23]\n"
"ds_read_b64_tr_b16 a[228:229], v214 offset:0\n"
"ds_read_b64_tr_b16 a[230:231], v214 offset:4096\n"
"v_mfma_f32_16x16x32_bf16 v[32:35], a[232:235], v[152:155], v[32:35]\n"
"ds_read_b64_tr_b16 a[232:233], v211 offset:512\n"
"ds_read_b64_tr_b16 a[234:235], v211 offset:4608\n"
"v_mfma_f32_16x16x32_bf16 v[44:47], v[172:175], v[152:155], v[44:47]\n"
"s_add_u32 m0, s77, 0x1c000\n"
"ds_read_b64_tr_b16 a[246:247], v214 offset:5120\n"
"buffer_load_dwordx4 v220, s[44:47], s79 offen offset:2048 nt lds\n"
"v_mfma_f32_16x16x32_bf16 v[56:59], v[176:179], v[152:155], v[56:59]\n"
"v_exp_f32 v144, v144\n"
"v_mfma_f32_16x16x32_bf16 v[68:71], v[180:183], v[152:155], v[68:71]\n"
"v_exp_f32 v145, v145\n"
"v_mfma_f32_16x16x32_bf16 v[80:83], v[184:187], v[152:155], v[80:83]\n"
"v_exp_f32 v146, v146\n"
"v_mfma_f32_16x16x32_bf16 v[92:95], v[188:191], v[152:155], v[92:95]\n"
"v_exp_f32 v147, v147\n"
"v_mfma_f32_16x16x32_bf16 v[152:155], a[192:195], v[112:115], v[164:167]\n"
"v_exp_f32 v148, v148\n"
"v_mfma_f32_16x16x32_bf16 v[152:155], a[196:199], v[116:119], v[152:155]\n"
"v_exp_f32 v149, v149\n"
"v_mfma_f32_16x16x32_bf16 v[152:155], a[200:203], v[120:123], v[152:155]\n"
"v_exp_f32 v150, v150\n"
"v_mfma_f32_16x16x32_bf16 v[152:155], a[204:207], v[124:127], v[152:155]\n"
"v_exp_f32 v151, v151\n"
"v_mfma_f32_16x16x32_bf16 v[156:159], a[208:211], v[112:115], v[164:167]\n"
"ds_read_b64_tr_b16 a[248:249], v211 offset:1536\n"
"ds_read_b64_tr_b16 a[250:251], v211 offset:5632\n"
"v_mfma_f32_16x16x32_bf16 v[156:159], a[212:215], v[116:119], v[156:159]\n"
"v_cvt_pk_bf16_f32 v144, v144, v145\n"
"v_cvt_pk_bf16_f32 v145, v146, v147\n"
"v_mfma_f32_16x16x32_bf16 v[156:159], a[216:219], v[120:123], v[156:159]\n"
"v_cvt_pk_bf16_f32 v146, v148, v149\n"
"v_cvt_pk_bf16_f32 v147, v150, v151\n"
"v_mfma_f32_16x16x32_bf16 v[156:159], a[220:223], v[124:127], v[156:159]\n"
"ds_read_b64_tr_b16 a[252:253], v214 offset:1536\n"
"ds_read_b64_tr_b16 a[254:255], v214 offset:5632\n"
"s_add_u32 m0, s78, 0x10000\n"
"v_mfma_f32_16x16x32_bf16 v[192:195], v[204:207], v[144:147], v[192:195]\n"
"buffer_load_dwordx4 v221, s[40:43], s80 offen lds\n"
"s_waitcnt lgkmcnt(2)\n"
"v_mfma_f32_16x16x32_bf16 v[0:3], a[224:227], v[144:147], v[0:3]\n"
"s_xor_b32 s80, s88, 0x1a1000\n"
"v_mfma_f32_16x16x32_bf16 v[12:15], a[228:231], v[144:147], v[12:15]\n"
"v_exp_f32 v152, v152\n"
"v_mfma_f32_16x16x32_bf16 v[24:27], a[232:235], v[144:147], v[24:27]\n"
"v_exp_f32 v153, v153\n"
"v_mfma_f32_16x16x32_bf16 v[36:39], a[236:239], v[144:147], v[36:39]\n"
"v_exp_f32 v154, v154\n"
"v_mfma_f32_16x16x32_bf16 v[48:51], a[240:243], v[144:147], v[48:51]\n"
"v_exp_f32 v155, v155\n"
"v_mfma_f32_16x16x32_bf16 v[60:63], a[244:247], v[144:147], v[60:63]\n"
"v_exp_f32 v156, v156\n"
"v_mfma_f32_16x16x32_bf16 v[72:75], a[248:251], v[144:147], v[72:75]\n"
"v_exp_f32 v157, v157\n"
"s_waitcnt lgkmcnt(0)\n"
"v_mfma_f32_16x16x32_bf16 v[84:87], a[252:255], v[144:147], v[84:87]\n"
"s_waitcnt vmcnt(15)\n"
"s_barrier\n"
"v_mfma_f32_16x16x32_bf16 v[144:147], a[192:195], v[128:131], v[168:171]\n"
"v_exp_f32 v158, v158\n"
"v_mfma_f32_16x16x32_bf16 v[144:147], a[196:199], v[132:135], v[144:147]\n"
"v_exp_f32 v159, v159\n"
"s_add_u32 m0, s77, 0x1e000\n"
"v_mfma_f32_16x16x32_bf16 v[144:147], a[200:203], v[136:139], v[144:147]\n"
"buffer_load_dwordx4 v220, s[48:51], s79 offen nt lds\n"
"v_mfma_f32_16x16x32_bf16 v[144:147], a[204:207], v[140:143], v[144:147]\n"
"v_cvt_pk_bf16_f32 v152, v152, v153\n"
"v_cvt_pk_bf16_f32 v153, v154, v155\n"
"v_mfma_f32_16x16x32_bf16 v[148:151], a[208:211], v[128:131], v[168:171]\n"
"v_cvt_pk_bf16_f32 v154, v156, v157\n"
"v_cvt_pk_bf16_f32 v155, v158, v159\n"
"s_add_u32 m0, s78, 0x11000\n"
"v_mfma_f32_16x16x32_bf16 v[148:151], a[212:215], v[132:135], v[148:151]\n"
"buffer_load_dwordx4 v221, s[40:43], s80 offen lds\n"
"v_mfma_f32_16x16x32_bf16 v[148:151], a[216:219], v[136:139], v[148:151]\n"
"ds_read_b128 a[192:195], v208 offset:32768\n"
"s_xor_b32 s80, s88, 0x1c1000\n"
"v_mfma_f32_16x16x32_bf16 v[148:151], a[220:223], v[140:143], v[148:151]\n"
"ds_read_b128 a[196:199], v208 offset:33280\n"
"ds_read_b128 a[200:203], v208 offset:33792\n"
"v_mfma_f32_16x16x32_bf16 v[196:199], v[204:207], v[152:155], v[196:199]\n"
"ds_read_b128 a[204:207], v208 offset:34304\n"
"ds_read_b128 a[208:211], v208 offset:36864\n"
"v_mfma_f32_16x16x32_bf16 v[4:7], a[224:227], v[152:155], v[4:7]\n"
"ds_read_b128 a[212:215], v208 offset:37376\n"
"ds_read_b128 a[216:219], v208 offset:37888\n"
"v_mfma_f32_16x16x32_bf16 v[16:19], a[228:231], v[152:155], v[16:19]\n"
"ds_read_b128 a[220:223], v208 offset:38400\n"
"ds_read_b64_tr_b16 v[172:173], v214 offset:33280\n"
"s_add_u32 m0, s78, 0x12000\n"
"v_mfma_f32_16x16x32_bf16 v[28:31], a[232:235], v[152:155], v[28:31]\n"
"buffer_load_dwordx4 v221, s[40:43], s80 offen lds\n"
"v_mfma_f32_16x16x32_bf16 v[40:43], a[236:239], v[152:155], v[40:43]\n"
"s_xor_b32 s80, s88, 0x1e1000\n"
"ds_read_b64_tr_b16 v[174:175], v214 offset:37376\n"
"v_mfma_f32_16x16x32_bf16 v[52:55], a[240:243], v[152:155], v[52:55]\n"
"v_exp_f32 v144, v144\n"
"v_mfma_f32_16x16x32_bf16 v[64:67], a[244:247], v[152:155], v[64:67]\n"
"v_exp_f32 v145, v145\n"
"v_mfma_f32_16x16x32_bf16 v[76:79], a[248:251], v[152:155], v[76:79]\n"
"v_exp_f32 v146, v146\n"
"v_mfma_f32_16x16x32_bf16 v[88:91], a[252:255], v[152:155], v[88:91]\n"
"v_exp_f32 v147, v147\n"
"s_waitcnt lgkmcnt(0)\n"
"v_mfma_f32_16x16x32_bf16 v[152:155], a[192:195], v[96:99], v[160:163]\n"
"v_exp_f32 v148, v148\n"
"v_mfma_f32_16x16x32_bf16 v[152:155], a[196:199], v[100:103], v[152:155]\n"
"v_exp_f32 v149, v149\n"
"v_mfma_f32_16x16x32_bf16 v[152:155], a[200:203], v[104:107], v[152:155]\n"
"v_exp_f32 v150, v150\n"
"v_mfma_f32_16x16x32_bf16 v[152:155], a[204:207], v[108:111], v[152:155]\n"
"v_exp_f32 v151, v151\n"
"v_mfma_f32_16x16x32_bf16 v[156:159], a[208:211], v[96:99], v[160:163]\n"
"v_cvt_pk_bf16_f32 v144, v144, v145\n"
"v_cvt_pk_bf16_f32 v145, v146, v147\n"
"v_mfma_f32_16x16x32_bf16 v[156:159], a[212:215], v[100:103], v[156:159]\n"
"v_cvt_pk_bf16_f32 v146, v148, v149\n"
"v_cvt_pk_bf16_f32 v147, v150, v151\n"
"s_add_u32 m0, s78, 0x13000\n"
"v_mfma_f32_16x16x32_bf16 v[156:159], a[216:219], v[104:107], v[156:159]\n"
"buffer_load_dwordx4 v221, s[40:43], s80 offen lds\n"
"v_mfma_f32_16x16x32_bf16 v[156:159], a[220:223], v[108:111], v[156:159]\n"
"s_add_u32 m0, s77, 0x1e000\n"
"s_xor_b32 s80, s88, 0x201000\n"
"buffer_load_dwordx4 v220, s[48:51], s79 offen offset:2048 nt lds\n"
"v_mfma_f32_16x16x32_bf16 v[200:203], v[204:207], v[144:147], v[200:203]\n"
"s_xor_b32 s79, s100, 0x204000\n"
"ds_read_b64_tr_b16 v[176:177], v211 offset:33792\n"
"v_mfma_f32_16x16x32_bf16 v[8:11], a[224:227], v[144:147], v[8:11]\n"
"ds_read_b64_tr_b16 a[224:225], v211 offset:32768\n"
"ds_read_b64_tr_b16 a[226:227], v211 offset:36864\n"
"v_mfma_f32_16x16x32_bf16 v[20:23], a[228:231], v[144:147], v[20:23]\n"
"ds_read_b64_tr_b16 a[228:229], v214 offset:32768\n"
"ds_read_b64_tr_b16 a[230:231], v214 offset:36864\n"
"v_mfma_f32_16x16x32_bf16 v[32:35], a[232:235], v[144:147], v[32:35]\n"
"ds_read_b64_tr_b16 a[232:233], v211 offset:33280\n"
"ds_read_b64_tr_b16 a[234:235], v211 offset:37376\n"
"v_mfma_f32_16x16x32_bf16 v[44:47], a[236:239], v[144:147], v[44:47]\n"
"ds_read_b64_tr_b16 v[178:179], v211 offset:37888\n"
"ds_read_b64_tr_b16 v[180:181], v214 offset:33792\n"
"v_mfma_f32_16x16x32_bf16 v[56:59], a[240:243], v[144:147], v[56:59]\n"
"ds_read_b64_tr_b16 v[182:183], v214 offset:37888\n"
"ds_read_b64_tr_b16 v[184:185], v211 offset:34304\n"
"v_mfma_f32_16x16x32_bf16 v[68:71], a[244:247], v[144:147], v[68:71]\n"
"v_exp_f32 v152, v152\n"
"v_mfma_f32_16x16x32_bf16 v[80:83], a[248:251], v[144:147], v[80:83]\n"
"v_exp_f32 v153, v153\n"
"v_mfma_f32_16x16x32_bf16 v[92:95], a[252:255], v[144:147], v[92:95]\n"
"v_exp_f32 v154, v154\n"
"v_mfma_f32_16x16x32_bf16 v[144:147], a[192:195], v[112:115], v[164:167]\n"
"v_exp_f32 v155, v155\n"
"v_mfma_f32_16x16x32_bf16 v[144:147], a[196:199], v[116:119], v[144:147]\n"
"v_exp_f32 v156, v156\n"
"v_mfma_f32_16x16x32_bf16 v[144:147], a[200:203], v[120:123], v[144:147]\n"
"v_exp_f32 v157, v157\n"
"v_mfma_f32_16x16x32_bf16 v[144:147], a[204:207], v[124:127], v[144:147]\n"
"v_exp_f32 v158, v158\n"
"v_mfma_f32_16x16x32_bf16 v[148:151], a[208:211], v[112:115], v[164:167]\n"
"v_exp_f32 v159, v159\n"
"v_mfma_f32_16x16x32_bf16 v[148:151], a[212:215], v[116:119], v[148:151]\n"
"ds_read_b64_tr_b16 v[186:187], v211 offset:38400\n"
"v_cvt_pk_bf16_f32 v152, v152, v153\n"
"v_mfma_f32_16x16x32_bf16 v[148:151], a[216:219], v[120:123], v[148:151]\n"
"v_cvt_pk_bf16_f32 v153, v154, v155\n"
"v_cvt_pk_bf16_f32 v154, v156, v157\n"
"v_cvt_pk_bf16_f32 v155, v158, v159\n"
"s_add_u32 m0, s78, 0x0\n"
"v_mfma_f32_16x16x32_bf16 v[148:151], a[220:223], v[124:127], v[148:151]\n"
"buffer_load_dwordx4 v221, s[40:43], s80 offen lds\n"
"ds_read_b64_tr_b16 v[188:189], v214 offset:34304\n"
"v_mfma_f32_16x16x32_bf16 v[192:195], v[204:207], v[152:155], v[192:195]\n"
"ds_read_b64_tr_b16 v[190:191], v214 offset:38400\n"
"s_xor_b32 s80, s88, 0x221000\n"
"s_add_u32 m0, s77, 0x20000\n"
"s_waitcnt lgkmcnt(3)\n"
"v_mfma_f32_16x16x32_bf16 v[0:3], a[224:227], v[152:155], v[0:3]\n"
"buffer_load_dwordx4 v220, s[44:47], s79 offen nt lds\n"
"v_mfma_f32_16x16x32_bf16 v[12:15], a[228:231], v[152:155], v[12:15]\n"
"v_exp_f32 v144, v144\n"
"v_mfma_f32_16x16x32_bf16 v[24:27], a[232:235], v[152:155], v[24:27]\n"
"v_exp_f32 v145, v145\n"
"v_mfma_f32_16x16x32_bf16 v[36:39], v[172:175], v[152:155], v[36:39]\n"
"v_exp_f32 v146, v146\n"
"v_mfma_f32_16x16x32_bf16 v[48:51], v[176:179], v[152:155], v[48:51]\n"
"v_exp_f32 v147, v147\n"
"v_mfma_f32_16x16x32_bf16 v[60:63], v[180:183], v[152:155], v[60:63]\n"
"v_exp_f32 v148, v148\n"
"s_waitcnt lgkmcnt(0)\n"
"v_mfma_f32_16x16x32_bf16 v[72:75], v[184:187], v[152:155], v[72:75]\n"
"v_exp_f32 v149, v149\n"
"v_mfma_f32_16x16x32_bf16 v[84:87], v[188:191], v[152:155], v[84:87]\n"
"s_barrier\n"
"v_mfma_f32_16x16x32_bf16 v[152:155], a[192:195], v[128:131], v[168:171]\n"
"v_exp_f32 v150, v150\n"
"v_mfma_f32_16x16x32_bf16 v[152:155], a[196:199], v[132:135], v[152:155]\n"
"v_exp_f32 v151, v151\n"
"s_add_u32 m0, s78, 0x1000\n"
"v_mfma_f32_16x16x32_bf16 v[152:155], a[200:203], v[136:139], v[152:155]\n"
"buffer_load_dwordx4 v221, s[40:43], s80 offen lds\n"
"v_mfma_f32_16x16x32_bf16 v[152:155], a[204:207], v[140:143], v[152:155]\n"
"s_xor_b32 s80, s88, 0x241000\n"
"v_cvt_pk_bf16_f32 v144, v144, v145\n"
"v_mfma_f32_16x16x32_bf16 v[156:159], a[208:211], v[128:131], v[168:171]\n"
"v_cvt_pk_bf16_f32 v145, v146, v147\n"
"v_cvt_pk_bf16_f32 v146, v148, v149\n"
"v_mfma_f32_16x16x32_bf16 v[156:159], a[212:215], v[132:135], v[156:159]\n"
"v_cvt_pk_bf16_f32 v147, v150, v151\n"
"ds_read_b64_tr_b16 a[192:193], v218 offset:16384\n"
"v_mfma_f32_16x16x32_bf16 v[156:159], a[216:219], v[136:139], v[156:159]\n"
"ds_read_b64_tr_b16 a[194:195], v218 offset:16640\n"
"ds_read_b64_tr_b16 a[196:197], v218 offset:16392\n"
"s_add_u32 m0, s78, 0x2000\n"
"v_mfma_f32_16x16x32_bf16 v[156:159], a[220:223], v[140:143], v[156:159]\n"
"buffer_load_dwordx4 v221, s[40:43], s80 offen lds\n"
"v_mfma_f32_16x16x32_bf16 v[196:199], v[204:207], v[144:147], v[196:199]\n"
"s_add_u32 m0, s77, 0x20000\n"
"s_xor_b32 s80, s88, 0x261000\n"
"buffer_load_dwordx4 v220, s[44:47], s79 offen offset:2048 nt lds\n"
"v_mfma_f32_16x16x32_bf16 v[4:7], a[224:227], v[144:147], v[4:7]\n"
"v_exp_f32 v152, v152\n"
"v_mfma_f32_16x16x32_bf16 v[16:19], a[228:231], v[144:147], v[16:19]\n"
"v_exp_f32 v153, v153\n"
"v_mfma_f32_16x16x32_bf16 v[28:31], a[232:235], v[144:147], v[28:31]\n"
"v_exp_f32 v154, v154\n"
"v_mfma_f32_16x16x32_bf16 v[40:43], v[172:175], v[144:147], v[40:43]\n"
"v_exp_f32 v155, v155\n"
"v_mfma_f32_16x16x32_bf16 v[52:55], v[176:179], v[144:147], v[52:55]\n"
"v_exp_f32 v156, v156\n"
"v_mfma_f32_16x16x32_bf16 v[64:67], v[180:183], v[144:147], v[64:67]\n"
"v_exp_f32 v157, v157\n"
"v_mfma_f32_16x16x32_bf16 v[76:79], v[184:187], v[144:147], v[76:79]\n"
"v_exp_f32 v158, v158\n"
"v_cvt_pk_bf16_f32 v152, v152, v153\n"
"v_mfma_f32_16x16x32_bf16 v[88:91], v[188:191], v[144:147], v[88:91]\n"
"v_exp_f32 v159, v159\n"
"v_cvt_pk_bf16_f32 v153, v154, v155\n"
"v_cvt_pk_bf16_f32 v154, v156, v157\n"
"v_cvt_pk_bf16_f32 v155, v158, v159\n"
"s_nop 1\n"
"v_mfma_f32_16x16x32_bf16 v[200:203], v[204:207], v[152:155], v[200:203]\n"
"ds_read_b64_tr_b16 a[198:199], v218 offset:16648\n"
"ds_read_b64_tr_b16 a[200:201], v218 offset:16896\n"
"s_add_u32 m0, s78, 0x3000\n"
"v_mfma_f32_16x16x32_bf16 v[8:11], a[224:227], v[152:155], v[8:11]\n"
"buffer_load_dwordx4 v221, s[40:43], s80 offen lds\n"
"v_mfma_f32_16x16x32_bf16 v[20:23], a[228:231], v[152:155], v[20:23]\n"
"s_xor_b32 s80, s88, 0x281000\n"
"ds_read_b64_tr_b16 a[202:203], v218 offset:17152\n"
"v_mfma_f32_16x16x32_bf16 v[32:35], a[232:235], v[152:155], v[32:35]\n"
"ds_read_b64_tr_b16 a[204:205], v218 offset:16904\n"
"ds_read_b64_tr_b16 a[206:207], v218 offset:17160\n"
"v_mfma_f32_16x16x32_bf16 v[44:47], v[172:175], v[152:155], v[44:47]\n"
"ds_read_b64_tr_b16 a[208:209], v218 offset:17408\n"
"ds_read_b64_tr_b16 a[210:211], v218 offset:17664\n"
"v_mfma_f32_16x16x32_bf16 v[56:59], v[176:179], v[152:155], v[56:59]\n"
"v_max3_f32 v245, v192, v196, v200\n"
"v_cmp_lt_f32 vcc, s86, v245\n"
"v_mfma_f32_16x16x32_bf16 v[68:71], v[180:183], v[152:155], v[68:71]\n"
"ds_read_b64_tr_b16 a[212:213], v218 offset:17416\n"
"ds_read_b64_tr_b16 a[214:215], v218 offset:17672\n"
"v_mfma_f32_16x16x32_bf16 v[80:83], v[184:187], v[152:155], v[80:83]\n"
"ds_read_b64_tr_b16 a[216:217], v218 offset:17920\n"
"ds_read_b64_tr_b16 a[218:219], v218 offset:18176\n"
"v_mfma_f32_16x16x32_bf16 v[92:95], v[188:191], v[152:155], v[92:95]\n"
"s_cbranch_vccnz Lslow0_%=\n"
"Lback0_%=:\n"
"s_lshl_b32 s21, s30, 19\n"
"s_waitcnt lgkmcnt(4)\n"
"v_mfma_f32_16x16x32_bf16 v[96:99], a[192:195], a[0:3], 0\n"
"v_rcp_f32 v242, v192\n"
"v_mfma_f32_16x16x32_bf16 v[100:103], a[192:195], a[4:7], 0\n"
"ds_read_b64_tr_b16 a[220:221], v218 offset:17928\n"
"ds_read_b64_tr_b16 a[222:223], v218 offset:18184\n"
"v_mfma_f32_16x16x32_bf16 v[104:107], a[192:195], a[8:11], 0\n"
"ds_read_b64_tr_b16 a[224:225], v218 offset:24576\n"
"ds_read_b64_tr_b16 a[226:227], v218 offset:24832\n"
"v_mfma_f32_16x16x32_bf16 v[108:111], a[196:199], a[0:3], 0\n"
"ds_read_b64_tr_b16 a[228:229], v218 offset:24584\n"
"ds_read_b64_tr_b16 a[230:231], v218 offset:24840\n"
"v_mfma_f32_16x16x32_bf16 v[112:115], a[196:199], a[4:7], 0\n"
"ds_read_b64_tr_b16 a[232:233], v218 offset:25088\n"
"ds_read_b64_tr_b16 a[234:235], v218 offset:25344\n"
"v_mfma_f32_16x16x32_bf16 v[116:119], a[196:199], a[8:11], 0\n"
"ds_read_b64_tr_b16 a[236:237], v218 offset:25096\n"
"ds_read_b64_tr_b16 a[238:239], v218 offset:25352\n"
"v_mfma_f32_16x16x32_bf16 v[120:123], a[200:203], a[0:3], 0\n"
"ds_read_b64_tr_b16 a[240:241], v218 offset:25600\n"
"ds_read_b64_tr_b16 a[242:243], v218 offset:25856\n"
"v_mfma_f32_16x16x32_bf16 v[124:127], a[200:203], a[4:7], 0\n"
"ds_read_b64_tr_b16 a[244:245], v218 offset:25608\n"
"ds_read_b64_tr_b16 a[246:247], v218 offset:25864\n"
"v_mfma_f32_16x16x32_bf16 v[128:131], a[200:203], a[8:11], 0\n"
"ds_read_b64_tr_b16 a[248:249], v218 offset:26112\n"
"ds_read_b64_tr_b16 a[250:251], v218 offset:26368\n"
"ds_read_b64_tr_b16 a[252:253], v218 offset:26120\n"
"ds_read_b64_tr_b16 a[254:255], v218 offset:26376\n"
"s_waitcnt vmcnt(20) lgkmcnt(0)\n"
"s_barrier\n"
"s_add_u32 m0, s77, 0x22000\n"
"v_mfma_f32_16x16x32_bf16 v[132:135], a[204:207], a[0:3], 0\n"
"buffer_load_dwordx4 v220, s[48:51], s79 offen nt lds\n"
"s_add_u32 m0, s78, 0x8000\n"
"v_mfma_f32_16x16x32_bf16 v[136:139], a[204:207], a[4:7], 0\n"
"buffer_load_dwordx4 v221, s[40:43], s80 offen lds\n"
"v_mfma_f32_16x16x32_bf16 v[140:143], a[204:207], a[8:11], 0\n"
"v_rcp_f32 v243, v196\n"
"v_mfma_f32_16x16x32_bf16 v[144:147], a[208:211], a[0:3], 0\n"
"s_xor_b32 s80, s88, 0x2a1000\n"
"v_mul_f32 v0, v242, v0\n"
"v_mfma_f32_16x16x32_bf16 v[148:151], a[208:211], a[4:7], 0\n"
"v_mul_f32 v1, v242, v1\n"
"v_cvt_pk_bf16_f32 v226, v0, v1\n"
"v_mfma_f32_16x16x32_bf16 v[152:155], a[208:211], a[8:11], 0\n"
"v_mul_f32 v2, v242, v2\n"
"v_mul_f32 v3, v242, v3\n"
"v_mfma_f32_16x16x32_bf16 v[156:159], a[212:215], a[0:3], 0\n"
"v_cvt_pk_bf16_f32 v227, v2, v3\n"
"v_mul_f32 v12, v242, v12\n"
"v_mfma_f32_16x16x32_bf16 v[160:163], a[212:215], a[4:7], 0\n"
"v_mul_f32 v13, v242, v13\n"
"v_mul_f32 v14, v242, v14\n"
"v_mfma_f32_16x16x32_bf16 v[164:167], a[212:215], a[8:11], 0\n"
"v_mul_f32 v15, v242, v15\n"
"v_cvt_pk_bf16_f32 v228, v12, v13\n"
"v_mfma_f32_16x16x32_bf16 v[168:171], a[216:219], a[0:3], 0\n"
"v_cvt_pk_bf16_f32 v229, v14, v15\n"
"buffer_store_dwordx4 v[226:229], v223, s[56:59], s83 offen sc1\n"
"v_mfma_f32_16x16x32_bf16 v[172:175], a[216:219], a[4:7], 0\n"
"s_add_u32 s83, s82, 0x4000\n"
"ds_read_b64_tr_b16 a[192:193], v218 offset:32768\n"
"v_mfma_f32_16x16x32_bf16 v[176:179], a[216:219], a[8:11], 0\n"
"v_rcp_f32 v244, v200\n"
"s_add_u32 m0, s78, 0x9000\n"
"v_mfma_f32_16x16x32_bf16 v[180:183], a[220:223], a[0:3], 0\n"
"buffer_load_dwordx4 v221, s[40:43], s80 offen lds\n"
"v_mfma_f32_16x16x32_bf16 v[184:187], a[220:223], a[4:7], 0\n"
"s_xor_b32 s80, s88, 0x2c1000\n"
"ds_read_b64_tr_b16 a[194:195], v218 offset:33024\n"
"v_mfma_f32_16x16x32_bf16 v[188:191], a[220:223], a[8:11], 0\n"
"ds_read_b64_tr_b16 a[196:197], v218 offset:32776\n"
"ds_read_b64_tr_b16 a[198:199], v218 offset:33032\n"
"v_mfma_f32_16x16x32_bf16 v[96:99], a[224:227], a[12:15], v[96:99]\n"
"ds_read_b64_tr_b16 a[200:201], v218 offset:33280\n"
"ds_read_b64_tr_b16 a[202:203], v218 offset:33536\n"
"s_add_u32 m0, s77, 0x22000\n"
"v_mfma_f32_16x16x32_bf16 v[100:103], a[224:227], a[16:19], v[100:103]\n"
"buffer_load_dwordx4 v220, s[48:51], s79 offen offset:2048 nt lds\n"
"s_xor_b32 s79, s100, 0x206000\n"
"s_add_u32 m0, s78, 0xa000\n"
"v_mfma_f32_16x16x32_bf16 v[104:107], a[224:227], a[20:23], v[104:107]\n"
"buffer_load_dwordx4 v221, s[40:43], s80 offen lds\n"
"v_mfma_f32_16x16x32_bf16 v[108:111], a[228:231], a[12:15], v[108:111]\n"
"s_xor_b32 s80, s88, 0x2e1000\n"
"ds_read_b64_tr_b16 a[204:205], v218 offset:33288\n"
"v_mfma_f32_16x16x32_bf16 v[112:115], a[228:231], a[16:19], v[112:115]\n"
"ds_read_b64_tr_b16 a[206:207], v218 offset:33544\n"
"ds_read_b64_tr_b16 a[208:209], v218 offset:33792\n"
"v_mfma_f32_16x16x32_bf16 v[116:119], a[228:231], a[20:23], v[116:119]\n"
"ds_read_b64_tr_b16 a[210:211], v218 offset:34048\n"
"ds_read_b64_tr_b16 a[212:213], v218 offset:33800\n"
"s_add_u32 m0, s78, 0xb000\n"
"v_mfma_f32_16x16x32_bf16 v[120:123], a[232:235], a[12:15], v[120:123]\n"
"buffer_load_dwordx4 v221, s[40:43], s80 offen lds\n"
"v_mfma_f32_16x16x32_bf16 v[124:127], a[232:235], a[16:19], v[124:127]\n"
"s_xor_b32 s80, s88, 0x301000\n"
"ds_read_b64_tr_b16 a[214:215], v218 offset:34056\n"
"v_mfma_f32_16x16x32_bf16 v[128:131], a[232:235], a[20:23], v[128:131]\n"
"v_mul_f32 v24, v242, v24\n"
"v_mul_f32 v25, v242, v25\n"
"v_mfma_f32_16x16x32_bf16 v[132:135], a[236:239], a[12:15], v[132:135]\n"
"v_cvt_pk_bf16_f32 v230, v24, v25\n"
"v_mul_f32 v26, v242, v26\n"
"v_mfma_f32_16x16x32_bf16 v[136:139], a[236:239], a[16:19], v[136:139]\n"
"v_mul_f32 v27, v242, v27\n"
"v_cvt_pk_bf16_f32 v231, v26, v27\n"
"v_mfma_f32_16x16x32_bf16 v[140:143], a[236:239], a[20:23], v[140:143]\n"
"v_mul_f32 v36, v242, v36\n"
"v_mul_f32 v37, v242, v37\n"
"v_mfma_f32_16x16x32_bf16 v[144:147], a[240:243], a[12:15], v[144:147]\n"
"v_cvt_pk_bf16_f32 v232, v36, v37\n"
"v_mul_f32 v38, v242, v38\n"
"v_mfma_f32_16x16x32_bf16 v[148:151], a[240:243], a[16:19], v[148:151]\n"
"v_mul_f32 v39, v242, v39\n"
"v_cvt_pk_bf16_f32 v233, v38, v39\n"
"v_mfma_f32_16x16x32_bf16 v[152:155], a[240:243], a[20:23], v[152:155]\n"
"ds_read_b64_tr_b16 a[216:217], v218 offset:34304\n"
"ds_read_b64_tr_b16 a[218:219], v218 offset:34560\n"
"v_mfma_f32_16x16x32_bf16 v[156:159], a[244:247], a[12:15], v[156:159]\n"
"ds_read_b64_tr_b16 a[220:221], v218 offset:34312\n"
"ds_read_b64_tr_b16 a[222:223], v218 offset:34568\n"
"v_mfma_f32_16x16x32_bf16 v[160:163], a[244:247], a[16:19], v[160:163]\n"
"buffer_store_dwordx4 v[230:233], v223, s[56:59], s83 offen sc1\n"
"s_add_u32 s83, s82, 0x8000\n"
"s_add_u32 m0, s77, 0x24000\n"
"v_mfma_f32_16x16x32_bf16 v[164:167], a[244:247], a[20:23], v[164:167]\n"
"buffer_load_dwordx4 v220, s[44:47], s79 offen nt lds\n"
"v_mfma_f32_16x16x32_bf16 v[168:171], a[248:251], a[12:15], v[168:171]\n"
"ds_read_b64_tr_b16 a[224:225], v218 offset:40960\n"
"ds_read_b64_tr_b16 a[226:227], v218 offset:41216\n"
"v_mfma_f32_16x16x32_bf16 v[172:175], a[248:251], a[16:19], v[172:175]\n"
"ds_read_b64_tr_b16 a[228:229], v218 offset:40968\n"
"ds_read_b64_tr_b16 a[230:231], v218 offset:41224\n"
"v_mfma_f32_16x16x32_bf16 v[176:179], a[248:251], a[20:23], v[176:179]\n"
"ds_read_b64_tr_b16 a[232:233], v218 offset:41472\n"
"ds_read_b64_tr_b16 a[234:235], v218 offset:41728\n"
"v_mfma_f32_16x16x32_bf16 v[180:183], a[252:255], a[12:15], v[180:183]\n"
"ds_read_b64_tr_b16 a[236:237], v218 offset:41480\n"
"ds_read_b64_tr_b16 a[238:239], v218 offset:41736\n"
"v_mfma_f32_16x16x32_bf16 v[184:187], a[252:255], a[16:19], v[184:187]\n"
"ds_read_b64_tr_b16 a[240:241], v218 offset:41984\n"
"ds_read_b64_tr_b16 a[242:243], v218 offset:42240\n"
"v_mfma_f32_16x16x32_bf16 v[188:191], a[252:255], a[20:23], v[188:191]\n"
"ds_read_b64_tr_b16 a[244:245], v218 offset:41992\n"
"ds_read_b64_tr_b16 a[246:247], v218 offset:42248\n"
"s_waitcnt lgkmcnt(8)\n"
"v_mfma_f32_16x16x32_bf16 v[96:99], a[192:195], a[24:27], v[96:99]\n"
"ds_read_b64_tr_b16 a[248:249], v218 offset:42496\n"
"ds_read_b64_tr_b16 a[250:251], v218 offset:42752\n"
"v_mfma_f32_16x16x32_bf16 v[100:103], a[192:195], a[28:31], v[100:103]\n"
"ds_read_b64_tr_b16 a[252:253], v218 offset:42504\n"
"ds_read_b64_tr_b16 a[254:255], v218 offset:42760\n"
"s_add_u32 m0, s78, 0x14000\n"
"v_mfma_f32_16x16x32_bf16 v[104:107], a[192:195], a[32:35], v[104:107]\n"
"buffer_load_dwordx4 v221, s[40:43], s80 offen lds\n"
"v_mfma_f32_16x16x32_bf16 v[108:111], a[196:199], a[24:27], v[108:111]\n"
"s_xor_b32 s80, s88, 0x321000\n"
"v_mfma_f32_16x16x32_bf16 v[112:115], a[196:199], a[28:31], v[112:115]\n"
"v_mfma_f32_16x16x32_bf16 v[116:119], a[196:199], a[32:35], v[116:119]\n"
"v_mfma_f32_16x16x32_bf16 v[120:123], a[200:203], a[24:27], v[120:123]\n"
"v_mul_f32 v48, v242, v48\n"
"v_mul_f32 v49, v242, v49\n"
"v_mfma_f32_16x16x32_bf16 v[124:127], a[200:203], a[28:31], v[124:127]\n"
"v_cvt_pk_bf16_f32 v234, v48, v49\n"
"v_mul_f32 v50, v242, v50\n"
"v_mfma_f32_16x16x32_bf16 v[128:131], a[200:203], a[32:35], v[128:131]\n"
"s_waitcnt vmcnt(23) lgkmcnt(0)\n"
"s_barrier\n"
"v_mfma_f32_16x16x32_bf16 v[132:135], a[204:207], a[24:27], v[132:135]\n"
"v_mul_f32 v51, v242, v51\n"
"v_cvt_pk_bf16_f32 v235, v50, v51\n"
"v_mfma_f32_16x16x32_bf16 v[136:139], a[204:207], a[28:31], v[136:139]\n"
"v_mul_f32 v60, v242, v60\n"
"v_mul_f32 v61, v242, v61\n"
"v_mfma_f32_16x16x32_bf16 v[140:143], a[204:207], a[32:35], v[140:143]\n"
"v_cvt_pk_bf16_f32 v236, v60, v61\n"
"v_mul_f32 v62, v242, v62\n"
"v_mfma_f32_16x16x32_bf16 v[144:147], a[208:211], a[24:27], v[144:147]\n"
"v_mul_f32 v63, v242, v63\n"
"v_cvt_pk_bf16_f32 v237, v62, v63\n"
"s_add_u32 m0, s78, 0x15000\n"
"v_mfma_f32_16x16x32_bf16 v[148:151], a[208:211], a[28:31], v[148:151]\n"
"buffer_load_dwordx4 v221, s[40:43], s80 offen lds\n"
"v_mfma_f32_16x16x32_bf16 v[152:155], a[208:211], a[32:35], v[152:155]\n"
"buffer_store_dwordx4 v[234:237], v223, s[56:59], s83 offen sc1\n"
"s_add_u32 s83, s82, 0xc000\n"
"v_mfma_f32_16x16x32_bf16 v[156:159], a[212:215], a[24:27], v[156:159]\n"
"s_add_u32 m0, s77, 0x24000\n"
"s_xor_b32 s80, s88, 0x341000\n"
"buffer_load_dwordx4 v220, s[44:47], s79 offen offset:2048 nt lds\n"
"v_mfma_f32_16x16x32_bf16 v[160:163], a[212:215], a[28:31], v[160:163]\n"
"ds_read_b64_tr_b16 a[192:193], v217 offset:16384\n"
"ds_read_b64_tr_b16 a[194:195], v217 offset:16640\n"
"s_add_u32 m0, s78, 0x16000\n"
"v_mfma_f32_16x16x32_bf16 v[164:167], a[212:215], a[32:35], v[164:167]\n"
"buffer_load_dwordx4 v221, s[40:43], s80 offen lds\n"
"v_mfma_f32_16x16x32_bf16 v[168:171], a[216:219], a[24:27], v[168:171]\n"
"s_xor_b32 s80, s88, 0x361000\n"
"ds_read_b64_tr_b16 a[196:197], v217 offset:16392\n"
"v_mfma_f32_16x16x32_bf16 v[172:175], a[216:219], a[28:31], v[172:175]\n"
"ds_read_b64_tr_b16 a[198:199], v217 offset:16648\n"
"ds_read_b64_tr_b16 a[200:201], v217 offset:16896\n"
"v_mfma_f32_16x16x32_bf16 v[176:179], a[216:219], a[32:35], v[176:179]\n"
"ds_read_b64_tr_b16 a[202:203], v217 offset:17152\n"
"ds_read_b64_tr_b16 a[204:205], v217 offset:16904\n"
"s_add_u32 m0, s78, 0x17000\n"
"v_mfma_f32_16x16x32_bf16 v[180:183], a[220:223], a[24:27], v[180:183]\n"
"buffer_load_dwordx4 v221, s[40:43], s80 offen lds\n"
"v_mfma_f32_16x16x32_bf16 v[184:187], a[220:223], a[28:31], v[184:187]\n"
"s_xor_b32 s80, s88, 0x381000\n"
"ds_read_b64_tr_b16 a[206:207], v217 offset:17160\n"
"v_mfma_f32_16x16x32_bf16 v[188:191], a[220:223], a[32:35], v[188:191]\n"
"ds_read_b64_tr_b16 a[208:209], v217 offset:17408\n"
"ds_read_b64_tr_b16 a[210:211], v217 offset:17664\n"
"v_mfma_f32_16x16x32_bf16 v[96:99], a[224:227], a[36:39], v[96:99]\n"
"ds_read_b64_tr_b16 a[212:213], v217 offset:17416\n"
"ds_read_b64_tr_b16 a[214:215], v217 offset:17672\n"
"v_mfma_f32_16x16x32_bf16 v[100:103], a[224:227], a[40:43], v[100:103]\n"
"ds_read_b64_tr_b16 a[216:217], v217 offset:17920\n"
"ds_read_b64_tr_b16 a[218:219], v217 offset:18176\n"
"v_mfma_f32_16x16x32_bf16 v[104:107], a[224:227], a[44:47], v[104:107]\n"
"ds_read_b64_tr_b16 a[220:221], v217 offset:17928\n"
"ds_read_b64_tr_b16 a[222:223], v217 offset:18184\n"
"v_mfma_f32_16x16x32_bf16 v[108:111], a[228:231], a[36:39], v[108:111]\n"
"ds_read_b64_tr_b16 a[224:225], v217 offset:24576\n"
"ds_read_b64_tr_b16 a[226:227], v217 offset:24832\n"
"v_mfma_f32_16x16x32_bf16 v[112:115], a[228:231], a[40:43], v[112:115]\n"
"v_mul_f32 v72, v242, v72\n"
"v_mul_f32 v73, v242, v73\n"
"v_mfma_f32_16x16x32_bf16 v[116:119], a[228:231], a[44:47], v[116:119]\n"
"v_cvt_pk_bf16_f32 v238, v72, v73\n"
"v_mul_f32 v74, v242, v74\n"
"v_mfma_f32_16x16x32_bf16 v[120:123], a[232:235], a[36:39], v[120:123]\n"
"v_mul_f32 v75, v242, v75\n"
"v_cvt_pk_bf16_f32 v239, v74, v75\n"
"v_mfma_f32_16x16x32_bf16 v[124:127], a[232:235], a[40:43], v[124:127]\n"
"v_mul_f32 v84, v242, v84\n"
"v_mul_f32 v85, v242, v85\n"
"v_mfma_f32_16x16x32_bf16 v[128:131], a[232:235], a[44:47], v[128:131]\n"
"v_cvt_pk_bf16_f32 v240, v84, v85\n"
"v_mul_f32 v86, v242, v86\n"
"v_mfma_f32_16x16x32_bf16 v[132:135], a[236:239], a[36:39], v[132:135]\n"
"v_mul_f32 v87, v242, v87\n"
"v_cvt_pk_bf16_f32 v241, v86, v87\n"
"s_add_u32 m0, s77, 0x26000\n"
"v_mfma_f32_16x16x32_bf16 v[136:139], a[236:239], a[40:43], v[136:139]\n"
"buffer_load_dwordx4 v220, s[48:51], s79 offen nt lds\n"
"v_mfma_f32_16x16x32_bf16 v[140:143], a[236:239], a[44:47], v[140:143]\n"
"ds_read_b64_tr_b16 a[228:229], v217 offset:24584\n"
"ds_read_b64_tr_b16 a[230:231], v217 offset:24840\n"
"v_mfma_f32_16x16x32_bf16 v[144:147], a[240:243], a[36:39], v[144:147]\n"
"buffer_store_dwordx4 v[238:241], v223, s[56:59], s83 offen sc1\n"
"s_add_u32 s83, s82, 0x400\n"
"v_mfma_f32_16x16x32_bf16 v[148:151], a[240:243], a[40:43], v[148:151]\n"
"ds_read_b64_tr_b16 a[232:233], v217 offset:25088\n"
"ds_read_b64_tr_b16 a[234:235], v217 offset:25344\n"
"v_mfma_f32_16x16x32_bf16 v[152:155], a[240:243], a[44:47], v[152:155]\n"
"ds_read_b64_tr_b16 a[236:237], v217 offset:25096\n"
"ds_read_b64_tr_b16 a[238:239], v217 offset:25352\n"
"v_mfma_f32_16x16x32_bf16 v[156:159], a[244:247], a[36:39], v[156:159]\n"
"ds_read_b64_tr_b16 a[240:241], v217 offset:25600\n"
"ds_read_b64_tr_b16 a[242:243], v217 offset:25856\n"
"v_mfma_f32_16x16x32_bf16 v[160:163], a[244:247], a[40:43], v[160:163]\n"
"v_mfma_f32_16x16x32_bf16 v[164:167], a[244:247], a[44:47], v[164:167]\n"
"ds_read_b64_tr_b16 a[244:245], v217 offset:25608\n"
"ds_read_b64_tr_b16 a[246:247], v217 offset:25864\n"
"v_mfma_f32_16x16x32_bf16 v[168:171], a[248:251], a[36:39], v[168:171]\n"
"v_mfma_f32_16x16x32_bf16 v[172:175], a[248:251], a[40:43], v[172:175]\n"
"v_mfma_f32_16x16x32_bf16 v[176:179], a[248:251], a[44:47], v[176:179]\n"
"ds_read_b64_tr_b16 a[248:249], v217 offset:26112\n"
"ds_read_b64_tr_b16 a[250:251], v217 offset:26368\n"
"v_mfma_f32_16x16x32_bf16 v[180:183], a[252:255], a[36:39], v[180:183]\n"
"v_mfma_f32_16x16x32_bf16 v[184:187], a[252:255], a[40:43], v[184:187]\n"
"v_mfma_f32_16x16x32_bf16 v[188:191], a[252:255], a[44:47], v[188:191]\n"
"ds_read_b64_tr_b16 a[252:253], v217 offset:26120\n"
"ds_read_b64_tr_b16 a[254:255], v217 offset:26376\n"
"s_add_u32 m0, s78, 0x18000\n"
"s_waitcnt lgkmcnt(4)\n"
"v_mfma_f32_16x16x32_bf16 v[96:99], a[192:195], a[48:51], v[96:99]\n"
"buffer_load_dwordx4 v221, s[40:43], s80 offen lds\n"
"v_mfma_f32_16x16x32_bf16 v[100:103], a[192:195], a[52:55], v[100:103]\n"
"s_xor_b32 s80, s88, 0x3a1000\n"
"v_mfma_f32_16x16x32_bf16 v[104:107], a[192:195], a[56:59], v[104:107]\n"
"v_mul_f32 v4, v243, v4\n"
"v_mul_f32 v5, v243, v5\n"
"v_mfma_f32_16x16x32_bf16 v[108:111], a[196:199], a[48:51], v[108:111]\n"
"v_cvt_pk_bf16_f32 v226, v4, v5\n"
"v_mul_f32 v6, v243, v6\n"
"v_mfma_f32_16x16x32_bf16 v[112:115], a[196:199], a[52:55], v[112:115]\n"
"v_mul_f32 v7, v243, v7\n"
"v_cvt_pk_bf16_f32 v227, v6, v7\n"
"v_mfma_f32_16x16x32_bf16 v[116:119], a[196:199], a[56:59], v[116:119]\n"
"v_mul_f32 v16, v243, v16\n"
"v_mul_f32 v17, v243, v17\n"
"v_mfma_f32_16x16x32_bf16 v[120:123], a[200:203], a[48:51], v[120:123]\n"
"v_cvt_pk_bf16_f32 v228, v16, v17\n"
"v_mul_f32 v18, v243, v18\n"
"v_mfma_f32_16x16x32_bf16 v[124:127], a[200:203], a[52:55], v[124:127]\n"
"v_mul_f32 v19, v243, v19\n"
"v_cvt_pk_bf16_f32 v229, v18, v19\n"
"v_mfma_f32_16x16x32_bf16 v[128:131], a[200:203], a[56:59], v[128:131]\n"
"s_waitcnt vmcnt(25) lgkmcnt(0)\n"
"s_barrier\n"
"s_add_u32 m0, s77, 0x26000\n"
"v_mfma_f32_16x16x32_bf16 v[132:135], a[204:207], a[48:51], v[132:135]\n"
"buffer_load_dwordx4 v220, s[48:51], s79 offen offset:2048 nt lds\n"
"s_xor_b32 s79, s100, 0x208000\n"
"v_mfma_f32_16x16x32_bf16 v[136:139], a[204:207], a[52:55], v[136:139]\n"
"buffer_store_dwordx4 v[226:229], v223, s[56:59], s83 offen sc1\n"
"s_add_u32 s83, s82, 0x4400\n"
"s_add_u32 m0, s78, 0x19000\n"
"v_mfma_f32_16x16x32_bf16 v[140:143], a[204:207], a[56:59], v[140:143]\n"
"buffer_load_dwordx4 v221, s[40:43], s80 offen lds\n"
"v_mfma_f32_16x16x32_bf16 v[144:147], a[208:211], a[48:51], v[144:147]\n"
"s_xor_b32 s80, s88, 0x3c1000\n"
"ds_read_b64_tr_b16 a[192:193], v218 offset:0\n"
"v_mfma_f32_16x16x32_bf16 v[148:151], a[208:211], a[52:55], v[148:151]\n"
"ds_read_b64_tr_b16 a[194:195], v218 offset:256\n"
"ds_read_b64_tr_b16 a[196:197], v218 offset:8\n"
"v_mfma_f32_16x16x32_bf16 v[152:155], a[208:211], a[56:59], v[152:155]\n"
"ds_read_b64_tr_b16 a[198:199], v218 offset:264\n"
"ds_read_b64_tr_b16 a[200:201], v218 offset:512\n"
"s_add_u32 m0, s78, 0x1a000\n"
"v_mfma_f32_16x16x32_bf16 v[156:159], a[212:215], a[48:51], v[156:159]\n"
"buffer_load_dwordx4 v221, s[40:43], s80 offen lds\n"
"v_mfma_f32_16x16x32_bf16 v[160:163], a[212:215], a[52:55], v[160:163]\n"
"s_xor_b32 s80, s88, 0x3e1000\n"
"ds_read_b64_tr_b16 a[202:203], v218 offset:768\n"
"v_mfma_f32_16x16x32_bf16 v[164:167], a[212:215], a[56:59], v[164:167]\n"
"ds_read_b64_tr_b16 a[204:205], v218 offset:520\n"
"ds_read_b64_tr_b16 a[206:207], v218 offset:776\n"
"v_mfma_f32_16x16x32_bf16 v[168:171], a[216:219], a[48:51], v[168:171]\n"
"ds_read_b64_tr_b16 a[208:209], v218 offset:1024\n"
"ds_read_b64_tr_b16 a[210:211], v218 offset:1280\n"
"s_add_u32 m0, s78, 0x1b000\n"
"v_mfma_f32_16x16x32_bf16 v[172:175], a[216:219], a[52:55], v[172:175]\n"
"buffer_load_dwordx4 v221, s[40:43], s80 offen lds\n"
"v_mfma_f32_16x16x32_bf16 v[176:179], a[216:219], a[56:59], v[176:179]\n"
"ds_read_b64_tr_b16 a[212:213], v218 offset:1032\n"
"ds_read_b64_tr_b16 a[214:215], v218 offset:1288\n"
"v_mfma_f32_16x16x32_bf16 v[180:183], a[220:223], a[48:51], v[180:183]\n"
"ds_read_b64_tr_b16 a[216:217], v218 offset:1536\n"
"ds_read_b64_tr_b16 a[218:219], v218 offset:1792\n"
"v_mfma_f32_16x16x32_bf16 v[184:187], a[220:223], a[52:55], v[184:187]\n"
"s_sub_u32 s80, s76, s30\n"
"s_and_b32 s80, s80, 3\n"
"v_mfma_f32_16x16x32_bf16 v[188:191], a[220:223], a[56:59], v[188:191]\n"
"ds_read_b64_tr_b16 a[220:221], v218 offset:1544\n"
"ds_read_b64_tr_b16 a[222:223], v218 offset:1800\n"
"v_mfma_f32_16x16x32_bf16 v[96:99], a[224:227], a[60:63], v[96:99]\n"
"v_mul_f32 v28, v243, v28\n"
"v_mul_f32 v29, v243, v29\n"
"v_mfma_f32_16x16x32_bf16 v[100:103], a[224:227], a[64:67], v[100:103]\n"
"v_cvt_pk_bf16_f32 v230, v28, v29\n"
"v_mul_f32 v30, v243, v30\n"
"v_mfma_f32_16x16x32_bf16 v[104:107], a[224:227], a[68:71], v[104:107]\n"
"v_mul_f32 v31, v243, v31\n"
"v_cvt_pk_bf16_f32 v231, v30, v31\n"
"v_mfma_f32_16x16x32_bf16 v[108:111], a[228:231], a[60:63], v[108:111]\n"
"v_mul_f32 v40, v243, v40\n"
"v_mul_f32 v41, v243, v41\n"
"v_mfma_f32_16x16x32_bf16 v[112:115], a[228:231], a[64:67], v[112:115]\n"
"v_cvt_pk_bf16_f32 v232, v40, v41\n"
"v_mul_f32 v42, v243, v42\n"
"v_mfma_f32_16x16x32_bf16 v[116:119], a[228:231], a[68:71], v[116:119]\n"
"v_mul_f32 v43, v243, v43\n"
"v_cvt_pk_bf16_f32 v233, v42, v43\n"
"s_add_u32 m0, s77, 0x4000\n"
"v_mfma_f32_16x16x32_bf16 v[120:123], a[232:235], a[60:63], v[120:123]\n"
"buffer_load_dwordx4 v220, s[44:47], s79 offen nt lds\n"
"v_mfma_f32_16x16x32_bf16 v[124:127], a[232:235], a[64:67], v[124:127]\n"
"ds_read_b64_tr_b16 a[224:225], v218 offset:8192\n"
"ds_read_b64_tr_b16 a[226:227], v218 offset:8448\n"
"v_mfma_f32_16x16x32_bf16 v[128:131], a[232:235], a[68:71], v[128:131]\n"
"buffer_store_dwordx4 v[230:233], v223, s[56:59], s83 offen sc1\n"
"s_add_u32 s83, s82, 0x8400\n"
"v_mfma_f32_16x16x32_bf16 v[132:135], a[236:239], a[60:63], v[132:135]\n"
"ds_read_b64_tr_b16 a[228:229], v218 offset:8200\n"
"ds_read_b64_tr_b16 a[230:231], v218 offset:8456\n"
"v_mfma_f32_16x16x32_bf16 v[136:139], a[236:239], a[64:67], v[136:139]\n"
"ds_read_b64_tr_b16 a[232:233], v218 offset:8704\n"
"ds_read_b64_tr_b16 a[234:235], v218 offset:8960\n"
"v_mfma_f32_16x16x32_bf16 v[140:143], a[236:239], a[68:71], v[140:143]\n"
"ds_read_b64_tr_b16 a[236:237], v218 offset:8712\n"
"ds_read_b64_tr_b16 a[238:239], v218 offset:8968\n"
"v_mfma_f32_16x16x32_bf16 v[144:147], a[240:243], a[60:63], v[144:147]\n"
"s_mul_i32 s80, s80, 0xc00\n"
"v_mfma_f32_16x16x32_bf16 v[148:151], a[240:243], a[64:67], v[148:151]\n"
"v_mfma_f32_16x16x32_bf16 v[152:155], a[240:243], a[68:71], v[152:155]\n"
"ds_read_b64_tr_b16 a[240:241], v218 offset:9216\n"
"ds_read_b64_tr_b16 a[242:243], v218 offset:9472\n"
"v_mfma_f32_16x16x32_bf16 v[156:159], a[244:247], a[60:63], v[156:159]\n"
"v_mfma_f32_16x16x32_bf16 v[160:163], a[244:247], a[64:67], v[160:163]\n"
"v_mfma_f32_16x16x32_bf16 v[164:167], a[244:247], a[68:71], v[164:167]\n"
"ds_read_b64_tr_b16 a[244:245], v218 offset:9224\n"
"ds_read_b64_tr_b16 a[246:247], v218 offset:9480\n"
"v_mfma_f32_16x16x32_bf16 v[168:171], a[248:251], a[60:63], v[168:171]\n"
"v_mfma_f32_16x16x32_bf16 v[172:175], a[248:251], a[64:67], v[172:175]\n"
"v_mfma_f32_16x16x32_bf16 v[176:179], a[248:251], a[68:71], v[176:179]\n"
"ds_read_b64_tr_b16 a[248:249], v218 offset:9728\n"
"ds_read_b64_tr_b16 a[250:251], v218 offset:9984\n"
"v_mfma_f32_16x16x32_bf16 v[180:183], a[252:255], a[60:63], v[180:183]\n"
"v_mfma_f32_16x16x32_bf16 v[184:187], a[252:255], a[64:67], v[184:187]\n"
"v_mul_f32 v52, v243, v52\n"
"v_mul_f32 v53, v243, v53\n"
"v_mfma_f32_16x16x32_bf16 v[188:191], a[252:255], a[68:71], v[188:191]\n"
"v_cvt_pk_bf16_f32 v234, v52, v53\n"
"v_mul_f32 v54, v243, v54\n"
"s_waitcnt lgkmcnt(2)\n"
"v_mfma_f32_16x16x32_bf16 v[96:99], a[192:195], a[72:75], v[96:99]\n"
"v_mul_f32 v55, v243, v55\n"
"v_cvt_pk_bf16_f32 v235, v54, v55\n"
"v_mfma_f32_16x16x32_bf16 v[100:103], a[192:195], a[76:79], v[100:103]\n"
"v_mul_f32 v64, v243, v64\n"
"v_mul_f32 v65, v243, v65\n"
"v_mfma_f32_16x16x32_bf16 v[104:107], a[192:195], a[80:83], v[104:107]\n"
"v_cvt_pk_bf16_f32 v236, v64, v65\n"
"v_mul_f32 v66, v243, v66\n"
"v_mfma_f32_16x16x32_bf16 v[108:111], a[196:199], a[72:75], v[108:111]\n"
"v_mul_f32 v67, v243, v67\n"
"v_cvt_pk_bf16_f32 v237, v66, v67\n"
"v_mfma_f32_16x16x32_bf16 v[112:115], a[196:199], a[76:79], v[112:115]\n"
"ds_read_b64_tr_b16 a[252:253], v218 offset:9736\n"
"ds_read_b64_tr_b16 a[254:255], v218 offset:9992\n"
"v_mfma_f32_16x16x32_bf16 v[116:119], a[196:199], a[80:83], v[116:119]\n"
"buffer_load_dwordx4 v220, s[44:47], s79 offen offset:2048 nt lds\n"
"v_mfma_f32_16x16x32_bf16 v[120:123], a[200:203], a[72:75], v[120:123]\n"
"buffer_store_dwordx4 v[234:237], v223, s[56:59], s83 offen sc1\n"
"s_add_u32 s83, s82, 0xc400\n"
"v_mfma_f32_16x16x32_bf16 v[124:127], a[200:203], a[76:79], v[124:127]\n"
"v_mfma_f32_16x16x32_bf16 v[128:131], a[200:203], a[80:83], v[128:131]\n"
"s_waitcnt vmcnt(27) lgkmcnt(0)\n"
"s_barrier\n"
"v_mfma_f32_16x16x32_bf16 v[132:135], a[204:207], a[72:75], v[132:135]\n"
"ds_read_b64_tr_b16 a[192:193], v217 offset:0\n"
"ds_read_b64_tr_b16 a[194:195], v217 offset:256\n"
"v_mfma_f32_16x16x32_bf16 v[136:139], a[204:207], a[76:79], v[136:139]\n"
"ds_read_b64_tr_b16 a[196:197], v217 offset:8\n"
"ds_read_b64_tr_b16 a[198:199], v217 offset:264\n"
"v_mfma_f32_16x16x32_bf16 v[140:143], a[204:207], a[80:83], v[140:143]\n"
"ds_read_b64_tr_b16 a[200:201], v217 offset:512\n"
"ds_read_b64_tr_b16 a[202:203], v217 offset:768\n"
"v_mfma_f32_16x16x32_bf16 v[144:147], a[208:211], a[72:75], v[144:147]\n"
"ds_read_b64_tr_b16 a[204:205], v217 offset:520\n"
"ds_read_b64_tr_b16 a[206:207], v217 offset:776\n"
"v_mfma_f32_16x16x32_bf16 v[148:151], a[208:211], a[76:79], v[148:151]\n"
"v_mfma_f32_16x16x32_bf16 v[152:155], a[208:211], a[80:83], v[152:155]\n"
"ds_read_b64_tr_b16 a[208:209], v217 offset:1024\n"
"ds_read_b64_tr_b16 a[210:211], v217 offset:1280\n"
"v_mfma_f32_16x16x32_bf16 v[156:159], a[212:215], a[72:75], v[156:159]\n"
"v_mfma_f32_16x16x32_bf16 v[160:163], a[212:215], a[76:79], v[160:163]\n"
"v_mfma_f32_16x16x32_bf16 v[164:167], a[212:215], a[80:83], v[164:167]\n"
"ds_read_b64_tr_b16 a[212:213], v217 offset:1032\n"
"ds_read_b64_tr_b16 a[214:215], v217 offset:1288\n"
"v_mfma_f32_16x16x32_bf16 v[168:171], a[216:219], a[72:75], v[168:171]\n"
"v_mfma_f32_16x16x32_bf16 v[172:175], a[216:219], a[76:79], v[172:175]\n"
"v_mfma_f32_16x16x32_bf16 v[176:179], a[216:219], a[80:83], v[176:179]\n"
"v_mul_f32 v76, v243, v76\n"
"v_mul_f32 v77, v243, v77\n"
"v_mfma_f32_16x16x32_bf16 v[180:183], a[220:223], a[72:75], v[180:183]\n"
"v_cvt_pk_bf16_f32 v238, v76, v77\n"
"v_mul_f32 v78, v243, v78\n"
"v_mfma_f32_16x16x32_bf16 v[184:187], a[220:223], a[76:79], v[184:187]\n"
"v_mul_f32 v79, v243, v79\n"
"v_cvt_pk_bf16_f32 v239, v78, v79\n"
"v_mfma_f32_16x16x32_bf16 v[188:191], a[220:223], a[80:83], v[188:191]\n"
"v_mul_f32 v88, v243, v88\n"
"v_mul_f32 v89, v243, v89\n"
"v_mfma_f32_16x16x32_bf16 v[96:99], a[224:227], a[84:87], v[96:99]\n"
"v_cvt_pk_bf16_f32 v240, v88, v89\n"
"v_mul_f32 v90, v243, v90\n"
"v_mfma_f32_16x16x32_bf16 v[100:103], a[224:227], a[88:91], v[100:103]\n"
"v_mul_f32 v91, v243, v91\n"
"v_cvt_pk_bf16_f32 v241, v90, v91\n"
"s_add_u32 m0, s77, 0x6000\n"
"v_mfma_f32_16x16x32_bf16 v[104:107], a[224:227], a[92:95], v[104:107]\n"
"buffer_load_dwordx4 v220, s[48:51], s79 offen nt lds\n"
"v_mfma_f32_16x16x32_bf16 v[108:111], a[228:231], a[84:87], v[108:111]\n"
"ds_read_b64_tr_b16 a[216:217], v217 offset:1536\n"
"ds_read_b64_tr_b16 a[218:219], v217 offset:1792\n"
"v_mfma_f32_16x16x32_bf16 v[112:115], a[228:231], a[88:91], v[112:115]\n"
"buffer_store_dwordx4 v[238:241], v223, s[56:59], s83 offen sc1\n"
"s_add_u32 s83, s82, 0x800\n"
"v_mfma_f32_16x16x32_bf16 v[116:119], a[228:231], a[92:95], v[116:119]\n"
"ds_read_b64_tr_b16 a[220:221], v217 offset:1544\n"
"ds_read_b64_tr_b16 a[222:223], v217 offset:1800\n"
"v_mfma_f32_16x16x32_bf16 v[120:123], a[232:235], a[84:87], v[120:123]\n"
"ds_read_b64_tr_b16 a[224:225], v217 offset:8192\n"
"ds_read_b64_tr_b16 a[226:227], v217 offset:8448\n"
"v_mfma_f32_16x16x32_bf16 v[124:127], a[232:235], a[88:91], v[124:127]\n"
"ds_read_b64_tr_b16 a[228:229], v217 offset:8200\n"
"ds_read_b64_tr_b16 a[230:231], v217 offset:8456\n"
"v_mfma_f32_16x16x32_bf16 v[128:131], a[232:235], a[92:95], v[128:131]\n"
"ds_read_b64_tr_b16 a[232:233], v217 offset:8704\n"
"ds_read_b64_tr_b16 a[234:235], v217 offset:8960\n"
"v_mfma_f32_16x16x32_bf16 v[132:135], a[236:239], a[84:87], v[132:135]\n"
"v_mfma_f32_16x16x32_bf16 v[136:139], a[236:239], a[88:91], v[136:139]\n"
"v_mfma_f32_16x16x32_bf16 v[140:143], a[236:239], a[92:95], v[140:143]\n"
"ds_read_b64_tr_b16 a[236:237], v217 offset:8712\n"
"ds_read_b64_tr_b16 a[238:239], v217 offset:8968\n"
"v_mfma_f32_16x16x32_bf16 v[144:147], a[240:243], a[84:87], v[144:147]\n"
"v_mfma_f32_16x16x32_bf16 v[148:151], a[240:243], a[88:91], v[148:151]\n"
"v_mfma_f32_16x16x32_bf16 v[152:155], a[240:243], a[92:95], v[152:155]\n"
"ds_read_b64_tr_b16 a[240:241], v217 offset:9216\n"
"ds_read_b64_tr_b16 a[242:243], v217 offset:9472\n"
"v_mfma_f32_16x16x32_bf16 v[156:159], a[244:247], a[84:87], v[156:159]\n"
"v_mfma_f32_16x16x32_bf16 v[160:163], a[244:247], a[88:91], v[160:163]\n"
"v_mfma_f32_16x16x32_bf16 v[164:167], a[244:247], a[92:95], v[164:167]\n"
"ds_read_b64_tr_b16 a[244:245], v217 offset:9224\n"
"ds_read_b64_tr_b16 a[246:247], v217 offset:9480\n"
"v_mfma_f32_16x16x32_bf16 v[168:171], a[248:251], a[84:87], v[168:171]\n"
"v_mul_f32 v8, v244, v8\n"
"v_mul_f32 v9, v244, v9\n"
"v_mfma_f32_16x16x32_bf16 v[172:175], a[248:251], a[88:91], v[172:175]\n"
"v_cvt_pk_bf16_f32 v226, v8, v9\n"
"v_mul_f32 v10, v244, v10\n"
"v_mfma_f32_16x16x32_bf16 v[176:179], a[248:251], a[92:95], v[176:179]\n"
"v_mul_f32 v11, v244, v11\n"
"v_cvt_pk_bf16_f32 v227, v10, v11\n"
"v_mfma_f32_16x16x32_bf16 v[180:183], a[252:255], a[84:87], v[180:183]\n"
"v_mul_f32 v20, v244, v20\n"
"v_mul_f32 v21, v244, v21\n"
"v_mfma_f32_16x16x32_bf16 v[184:187], a[252:255], a[88:91], v[184:187]\n"
"v_cvt_pk_bf16_f32 v228, v20, v21\n"
"v_mul_f32 v22, v244, v22\n"
"v_mfma_f32_16x16x32_bf16 v[188:191], a[252:255], a[92:95], v[188:191]\n"
"v_mul_f32 v23, v244, v23\n"
"v_cvt_pk_bf16_f32 v229, v22, v23\n"
"s_waitcnt lgkmcnt(0)\n"
"v_mfma_f32_16x16x32_bf16 v[96:99], a[192:195], a[96:99], v[96:99]\n"
"buffer_load_dwordx4 v220, s[48:51], s79 offen offset:2048 nt lds\n"
"s_xor_b32 s79, s100, 0x20a000\n"
"v_mfma_f32_16x16x32_bf16 v[100:103], a[192:195], a[100:103], v[100:103]\n"
"ds_read_b64_tr_b16 a[248:249], v217 offset:9728\n"
"ds_read_b64_tr_b16 a[250:251], v217 offset:9984\n"
"v_mfma_f32_16x16x32_bf16 v[104:107], a[192:195], a[104:107], v[104:107]\n"
"buffer_store_dwordx4 v[226:229], v223, s[56:59], s83 offen sc1\n"
"s_add_u32 s83, s82, 0x4800\n"
"v_mfma_f32_16x16x32_bf16 v[108:111], a[196:199], a[96:99], v[108:111]\n"
"ds_read_b64_tr_b16 a[252:253], v217 offset:9736\n"
"ds_read_b64_tr_b16 a[254:255], v217 offset:9992\n"
"v_mfma_f32_16x16x32_bf16 v[112:115], a[196:199], a[100:103], v[112:115]\n"
"v_mfma_f32_16x16x32_bf16 v[116:119], a[196:199], a[104:107], v[116:119]\n"
"v_mfma_f32_16x16x32_bf16 v[120:123], a[200:203], a[96:99], v[120:123]\n"
"v_mfma_f32_16x16x32_bf16 v[124:127], a[200:203], a[100:103], v[124:127]\n"
"v_mfma_f32_16x16x32_bf16 v[128:131], a[200:203], a[104:107], v[128:131]\n"
"s_waitcnt vmcnt(24) lgkmcnt(0)\n"
"s_barrier\n"
"v_mfma_f32_16x16x32_bf16 v[132:135], a[204:207], a[96:99], v[132:135]\n"
"ds_read_b64_tr_b16 a[192:193], v217 offset:32768\n"
"ds_read_b64_tr_b16 a[194:195], v217 offset:33024\n"
"v_mfma_f32_16x16x32_bf16 v[136:139], a[204:207], a[100:103], v[136:139]\n"
"ds_read_b64_tr_b16 a[196:197], v217 offset:32776\n"
"ds_read_b64_tr_b16 a[198:199], v217 offset:33032\n"
"v_mfma_f32_16x16x32_bf16 v[140:143], a[204:207], a[104:107], v[140:143]\n"
"ds_read_b64_tr_b16 a[200:201], v217 offset:33280\n"
"ds_read_b64_tr_b16 a[202:203], v217 offset:33536\n"
"v_mfma_f32_16x16x32_bf16 v[144:147], a[208:211], a[96:99], v[144:147]\n"
"ds_read_b64_tr_b16 a[204:205], v217 offset:33288\n"
"ds_read_b64_tr_b16 a[206:207], v217 offset:33544\n"
"v_mfma_f32_16x16x32_bf16 v[148:151], a[208:211], a[100:103], v[148:151]\n"
"v_mfma_f32_16x16x32_bf16 v[152:155], a[208:211], a[104:107], v[152:155]\n"
"ds_read_b64_tr_b16 a[208:209], v217 offset:33792\n"
"ds_read_b64_tr_b16 a[210:211], v217 offset:34048\n"
"v_mfma_f32_16x16x32_bf16 v[156:159], a[212:215], a[96:99], v[156:159]\n"
"v_mfma_f32_16x16x32_bf16 v[160:163], a[212:215], a[100:103], v[160:163]\n"
"v_mul_f32 v32, v244, v32\n"
"v_mul_f32 v33, v244, v33\n"
"v_mfma_f32_16x16x32_bf16 v[164:167], a[212:215], a[104:107], v[164:167]\n"
"v_cvt_pk_bf16_f32 v230, v32, v33\n"
"v_mul_f32 v34, v244, v34\n"
"v_mfma_f32_16x16x32_bf16 v[168:171], a[216:219], a[96:99], v[168:171]\n"
"v_mul_f32 v35, v244, v35\n"
"v_cvt_pk_bf16_f32 v231, v34, v35\n"
"v_mfma_f32_16x16x32_bf16 v[172:175], a[216:219], a[100:103], v[172:175]\n"
"v_mul_f32 v44, v244, v44\n"
"v_mul_f32 v45, v244, v45\n"
"v_mfma_f32_16x16x32_bf16 v[176:179], a[216:219], a[104:107], v[176:179]\n"
"v_cvt_pk_bf16_f32 v232, v44, v45\n"
"v_mul_f32 v46, v244, v46\n"
"v_mfma_f32_16x16x32_bf16 v[180:183], a[220:223], a[96:99], v[180:183]\n"
"v_mul_f32 v47, v244, v47\n"
"v_cvt_pk_bf16_f32 v233, v46, v47\n"
"s_add_u32 m0, s77, 0x10000\n"
"v_mfma_f32_16x16x32_bf16 v[184:187], a[220:223], a[100:103], v[184:187]\n"
"buffer_load_dwordx4 v220, s[44:47], s79 offen nt lds\n"
"v_mfma_f32_16x16x32_bf16 v[188:191], a[220:223], a[104:107], v[188:191]\n"
"ds_read_b64_tr_b16 a[212:213], v217 offset:33800\n"
"ds_read_b64_tr_b16 a[214:215], v217 offset:34056\n"
"v_mfma_f32_16x16x32_bf16 v[96:99], a[224:227], a[108:111], v[96:99]\n"
"buffer_store_dwordx4 v[230:233], v223, s[56:59], s83 offen sc1\n"
"s_add_u32 s83, s82, 0x8800\n"
"v_mfma_f32_16x16x32_bf16 v[100:103], a[224:227], a[112:115], v[100:103]\n"
"ds_read_b64_tr_b16 a[216:217], v217 offset:34304\n"
"ds_read_b64_tr_b16 a[218:219], v217 offset:34560\n"
"v_mfma_f32_16x16x32_bf16 v[104:107], a[224:227], a[116:119], v[104:107]\n"
"ds_read_b64_tr_b16 a[220:221], v217 offset:34312\n"
"ds_read_b64_tr_b16 a[222:223], v217 offset:34568\n"
"v_mfma_f32_16x16x32_bf16 v[108:111], a[228:231], a[108:111], v[108:111]\n"
"ds_read_b64_tr_b16 a[224:225], v217 offset:40960\n"
"ds_read_b64_tr_b16 a[226:227], v217 offset:41216\n"
"v_mfma_f32_16x16x32_bf16 v[112:115], a[228:231], a[112:115], v[112:115]\n"
"v_mfma_f32_16x16x32_bf16 v[116:119], a[228:231], a[116:119], v[116:119]\n"
"ds_read_b64_tr_b16 a[228:229], v217 offset:40968\n"
"ds_read_b64_tr_b16 a[230:231], v217 offset:41224\n"
"v_mfma_f32_16x16x32_bf16 v[120:123], a[232:235], a[108:111], v[120:123]\n"
"v_mfma_f32_16x16x32_bf16 v[124:127], a[232:235], a[112:115], v[124:127]\n"
"v_mfma_f32_16x16x32_bf16 v[128:131], a[232:235], a[116:119], v[128:131]\n"
"ds_read_b64_tr_b16 a[232:233], v217 offset:41472\n"
"ds_read_b64_tr_b16 a[234:235], v217 offset:41728\n"
"v_mfma_f32_16x16x32_bf16 v[132:135], a[236:239], a[108:111], v[132:135]\n"
"v_mfma_f32_16x16x32_bf16 v[136:139], a[236:239], a[112:115], v[136:139]\n"
"v_mfma_f32_16x16x32_bf16 v[140:143], a[236:239], a[116:119], v[140:143]\n"
"ds_read_b64_tr_b16 a[236:237], v217 offset:41480\n"
"ds_read_b64_tr_b16 a[238:239], v217 offset:41736\n"
"v_mfma_f32_16x16x32_bf16 v[144:147], a[240:243], a[108:111], v[144:147]\n"
"v_mfma_f32_16x16x32_bf16 v[148:151], a[240:243], a[112:115], v[148:151]\n"
"v_mfma_f32_16x16x32_bf16 v[152:155], a[240:243], a[116:119], v[152:155]\n"
"v_mul_f32 v56, v244, v56\n"
"v_mul_f32 v57, v244, v57\n"
"v_mfma_f32_16x16x32_bf16 v[156:159], a[244:247], a[108:111], v[156:159]\n"
"v_cvt_pk_bf16_f32 v234, v56, v57\n"
"v_mul_f32 v58, v244, v58\n"
"v_mfma_f32_16x16x32_bf16 v[160:163], a[244:247], a[112:115], v[160:163]\n"
"v_mul_f32 v59, v244, v59\n"
"v_cvt_pk_bf16_f32 v235, v58, v59\n"
"v_mfma_f32_16x16x32_bf16 v[164:167], a[244:247], a[116:119], v[164:167]\n"
"v_mul_f32 v68, v244, v68\n"
"v_mul_f32 v69, v244, v69\n"
"v_mfma_f32_16x16x32_bf16 v[168:171], a[248:251], a[108:111], v[168:171]\n"
"v_cvt_pk_bf16_f32 v236, v68, v69\n"
"v_mul_f32 v70, v244, v70\n"
"v_mfma_f32_16x16x32_bf16 v[172:175], a[248:251], a[112:115], v[172:175]\n"
"v_mul_f32 v71, v244, v71\n"
"v_cvt_pk_bf16_f32 v237, v70, v71\n"
"v_mfma_f32_16x16x32_bf16 v[176:179], a[248:251], a[116:119], v[176:179]\n"
"buffer_load_dwordx4 v220, s[44:47], s79 offen offset:2048 nt lds\n"
"ds_read_b64_tr_b16 a[240:241], v217 offset:41984\n"
"v_mfma_f32_16x16x32_bf16 v[180:183], a[252:255], a[108:111], v[180:183]\n"
"ds_read_b64_tr_b16 a[242:243], v217 offset:42240\n"
"ds_read_b64_tr_b16 a[244:245], v217 offset:41992\n"
"v_mfma_f32_16x16x32_bf16 v[184:187], a[252:255], a[112:115], v[184:187]\n"
"buffer_store_dwordx4 v[234:237], v223, s[56:59], s83 offen sc1\n"
"s_add_u32 s83, s82, 0xc800\n"
"v_mfma_f32_16x16x32_bf16 v[188:191], a[252:255], a[116:119], v[188:191]\n"
"ds_read_b64_tr_b16 a[246:247], v217 offset:42248\n"
"ds_read_b64_tr_b16 a[248:249], v217 offset:42496\n"
"s_waitcnt lgkmcnt(5)\n"
"v_mfma_f32_16x16x32_bf16 v[96:99], a[192:195], a[120:123], v[96:99]\n"
"ds_read_b64_tr_b16 a[250:251], v217 offset:42752\n"
"ds_read_b64_tr_b16 a[252:253], v217 offset:42504\n"
"v_mfma_f32_16x16x32_bf16 v[100:103], a[192:195], a[124:127], v[100:103]\n"
"ds_read_b64_tr_b16 a[254:255], v217 offset:42760\n"
"v_mfma_f32_16x16x32_bf16 v[104:107], a[192:195], a[128:131], v[104:107]\n"
"v_mfma_f32_16x16x32_bf16 v[108:111], a[196:199], a[120:123], v[108:111]\n"
"v_mfma_f32_16x16x32_bf16 v[112:115], a[196:199], a[124:127], v[112:115]\n"
"v_mfma_f32_16x16x32_bf16 v[116:119], a[196:199], a[128:131], v[116:119]\n"
"v_mfma_f32_16x16x32_bf16 v[120:123], a[200:203], a[120:123], v[120:123]\n"
"v_mfma_f32_16x16x32_bf16 v[124:127], a[200:203], a[124:127], v[124:127]\n"
"v_mfma_f32_16x16x32_bf16 v[128:131], a[200:203], a[128:131], v[128:131]\n"
"s_waitcnt vmcnt(20) lgkmcnt(0)\n"
"s_barrier\n"
"v_mfma_f32_16x16x32_bf16 v[132:135], a[204:207], a[120:123], v[132:135]\n"
"ds_read_b64_tr_b16 a[192:193], v218 offset:16384\n"
"ds_read_b64_tr_b16 a[194:195], v218 offset:16640\n"
"v_mfma_f32_16x16x32_bf16 v[136:139], a[204:207], a[124:127], v[136:139]\n"
"ds_read_b64_tr_b16 a[196:197], v218 offset:16392\n"
"ds_read_b64_tr_b16 a[198:199], v218 offset:16648\n"
"v_mfma_f32_16x16x32_bf16 v[140:143], a[204:207], a[128:131], v[140:143]\n"
"ds_read_b64_tr_b16 a[200:201], v218 offset:16896\n"
"ds_read_b64_tr_b16 a[202:203], v218 offset:17152\n"
"v_mfma_f32_16x16x32_bf16 v[144:147], a[208:211], a[120:123], v[144:147]\n"
"v_mul_f32 v80, v244, v80\n"
"v_mul_f32 v81, v244, v81\n"
"v_mfma_f32_16x16x32_bf16 v[148:151], a[208:211], a[124:127], v[148:151]\n"
"v_cvt_pk_bf16_f32 v238, v80, v81\n"
"v_mul_f32 v82, v244, v82\n"
"v_mfma_f32_16x16x32_bf16 v[152:155], a[208:211], a[128:131], v[152:155]\n"
"v_mul_f32 v83, v244, v83\n"
"v_cvt_pk_bf16_f32 v239, v82, v83\n"
"v_mfma_f32_16x16x32_bf16 v[156:159], a[212:215], a[120:123], v[156:159]\n"
"v_mul_f32 v92, v244, v92\n"
"v_mul_f32 v93, v244, v93\n"
"v_mfma_f32_16x16x32_bf16 v[160:163], a[212:215], a[124:127], v[160:163]\n"
"v_cvt_pk_bf16_f32 v240, v92, v93\n"
"v_mul_f32 v94, v244, v94\n"
"v_mfma_f32_16x16x32_bf16 v[164:167], a[212:215], a[128:131], v[164:167]\n"
"v_mul_f32 v95, v244, v95\n"
"v_cvt_pk_bf16_f32 v241, v94, v95\n"
"s_add_u32 m0, s77, 0x12000\n"
"v_mfma_f32_16x16x32_bf16 v[168:171], a[216:219], a[120:123], v[168:171]\n"
"buffer_load_dwordx4 v220, s[48:51], s79 offen nt lds\n"
"v_mfma_f32_16x16x32_bf16 v[172:175], a[216:219], a[124:127], v[172:175]\n"
"ds_read_b64_tr_b16 a[204:205], v218 offset:16904\n"
"ds_read_b64_tr_b16 a[206:207], v218 offset:17160\n"
"v_mfma_f32_16x16x32_bf16 v[176:179], a[216:219], a[128:131], v[176:179]\n"
"buffer_store_dwordx4 v[238:241], v223, s[56:59], s83 offen sc1\n"
"ds_read_b64_tr_b16 a[208:209], v218 offset:17408\n"
"v_mfma_f32_16x16x32_bf16 v[180:183], a[220:223], a[120:123], v[180:183]\n"
"ds_read_b64_tr_b16 a[210:211], v218 offset:17664\n"
"ds_read_b64_tr_b16 a[212:213], v218 offset:17416\n"
"v_mfma_f32_16x16x32_bf16 v[184:187], a[220:223], a[124:127], v[184:187]\n"
"ds_read_b64_tr_b16 a[214:215], v218 offset:17672\n"
"ds_read_b64_tr_b16 a[216:217], v218 offset:17920\n"
"v_mfma_f32_16x16x32_bf16 v[188:191], a[220:223], a[128:131], v[188:191]\n"
"ds_read_b64_tr_b16 a[218:219], v218 offset:18176\n"
"ds_read_b64_tr_b16 a[220:221], v218 offset:17928\n"
"v_mfma_f32_16x16x32_bf16 v[96:99], a[224:227], a[132:135], v[96:99]\n"
"ds_read_b64_tr_b16 a[222:223], v218 offset:18184\n"
"v_mfma_f32_16x16x32_bf16 v[100:103], a[224:227], a[136:139], v[100:103]\n"
"v_mfma_f32_16x16x32_bf16 v[104:107], a[224:227], a[140:143], v[104:107]\n"
"ds_read_b64_tr_b16 a[224:225], v218 offset:24576\n"
"ds_read_b64_tr_b16 a[226:227], v218 offset:24832\n"
"v_mfma_f32_16x16x32_bf16 v[108:111], a[228:231], a[132:135], v[108:111]\n"
"v_mfma_f32_16x16x32_bf16 v[112:115], a[228:231], a[136:139], v[112:115]\n"
"v_mfma_f32_16x16x32_bf16 v[116:119], a[228:231], a[140:143], v[116:119]\n"
"ds_read_b64_tr_b16 a[228:229], v218 offset:24584\n"
"ds_read_b64_tr_b16 a[230:231], v218 offset:24840\n"
"v_mfma_f32_16x16x32_bf16 v[120:123], a[232:235], a[132:135], v[120:123]\n"
"v_mfma_f32_16x16x32_bf16 v[124:127], a[232:235], a[136:139], v[124:127]\n"
"v_mfma_f32_16x16x32_bf16 v[128:131], a[232:235], a[140:143], v[128:131]\n"
"ds_read_b64_tr_b16 a[232:233], v218 offset:25088\n"
"ds_read_b64_tr_b16 a[234:235], v218 offset:25344\n"
"v_mfma_f32_16x16x32_bf16 v[132:135], a[236:239], a[132:135], v[132:135]\n"
"v_mfma_f32_16x16x32_bf16 v[136:139], a[236:239], a[136:139], v[136:139]\n"
"buffer_load_dwordx4 v220, s[48:51], s79 offen offset:2048 nt lds\n"
"s_xor_b32 s79, s100, 0x20c000\n"
"v_mfma_f32_16x16x32_bf16 v[140:143], a[236:239], a[140:143], v[140:143]\n"
"ds_read_b64_tr_b16 a[236:237], v218 offset:25096\n"
"ds_read_b64_tr_b16 a[238:239], v218 offset:25352\n"
"v_mfma_f32_16x16x32_bf16 v[144:147], a[240:243], a[132:135], v[144:147]\n"
"v_mfma_f32_16x16x32_bf16 v[148:151], a[240:243], a[136:139], v[148:151]\n"
"v_mfma_f32_16x16x32_bf16 v[152:155], a[240:243], a[140:143], v[152:155]\n"
"ds_read_b64_tr_b16 a[240:241], v218 offset:25600\n"
"ds_read_b64_tr_b16 a[242:243], v218 offset:25856\n"
"v_mfma_f32_16x16x32_bf16 v[156:159], a[244:247], a[132:135], v[156:159]\n"
"v_mfma_f32_16x16x32_bf16 v[160:163], a[244:247], a[136:139], v[160:163]\n"
"v_mfma_f32_16x16x32_bf16 v[164:167], a[244:247], a[140:143], v[164:167]\n"
"ds_read_b64_tr_b16 a[244:245], v218 offset:25608\n"
"ds_read_b64_tr_b16 a[246:247], v218 offset:25864\n"
"v_mfma_f32_16x16x32_bf16 v[168:171], a[248:251], a[132:135], v[168:171]\n"
"v_mfma_f32_16x16x32_bf16 v[172:175], a[248:251], a[136:139], v[172:175]\n"
"v_mfma_f32_16x16x32_bf16 v[176:179], a[248:251], a[140:143], v[176:179]\n"
"ds_read_b64_tr_b16 a[248:249], v218 offset:26112\n"
"ds_read_b64_tr_b16 a[250:251], v218 offset:26368\n"
"v_mfma_f32_16x16x32_bf16 v[180:183], a[252:255], a[132:135], v[180:183]\n"
"v_mfma_f32_16x16x32_bf16 v[184:187], a[252:255], a[136:139], v[184:187]\n"
"v_mfma_f32_16x16x32_bf16 v[188:191], a[252:255], a[140:143], v[188:191]\n"
"ds_read_b64_tr_b16 a[252:253], v218 offset:26120\n"
"ds_read_b64_tr_b16 a[254:255], v218 offset:26376\n"
"s_waitcnt lgkmcnt(4)\n"
"v_mfma_f32_16x16x32_bf16 v[96:99], a[192:195], a[144:147], v[96:99]\n"
"v_mfma_f32_16x16x32_bf16 v[100:103], a[192:195], a[148:151], v[100:103]\n"
"s_add_u32 m0, s77, 0x0\n"
"v_mfma_f32_16x16x32_bf16 v[104:107], a[192:195], a[152:155], v[104:107]\n"
"buffer_load_dwordx4 v220, s[44:47], s79 offen nt lds\n"
"v_mfma_f32_16x16x32_bf16 v[108:111], a[196:199], a[144:147], v[108:111]\n"
"v_mfma_f32_16x16x32_bf16 v[112:115], a[196:199], a[148:151], v[112:115]\n"
"v_mfma_f32_16x16x32_bf16 v[116:119], a[196:199], a[152:155], v[116:119]\n"
"v_mfma_f32_16x16x32_bf16 v[120:123], a[200:203], a[144:147], v[120:123]\n"
"v_mfma_f32_16x16x32_bf16 v[124:127], a[200:203], a[148:151], v[124:127]\n"
"v_mfma_f32_16x16x32_bf16 v[128:131], a[200:203], a[152:155], v[128:131]\n"
"s_waitcnt vmcnt(16) lgkmcnt(0)\n"
"s_barrier\n"
"v_mfma_f32_16x16x32_bf16 v[132:135], a[204:207], a[144:147], v[132:135]\n"
"ds_read_b64_tr_b16 a[192:193], v218 offset:32768\n"
"ds_read_b64_tr_b16 a[194:195], v218 offset:33024\n"
"v_mfma_f32_16x16x32_bf16 v[136:139], a[204:207], a[148:151], v[136:139]\n"
"ds_read_b64_tr_b16 a[196:197], v218 offset:32776\n"
"ds_read_b64_tr_b16 a[198:199], v218 offset:33032\n"
"v_mfma_f32_16x16x32_bf16 v[140:143], a[204:207], a[152:155], v[140:143]\n"
"ds_read_b64_tr_b16 a[200:201], v218 offset:33280\n"
"ds_read_b64_tr_b16 a[202:203], v218 offset:33536\n"
"v_mfma_f32_16x16x32_bf16 v[144:147], a[208:211], a[144:147], v[144:147]\n"
"ds_read_b64_tr_b16 a[204:205], v218 offset:33288\n"
"ds_read_b64_tr_b16 a[206:207], v218 offset:33544\n"
"v_mfma_f32_16x16x32_bf16 v[148:151], a[208:211], a[148:151], v[148:151]\n"
"v_mfma_f32_16x16x32_bf16 v[152:155], a[208:211], a[152:155], v[152:155]\n"
"ds_read_b64_tr_b16 a[208:209], v218 offset:33792\n"
"ds_read_b64_tr_b16 a[210:211], v218 offset:34048\n"
"v_mfma_f32_16x16x32_bf16 v[156:159], a[212:215], a[144:147], v[156:159]\n"
"v_mfma_f32_16x16x32_bf16 v[160:163], a[212:215], a[148:151], v[160:163]\n"
"v_mfma_f32_16x16x32_bf16 v[164:167], a[212:215], a[152:155], v[164:167]\n"
"ds_read_b64_tr_b16 a[212:213], v218 offset:33800\n"
"ds_read_b64_tr_b16 a[214:215], v218 offset:34056\n"
"v_mfma_f32_16x16x32_bf16 v[168:171], a[216:219], a[144:147], v[168:171]\n"
"buffer_load_dwordx4 v220, s[44:47], s79 offen offset:2048 nt lds\n"
"v_mfma_f32_16x16x32_bf16 v[172:175], a[216:219], a[148:151], v[172:175]\n"
"v_mfma_f32_16x16x32_bf16 v[176:179], a[216:219], a[152:155], v[176:179]\n"
"ds_read_b64_tr_b16 a[216:217], v218 offset:34304\n"
"ds_read_b64_tr_b16 a[218:219], v218 offset:34560\n"
"v_mfma_f32_16x16x32_bf16 v[180:183], a[220:223], a[144:147], v[180:183]\n"
"v_mfma_f32_16x16x32_bf16 v[184:187], a[220:223], a[148:151], v[184:187]\n"
"v_mfma_f32_16x16x32_bf16 v[188:191], a[220:223], a[152:155], v[188:191]\n"
"ds_read_b64_tr_b16 a[220:221], v218 offset:34312\n"
"ds_read_b64_tr_b16 a[222:223], v218 offset:34568\n"
"v_mfma_f32_16x16x32_bf16 v[96:99], a[224:227], a[156:159], v[96:99]\n"
"v_mfma_f32_16x16x32_bf16 v[100:103], a[224:227], a[160:163], v[100:103]\n"
"v_mfma_f32_16x16x32_bf16 v[104:107], a[224:227], a[164:167], v[104:107]\n"
"ds_read_b64_tr_b16 a[224:225], v218 offset:40960\n"
"ds_read_b64_tr_b16 a[226:227], v218 offset:41216\n"
"v_mfma_f32_16x16x32_bf16 v[108:111], a[228:231], a[156:159], v[108:111]\n"
"v_mfma_f32_16x16x32_bf16 v[112:115], a[228:231], a[160:163], v[112:115]\n"
"v_mfma_f32_16x16x32_bf16 v[116:119], a[228:231], a[164:167], v[116:119]\n"
"ds_read_b64_tr_b16 a[228:229], v218 offset:40968\n"
"ds_read_b64_tr_b16 a[230:231], v218 offset:41224\n"
"v_mfma_f32_16x16x32_bf16 v[120:123], a[232:235], a[156:159], v[120:123]\n"
"v_mfma_f32_16x16x32_bf16 v[124:127], a[232:235], a[160:163], v[124:127]\n"
"v_mfma_f32_16x16x32_bf16 v[128:131], a[232:235], a[164:167], v[128:131]\n"
"ds_read_b64_tr_b16 a[232:233], v218 offset:41472\n"
"ds_read_b64_tr_b16 a[234:235], v218 offset:41728\n"
"v_mfma_f32_16x16x32_bf16 v[132:135], a[236:239], a[156:159], v[132:135]\n"
"s_add_u32 m0, s77, 0x2000\n"
"v_mfma_f32_16x16x32_bf16 v[136:139], a[236:239], a[160:163], v[136:139]\n"
"buffer_load_dwordx4 v220, s[48:51], s79 offen nt lds\n"
"v_mfma_f32_16x16x32_bf16 v[140:143], a[236:239], a[164:167], v[140:143]\n"
"ds_read_b64_tr_b16 a[236:237], v218 offset:41480\n"
"ds_read_b64_tr_b16 a[238:239], v218 offset:41736\n"
"v_mfma_f32_16x16x32_bf16 v[144:147], a[240:243], a[156:159], v[144:147]\n"
"v_mfma_f32_16x16x32_bf16 v[148:151], a[240:243], a[160:163], v[148:151]\n"
"v_mfma_f32_16x16x32_bf16 v[152:155], a[240:243], a[164:167], v[152:155]\n"
"ds_read_b64_tr_b16 a[240:241], v218 offset:41984\n"
"ds_read_b64_tr_b16 a[242:243], v218 offset:42240\n"
"v_mfma_f32_16x16x32_bf16 v[156:159], a[244:247], a[156:159], v[156:159]\n"
"v_mfma_f32_16x16x32_bf16 v[160:163], a[244:247], a[160:163], v[160:163]\n"
"v_mfma_f32_16x16x32_bf16 v[164:167], a[244:247], a[164:167], v[164:167]\n"
"ds_read_b64_tr_b16 a[244:245], v218 offset:41992\n"
"ds_read_b64_tr_b16 a[246:247], v218 offset:42248\n"
"v_mfma_f32_16x16x32_bf16 v[168:171], a[248:251], a[156:159], v[168:171]\n"
"v_mfma_f32_16x16x32_bf16 v[172:175], a[248:251], a[160:163], v[172:175]\n"
"v_mfma_f32_16x16x32_bf16 v[176:179], a[248:251], a[164:167], v[176:179]\n"
"ds_read_b64_tr_b16 a[248:249], v218 offset:42496\n"
"ds_read_b64_tr_b16 a[250:251], v218 offset:42752\n"
"v_mfma_f32_16x16x32_bf16 v[180:183], a[252:255], a[156:159], v[180:183]\n"
"v_mfma_f32_16x16x32_bf16 v[184:187], a[252:255], a[160:163], v[184:187]\n"
"v_mfma_f32_16x16x32_bf16 v[188:191], a[252:255], a[164:167], v[188:191]\n"
"ds_read_b64_tr_b16 a[252:253], v218 offset:42504\n"
"ds_read_b64_tr_b16 a[254:255], v218 offset:42760\n"
"s_waitcnt lgkmcnt(4)\n"
"v_mfma_f32_16x16x32_bf16 v[96:99], a[192:195], a[168:171], v[96:99]\n"
"v_mfma_f32_16x16x32_bf16 v[96:99], a[224:227], a[180:183], v[96:99]\n"
"v_mfma_f32_16x16x32_bf16 v[100:103], a[192:195], a[172:175], v[100:103]\n"
"buffer_load_dwordx4 v220, s[48:51], s79 offen offset:2048 nt lds\n"
"s_xor_b32 s79, s100, 0x20e000\n"
"v_mfma_f32_16x16x32_bf16 v[100:103], a[224:227], a[184:187], v[100:103]\n"
"v_mfma_f32_16x16x32_bf16 v[104:107], a[192:195], a[176:179], v[104:107]\n"
"v_mfma_f32_16x16x32_bf16 v[104:107], a[224:227], a[188:191], v[104:107]\n"
"v_mfma_f32_16x16x32_bf16 v[108:111], a[196:199], a[168:171], v[108:111]\n"
"s_nop 0\n"
"v_mul_f32 v96, s85, v96\n"
"v_mul_f32 v97, s85, v97\n"
"v_mfma_f32_16x16x32_bf16 v[108:111], a[228:231], a[180:183], v[108:111]\n"
"v_mul_f32 v98, s85, v98\n"
"v_mul_f32 v99, s85, v99\n"
"v_mfma_f32_16x16x32_bf16 v[112:115], a[196:199], a[172:175], v[112:115]\n"
"s_waitcnt lgkmcnt(0)\n"
"s_barrier\n"
"v_mfma_f32_16x16x32_bf16 v[112:115], a[228:231], a[184:187], v[112:115]\n"
"ds_read_b128 a[192:195], v208 offset:49152\n"
"v_cvt_pk_bf16_f32 v0, v96, v97\n"
"v_mfma_f32_16x16x32_bf16 v[116:119], a[196:199], a[176:179], v[116:119]\n"
"ds_read_b128 a[196:199], v208 offset:49664\n"
"v_cvt_pk_bf16_f32 v1, v98, v99\n"
"v_mfma_f32_16x16x32_bf16 v[116:119], a[228:231], a[188:191], v[116:119]\n"
"v_mul_f32 v100, s85, v100\n"
"v_mul_f32 v101, s85, v101\n"
"v_mfma_f32_16x16x32_bf16 v[120:123], a[200:203], a[168:171], v[120:123]\n"
"v_mul_f32 v108, s85, v108\n"
"v_mul_f32 v109, s85, v109\n"
"v_mfma_f32_16x16x32_bf16 v[120:123], a[232:235], a[180:183], v[120:123]\n"
"v_mul_f32 v110, s85, v110\n"
"v_mul_f32 v111, s85, v111\n"
"v_mfma_f32_16x16x32_bf16 v[124:127], a[200:203], a[172:175], v[124:127]\n"
"v_cvt_pk_bf16_f32 v2, v108, v109\n"
"v_cvt_pk_bf16_f32 v3, v110, v111\n"
"v_mfma_f32_16x16x32_bf16 v[124:127], a[232:235], a[184:187], v[124:127]\n"
"v_mul_f32 v102, s85, v102\n"
"v_mul_f32 v103, s85, v103\n"
"s_add_u32 m0, s77, 0x8000\n"
"v_mfma_f32_16x16x32_bf16 v[128:131], a[200:203], a[176:179], v[128:131]\n"
"buffer_load_dwordx4 v220, s[44:47], s79 offen nt lds\n"
"v_mfma_f32_16x16x32_bf16 v[128:131], a[232:235], a[188:191], v[128:131]\n"
"ds_read_b128 a[200:203], v208 offset:50176\n"
"v_cvt_pk_bf16_f32 v16, v100, v101\n"
"v_mfma_f32_16x16x32_bf16 v[132:135], a[204:207], a[168:171], v[132:135]\n"
"v_mul_f32 v120, s85, v120\n"
"v_mul_f32 v121, s85, v121\n"
"v_mfma_f32_16x16x32_bf16 v[132:135], a[236:239], a[180:183], v[132:135]\n"
"v_mul_f32 v122, s85, v122\n"
"v_mul_f32 v123, s85, v123\n"
"v_mfma_f32_16x16x32_bf16 v[136:139], a[204:207], a[172:175], v[136:139]\n"
"v_cvt_pk_bf16_f32 v4, v120, v121\n"
"v_cvt_pk_bf16_f32 v5, v122, v123\n"
"v_mfma_f32_16x16x32_bf16 v[136:139], a[236:239], a[184:187], v[136:139]\n"
"v_cvt_pk_bf16_f32 v17, v102, v103\n"
"v_mul_f32 v112, s85, v112\n"
"v_mfma_f32_16x16x32_bf16 v[140:143], a[204:207], a[176:179], v[140:143]\n"
"ds_read_b128 a[204:207], v208 offset:50688\n"
"v_mul_f32 v113, s85, v113\n"
"v_mfma_f32_16x16x32_bf16 v[140:143], a[236:239], a[188:191], v[140:143]\n"
"v_mul_f32 v114, s85, v114\n"
"v_mul_f32 v115, s85, v115\n"
"v_mfma_f32_16x16x32_bf16 v[144:147], a[208:211], a[168:171], v[144:147]\n"
"v_mul_f32 v132, s85, v132\n"
"v_mul_f32 v133, s85, v133\n"
"v_mfma_f32_16x16x32_bf16 v[144:147], a[240:243], a[180:183], v[144:147]\n"
"v_mul_f32 v134, s85, v134\n"
"v_mul_f32 v135, s85, v135\n"
"v_mfma_f32_16x16x32_bf16 v[148:151], a[208:211], a[172:175], v[148:151]\n"
"v_cvt_pk_bf16_f32 v6, v132, v133\n"
"v_cvt_pk_bf16_f32 v7, v134, v135\n"
"v_mfma_f32_16x16x32_bf16 v[148:151], a[240:243], a[184:187], v[148:151]\n"
"v_cvt_pk_bf16_f32 v18, v112, v113\n"
"v_cvt_pk_bf16_f32 v19, v114, v115\n"
"v_mfma_f32_16x16x32_bf16 v[152:155], a[208:211], a[176:179], v[152:155]\n"
"ds_read_b128 a[208:211], v208 offset:53248\n"
"v_mul_f32 v124, s85, v124\n"
"v_mfma_f32_16x16x32_bf16 v[152:155], a[240:243], a[188:191], v[152:155]\n"
"v_mul_f32 v125, s85, v125\n"
"v_mul_f32 v126, s85, v126\n"
"v_mfma_f32_16x16x32_bf16 v[156:159], a[212:215], a[168:171], v[156:159]\n"
"buffer_load_dwordx4 v220, s[44:47], s79 offen offset:2048 nt lds\n"
"v_mul_f32 v144, s85, v144\n"
"v_mfma_f32_16x16x32_bf16 v[156:159], a[244:247], a[180:183], v[156:159]\n"
"v_mul_f32 v145, s85, v145\n"
"v_mul_f32 v146, s85, v146\n"
"v_mfma_f32_16x16x32_bf16 v[160:163], a[212:215], a[172:175], v[160:163]\n"
"v_mul_f32 v147, s85, v147\n"
"v_cvt_pk_bf16_f32 v8, v144, v145\n"
"v_mfma_f32_16x16x32_bf16 v[160:163], a[244:247], a[184:187], v[160:163]\n"
"v_cvt_pk_bf16_f32 v9, v146, v147\n"
"v_mul_f32 v127, s85, v127\n"
"v_mfma_f32_16x16x32_bf16 v[164:167], a[212:215], a[176:179], v[164:167]\n"
"ds_read_b128 a[212:215], v208 offset:53760\n"
"v_cvt_pk_bf16_f32 v20, v124, v125\n"
"v_mfma_f32_16x16x32_bf16 v[164:167], a[244:247], a[188:191], v[164:167]\n"
"v_cvt_pk_bf16_f32 v21, v126, v127\n"
"v_mul_f32 v136, s85, v136\n"
"v_mfma_f32_16x16x32_bf16 v[168:171], a[216:219], a[168:171], v[168:171]\n"
"v_mul_f32 v156, s85, v156\n"
"v_mul_f32 v157, s85, v157\n"
"v_mfma_f32_16x16x32_bf16 v[168:171], a[248:251], a[180:183], v[168:171]\n"
"v_mul_f32 v158, s85, v158\n"
"v_mul_f32 v159, s85, v159\n"
"v_mfma_f32_16x16x32_bf16 v[172:175], a[216:219], a[172:175], v[172:175]\n"
"v_cvt_pk_bf16_f32 v10, v156, v157\n"
"v_cvt_pk_bf16_f32 v11, v158, v159\n"
"v_mfma_f32_16x16x32_bf16 v[172:175], a[248:251], a[184:187], v[172:175]\n"
"v_mul_f32 v137, s85, v137\n"
"v_mul_f32 v138, s85, v138\n"
"v_mfma_f32_16x16x32_bf16 v[176:179], a[216:219], a[176:179], v[176:179]\n"
"ds_read_b128 a[216:219], v208 offset:54272\n"
"v_mul_f32 v139, s85, v139\n"
"v_mfma_f32_16x16x32_bf16 v[176:179], a[248:251], a[188:191], v[176:179]\n"
"v_cvt_pk_bf16_f32 v22, v136, v137\n"
"v_cvt_pk_bf16_f32 v23, v138, v139\n"
"v_mfma_f32_16x16x32_bf16 v[180:183], a[220:223], a[168:171], v[180:183]\n"
"v_mul_f32 v168, s85, v168\n"
"v_mul_f32 v169, s85, v169\n"
"v_mfma_f32_16x16x32_bf16 v[180:183], a[252:255], a[180:183], v[180:183]\n"
"v_mul_f32 v170, s85, v170\n"
"v_mul_f32 v171, s85, v171\n"
"v_mfma_f32_16x16x32_bf16 v[184:187], a[220:223], a[172:175], v[184:187]\n"
"v_cvt_pk_bf16_f32 v12, v168, v169\n"
"v_cvt_pk_bf16_f32 v13, v170, v171\n"
"s_add_u32 m0, s77, 0xa000\n"
"v_mfma_f32_16x16x32_bf16 v[184:187], a[252:255], a[184:187], v[184:187]\n"
"buffer_load_dwordx4 v220, s[48:51], s79 offen nt lds\n"
"v_mfma_f32_16x16x32_bf16 v[188:191], a[220:223], a[176:179], v[188:191]\n"
"ds_read_b128 a[220:223], v208 offset:54784\n"
"v_mul_f32 v148, s85, v148\n"
"v_mfma_f32_16x16x32_bf16 v[188:191], a[252:255], a[188:191], v[188:191]\n"
"v_mul_f32 v149, s85, v149\n"
"v_mul_f32 v150, s85, v150\n"
"s_waitcnt lgkmcnt(1)\n"
"v_mfma_f32_16x16x32_bf16 v[48:51], a[192:195], v[0:3], 0\n"
"v_mul_f32 v180, s85, v180\n"
"v_mul_f32 v181, s85, v181\n"
"v_mfma_f32_16x16x32_bf16 v[48:51], a[196:199], v[4:7], v[48:51]\n"
"v_mul_f32 v182, s85, v182\n"
"v_mul_f32 v183, s85, v183\n"
"v_cvt_pk_bf16_f32 v14, v180, v181\n"
"v_mfma_f32_16x16x32_bf16 v[48:51], a[200:203], v[8:11], v[48:51]\n"
"v_cvt_pk_bf16_f32 v15, v182, v183\n"
"v_mul_f32 v151, s85, v151\n"
"s_nop 0\n"
"v_mfma_f32_16x16x32_bf16 v[48:51], a[204:207], v[12:15], v[48:51]\n"
"v_cvt_pk_bf16_f32 v24, v148, v149\n"
"v_cvt_pk_bf16_f32 v25, v150, v151\n"
"v_mfma_f32_16x16x32_bf16 v[52:55], a[208:211], v[0:3], 0\n"
"v_mul_f32 v160, s85, v160\n"
"v_mul_f32 v161, s85, v161\n"
"v_mfma_f32_16x16x32_bf16 v[52:55], a[212:215], v[4:7], v[52:55]\n"
"v_mul_f32 v162, s85, v162\n"
"v_mul_f32 v163, s85, v163\n"
"v_mfma_f32_16x16x32_bf16 v[52:55], a[216:219], v[8:11], v[52:55]\n"
"v_cvt_pk_bf16_f32 v26, v160, v161\n"
"v_cvt_pk_bf16_f32 v27, v162, v163\n"
"s_waitcnt lgkmcnt(0)\n"
"v_mfma_f32_16x16x32_bf16 v[52:55], a[220:223], v[12:15], v[52:55]\n"
"ds_read_b64_tr_b16 a[224:225], v211 offset:49152\n"
"ds_read_b64_tr_b16 a[226:227], v211 offset:53248\n"
"v_mfma_f32_16x16x32_bf16 v[56:59], a[192:195], v[16:19], 0\n"
"v_mul_f32 v172, s85, v172\n"
"v_mul_f32 v173, s85, v173\n"
"v_mfma_f32_16x16x32_bf16 v[56:59], a[196:199], v[20:23], v[56:59]\n"
"v_mul_f32 v174, s85, v174\n"
"v_mul_f32 v175, s85, v175\n"
"v_cvt_pk_bf16_f32 v28, v172, v173\n"
"v_mfma_f32_16x16x32_bf16 v[56:59], a[200:203], v[24:27], v[56:59]\n"
"v_cvt_pk_bf16_f32 v29, v174, v175\n"
"v_mul_f32 v184, s85, v184\n"
"v_mul_f32 v185, s85, v185\n"
"v_mul_f32 v186, s85, v186\n"
"v_mul_f32 v187, s85, v187\n"
"v_cvt_pk_bf16_f32 v30, v184, v185\n"
"v_cvt_pk_bf16_f32 v31, v186, v187\n"
"s_nop 1\n"
"v_mfma_f32_16x16x32_bf16 v[56:59], a[204:207], v[28:31], v[56:59]\n"
"ds_read_b64_tr_b16 a[228:229], v214 offset:49152\n"
"ds_read_b64_tr_b16 a[230:231], v214 offset:53248\n"
"ds_read_b64_tr_b16 a[232:233], v211 offset:49664\n"
"v_mfma_f32_16x16x32_bf16 v[60:63], a[208:211], v[16:19], 0\n"
"ds_read_b64_tr_b16 a[234:235], v211 offset:53760\n"
"v_max3_f32 v245, v48, v49, v50\n"
"v_max3_f32 v246, v51, v52, v53\n"
"v_mfma_f32_16x16x32_bf16 v[60:63], a[212:215], v[20:23], v[60:63]\n"
"v_max3_f32 v245, v245, v54, v55\n"
"v_max_f32 v245, v245, v246\n"
"v_mov_b32 v246, v245\n"
"v_mfma_f32_16x16x32_bf16 v[60:63], a[216:219], v[24:27], v[60:63]\n"
"ds_read_b64_tr_b16 a[236:237], v214 offset:49664\n"
"ds_read_b64_tr_b16 a[238:239], v214 offset:53760\n"
"v_permlane32_swap_b32 v245, v246\n"
"v_mfma_f32_16x16x32_bf16 v[60:63], a[220:223], v[28:31], v[60:63]\n"
"v_max_f32 v245, v245, v246\n"
"v_mov_b32 v246, v245\n"
"ds_read_b64_tr_b16 a[240:241], v211 offset:50176\n"
"s_nop 0\n"
"v_permlane16_swap_b32 v245, v246\n"
"v_min_f32_e64 v64, -v245, -v246\n"
"v_add_f32 v48, v64, v48\n"
"v_add_f32 v49, v64, v49\n"
"v_add_f32 v50, v64, v50\n"
"v_add_f32 v51, v64, v51\n"
"v_add_f32 v52, v64, v52\n"
"v_add_f32 v53, v64, v53\n"
"v_add_f32 v54, v64, v54\n"
"v_add_f32 v55, v64, v55\n"
"v_exp_f32 v48, v48\n"
"v_exp_f32 v49, v49\n"
"v_exp_f32 v50, v50\n"
"v_exp_f32 v51, v51\n"
"v_exp_f32 v52, v52\n"
"v_exp_f32 v53, v53\n"
"v_exp_f32 v54, v54\n"
"v_exp_f32 v55, v55\n"
"v_cvt_pk_bf16_f32 v48, v48, v49\n"
"v_cvt_pk_bf16_f32 v49, v50, v51\n"
"v_cvt_pk_bf16_f32 v50, v52, v53\n"
"v_cvt_pk_bf16_f32 v51, v54, v55\n"
"s_nop 1\n"
"v_mfma_f32_16x16x32_bf16 v[192:195], v[204:207], v[48:51], 0\n"
"ds_read_b64_tr_b16 a[242:243], v211 offset:54272\n"
"ds_read_b64_tr_b16 a[244:245], v214 offset:50176\n"
"ds_read_b64_tr_b16 a[246:247], v214 offset:54272\n"
"s_waitcnt lgkmcnt(6)\n"
"v_mfma_f32_16x16x32_bf16 v[96:99], a[224:227], v[48:51], 0\n"
"ds_read_b64_tr_b16 a[248:249], v211 offset:50688\n"
"ds_read_b64_tr_b16 a[250:251], v211 offset:54784\n"
"ds_read_b64_tr_b16 a[252:253], v214 offset:50688\n"
"v_mfma_f32_16x16x32_bf16 v[108:111], a[228:231], v[48:51], 0\n"
"ds_read_b64_tr_b16 a[254:255], v214 offset:54784\n"
"buffer_load_dwordx4 v220, s[48:51], s79 offen offset:2048 nt lds\n"
"v_mfma_f32_16x16x32_bf16 v[120:123], a[232:235], v[48:51], 0\n"
"s_xor_b32 s79, s100, 0x210000\n"
"v_mul_f32 v104, s85, v104\n"
"s_waitcnt lgkmcnt(7)\n"
"v_mfma_f32_16x16x32_bf16 v[132:135], a[236:239], v[48:51], 0\n"
"v_mul_f32 v105, s85, v105\n"
"v_mul_f32 v106, s85, v106\n"
"s_waitcnt lgkmcnt(4)\n"
"v_mfma_f32_16x16x32_bf16 v[144:147], a[240:243], v[48:51], 0\n"
"v_mul_f32 v107, s85, v107\n"
"v_cvt_pk_bf16_f32 v32, v104, v105\n"
"v_mfma_f32_16x16x32_bf16 v[156:159], a[244:247], v[48:51], 0\n"
"v_cvt_pk_bf16_f32 v33, v106, v107\n"
"v_mul_f32 v116, s85, v116\n"
"s_waitcnt lgkmcnt(0)\n"
"v_mfma_f32_16x16x32_bf16 v[168:171], a[248:251], v[48:51], 0\n"
"v_mul_f32 v117, s85, v117\n"
"v_mul_f32 v118, s85, v118\n"
"v_mul_f32 v119, s85, v119\n"
"v_mfma_f32_16x16x32_bf16 v[180:183], a[252:255], v[48:51], 0\n"
"v_cvt_pk_bf16_f32 v34, v116, v117\n"
"v_cvt_pk_bf16_f32 v35, v118, v119\n"
"v_mul_f32 v128, s85, v128\n"
"s_barrier\n"
"v_mfma_f32_16x16x32_bf16 v[48:51], a[192:195], v[32:35], 0\n"
"v_mul_f32 v129, s85, v129\n"
"v_mul_f32 v130, s85, v130\n"
"v_mul_f32 v131, s85, v131\n"
"v_cvt_pk_bf16_f32 v36, v128, v129\n"
"v_cvt_pk_bf16_f32 v37, v130, v131\n"
"v_mul_f32 v140, s85, v140\n"
"v_mul_f32 v141, s85, v141\n"
"v_mul_f32 v142, s85, v142\n"
"v_mul_f32 v143, s85, v143\n"
"v_cvt_pk_bf16_f32 v38, v140, v141\n"
"v_cvt_pk_bf16_f32 v39, v142, v143\n"
"s_nop 1\n"
"v_mfma_f32_16x16x32_bf16 v[48:51], a[196:199], v[36:39], v[48:51]\n"
"v_mul_f32 v152, s85, v152\n"
"v_mul_f32 v153, s85, v153\n"
"v_mul_f32 v154, s85, v154\n"
"v_mul_f32 v155, s85, v155\n"
"v_cvt_pk_bf16_f32 v40, v152, v153\n"
"v_cvt_pk_bf16_f32 v41, v154, v155\n"
"v_mul_f32 v164, s85, v164\n"
"v_mul_f32 v165, s85, v165\n"
"v_mul_f32 v166, s85, v166\n"
"v_mul_f32 v167, s85, v167\n"
"v_cvt_pk_bf16_f32 v42, v164, v165\n"
"v_cvt_pk_bf16_f32 v43, v166, v167\n"
"s_nop 1\n"
"v_mfma_f32_16x16x32_bf16 v[48:51], a[200:203], v[40:43], v[48:51]\n"
"v_mul_f32 v176, s85, v176\n"
"v_mul_f32 v177, s85, v177\n"
"v_mul_f32 v178, s85, v178\n"
"v_mul_f32 v179, s85, v179\n"
"v_cvt_pk_bf16_f32 v44, v176, v177\n"
"v_cvt_pk_bf16_f32 v45, v178, v179\n"
"v_mul_f32 v188, s85, v188\n"
"v_mul_f32 v189, s85, v189\n"
"v_mul_f32 v190, s85, v190\n"
"v_mul_f32 v191, s85, v191\n"
"v_cvt_pk_bf16_f32 v46, v188, v189\n"
"v_cvt_pk_bf16_f32 v47, v190, v191\n"
"s_nop 1\n"
"v_mfma_f32_16x16x32_bf16 v[48:51], a[204:207], v[44:47], v[48:51]\n"
"v_max3_f32 v247, v56, v57, v58\n"
"v_max3_f32 v248, v59, v60, v61\n"
"v_max3_f32 v247, v247, v62, v63\n"
"v_mfma_f32_16x16x32_bf16 v[52:55], a[208:211], v[32:35], 0\n"
"v_max_f32 v247, v247, v248\n"
"v_mov_b32 v248, v247\n"
"v_mfma_f32_16x16x32_bf16 v[52:55], a[212:215], v[36:39], v[52:55]\n"
"s_nop 0\n"
"v_permlane32_swap_b32 v247, v248\n"
"v_max_f32 v247, v247, v248\n"
"v_mov_b32 v248, v247\n"
"v_mfma_f32_16x16x32_bf16 v[52:55], a[216:219], v[40:43], v[52:55]\n"
"s_nop 0\n"
"v_permlane16_swap_b32 v247, v248\n"
"v_min_f32_e64 v68, -v247, -v248\n"
"v_add_f32 v56, v68, v56\n"
"v_mfma_f32_16x16x32_bf16 v[52:55], a[220:223], v[44:47], v[52:55]\n"
"v_add_f32 v57, v68, v57\n"
"v_add_f32 v58, v68, v58\n"
"v_add_f32 v59, v68, v59\n"
"v_add_f32 v60, v68, v60\n"
"v_add_f32 v61, v68, v61\n"
"v_add_f32 v62, v68, v62\n"
"v_add_f32 v63, v68, v63\n"
"v_exp_f32 v56, v56\n"
"v_exp_f32 v57, v57\n"
"v_exp_f32 v58, v58\n"
"v_exp_f32 v59, v59\n"
"v_exp_f32 v60, v60\n"
"v_exp_f32 v61, v61\n"
"v_exp_f32 v62, v62\n"
"v_exp_f32 v63, v63\n"
"v_cvt_pk_bf16_f32 v56, v56, v57\n"
"v_cvt_pk_bf16_f32 v57, v58, v59\n"
"v_cvt_pk_bf16_f32 v58, v60, v61\n"
"v_cvt_pk_bf16_f32 v59, v62, v63\n"
"s_nop 1\n"
"v_mfma_f32_16x16x32_bf16 v[196:199], v[204:207], v[56:59], 0\n"
"ds_read_b128 a[192:195], v209 offset:49152\n"
"ds_read_b128 a[196:199], v209 offset:49664\n"
"v_mfma_f32_16x16x32_bf16 v[100:103], a[224:227], v[56:59], 0\n"
"ds_read_b128 a[200:203], v209 offset:50176\n"
"ds_read_b128 a[204:207], v209 offset:50688\n"
"v_mfma_f32_16x16x32_bf16 v[112:115], a[228:231], v[56:59], 0\n"
"ds_read_b128 a[208:211], v209 offset:53248\n"
"ds_read_b128 a[212:215], v209 offset:53760\n"
"v_mfma_f32_16x16x32_bf16 v[124:127], a[232:235], v[56:59], 0\n"
"ds_read_b128 a[216:219], v209 offset:54272\n"
"v_mov_b32 v65, v64\n"
"v_mfma_f32_16x16x32_bf16 v[136:139], a[236:239], v[56:59], 0\n"
"v_mov_b32 v66, v64\n"
"v_mov_b32 v67, v64\n"
"v_mfma_f32_16x16x32_bf16 v[148:151], a[240:243], v[56:59], 0\n"
"ds_read_b128 a[220:223], v209 offset:54784\n"
"v_max3_f32 v249, v48, v49, v50\n"
"s_add_u32 m0, s77, 0x14000\n"
"v_mfma_f32_16x16x32_bf16 v[160:163], a[244:247], v[56:59], 0\n"
"buffer_load_dwordx4 v220, s[44:47], s79 offen nt lds\n"
"v_mfma_f32_16x16x32_bf16 v[172:175], a[248:251], v[56:59], 0\n"
"v_max3_f32 v250, v51, v52, v53\n"
"v_max3_f32 v249, v249, v54, v55\n"
"v_mfma_f32_16x16x32_bf16 v[184:187], a[252:255], v[56:59], 0\n"
"v_max_f32 v249, v249, v250\n"
"v_mov_b32 v250, v249\n"
"s_waitcnt lgkmcnt(1)\n"
"v_mfma_f32_16x16x32_bf16 v[56:59], a[192:195], v[0:3], v[64:67]\n"
"v_permlane32_swap_b32 v249, v250\n"
"v_max_f32 v249, v249, v250\n"
"v_mfma_f32_16x16x32_bf16 v[56:59], a[196:199], v[4:7], v[56:59]\n"
"v_mov_b32 v250, v249\n"
"s_lshl2_add_u32 s37, s30, s31\n"
"v_mfma_f32_16x16x32_bf16 v[56:59], a[200:203], v[8:11], v[56:59]\n"
"s_lshl_b32 s36, s84, 7\n"
"s_add_u32 s38, s76, s30\n"
"v_mfma_f32_16x16x32_bf16 v[56:59], a[204:207], v[12:15], v[56:59]\n"
"v_add_u32 v217, 0x0, v223\n"
"v_add_u32 v218, 0x10000, v223\n"
"v_mfma_f32_16x16x32_bf16 v[60:63], a[208:211], v[0:3], v[64:67]\n"
"v_add_u32 v219, 0x20000, v223\n"
"v_add_u32 v222, 0x1000, v223\n"
"v_mfma_f32_16x16x32_bf16 v[60:63], a[212:215], v[4:7], v[60:63]\n"
"v_permlane16_swap_b32 v249, v250\n"
"v_min_f32_e64 v72, -v249, -v250\n"
"v_add_f32 v48, v72, v48\n"
"v_mfma_f32_16x16x32_bf16 v[60:63], a[216:219], v[8:11], v[60:63]\n"
"v_add_f32 v49, v72, v49\n"
"v_add_f32 v50, v72, v50\n"
"v_add_f32 v51, v72, v51\n"
"s_waitcnt lgkmcnt(0)\n"
"v_mfma_f32_16x16x32_bf16 v[60:63], a[220:223], v[12:15], v[60:63]\n"
"v_add_f32 v52, v72, v52\n"
"v_add_f32 v53, v72, v53\n"
"v_add_f32 v54, v72, v54\n"
"v_add_f32 v55, v72, v55\n"
"v_exp_f32 v48, v48\n"
"v_exp_f32 v49, v49\n"
"v_exp_f32 v50, v50\n"
"v_exp_f32 v51, v51\n"
"v_exp_f32 v52, v52\n"
"v_exp_f32 v53, v53\n"
"v_exp_f32 v54, v54\n"
"v_exp_f32 v55, v55\n"
"v_cvt_pk_bf16_f32 v48, v48, v49\n"
"v_cvt_pk_bf16_f32 v49, v50, v51\n"
"v_cvt_pk_bf16_f32 v50, v52, v53\n"
"v_cvt_pk_bf16_f32 v51, v54, v55\n"
"s_nop 1\n"
"v_mfma_f32_16x16x32_bf16 v[200:203], v[204:207], v[48:51], 0\n"
"v_mov_b32 v69, v68\n"
"v_mov_b32 v70, v68\n"
"v_mfma_f32_16x16x32_bf16 v[104:107], a[224:227], v[48:51], 0\n"
"v_mov_b32 v71, v68\n"
"ds_read_b64_tr_b16 a[224:225], v212 offset:49152\n"
"v_mfma_f32_16x16x32_bf16 v[116:119], a[228:231], v[48:51], 0\n"
"ds_read_b64_tr_b16 a[226:227], v212 offset:53248\n"
"ds_read_b64_tr_b16 a[228:229], v215 offset:49152\n"
"v_mfma_f32_16x16x32_bf16 v[128:131], a[232:235], v[48:51], 0\n"
"ds_read_b64_tr_b16 a[230:231], v215 offset:53248\n"
"ds_read_b64_tr_b16 a[232:233], v212 offset:49664\n"
"v_mfma_f32_16x16x32_bf16 v[140:143], a[236:239], v[48:51], 0\n"
"ds_read_b64_tr_b16 a[234:235], v212 offset:53760\n"
"ds_read_b64_tr_b16 v[76:77], v215 offset:49664\n"
"v_mfma_f32_16x16x32_bf16 v[152:155], a[240:243], v[48:51], 0\n"
"ds_read_b64_tr_b16 v[78:79], v215 offset:53760\n"
"s_lshl_b32 s37, s37, 2\n"
"v_mfma_f32_16x16x32_bf16 v[164:167], a[244:247], v[48:51], 0\n"
"ds_read_b64_tr_b16 v[80:81], v212 offset:50176\n"
"ds_read_b64_tr_b16 v[82:83], v212 offset:54272\n"
"v_mfma_f32_16x16x32_bf16 v[176:179], a[248:251], v[48:51], 0\n"
"s_and_b32 s38, s38, 3\n"
"ds_read_b64_tr_b16 v[84:85], v215 offset:50176\n"
"v_mfma_f32_16x16x32_bf16 v[188:191], a[252:255], v[48:51], 0\n"
"ds_read_b64_tr_b16 v[86:87], v215 offset:54272\n"
"buffer_load_dwordx4 v220, s[44:47], s79 offen offset:2048 nt lds\n"
"v_mfma_f32_16x16x32_bf16 v[48:51], a[192:195], v[16:19], v[68:71]\n"
"v_exp_f32 v56, v56\n"
"v_mfma_f32_16x16x32_bf16 v[48:51], a[196:199], v[20:23], v[48:51]\n"
"v_exp_f32 v57, v57\n"
"v_mfma_f32_16x16x32_bf16 v[48:51], a[200:203], v[24:27], v[48:51]\n"
"v_exp_f32 v58, v58\n"
"v_mfma_f32_16x16x32_bf16 v[48:51], a[204:207], v[28:31], v[48:51]\n"
"v_exp_f32 v59, v59\n"
"v_mfma_f32_16x16x32_bf16 v[52:55], a[208:211], v[16:19], v[68:71]\n"
"v_exp_f32 v60, v60\n"
"v_mfma_f32_16x16x32_bf16 v[52:55], a[212:215], v[20:23], v[52:55]\n"
"v_exp_f32 v61, v61\n"
"v_mfma_f32_16x16x32_bf16 v[52:55], a[216:219], v[24:27], v[52:55]\n"
"v_exp_f32 v62, v62\n"
"ds_read_b64_tr_b16 v[88:89], v212 offset:50688\n"
"v_mfma_f32_16x16x32_bf16 v[52:55], a[220:223], v[28:31], v[52:55]\n"
"v_exp_f32 v63, v63\n"
"ds_read_b64_tr_b16 v[90:91], v212 offset:54784\n"
"v_cvt_pk_bf16_f32 v56, v56, v57\n"
"v_cvt_pk_bf16_f32 v57, v58, v59\n"
"v_cvt_pk_bf16_f32 v58, v60, v61\n"
"v_cvt_pk_bf16_f32 v59, v62, v63\n"
"s_nop 1\n"
"v_mfma_f32_16x16x32_bf16 v[192:195], v[204:207], v[56:59], v[192:195]\n"
"ds_read_b64_tr_b16 v[92:93], v215 offset:50688\n"
"ds_read_b64_tr_b16 v[94:95], v215 offset:54784\n"
"s_waitcnt lgkmcnt(4)\n"
"v_mfma_f32_16x16x32_bf16 v[96:99], a[224:227], v[56:59], v[96:99]\n"
"v_mov_b32 v73, v72\n"
"v_mov_b32 v74, v72\n"
"v_mfma_f32_16x16x32_bf16 v[108:111], a[228:231], v[56:59], v[108:111]\n"
"v_mov_b32 v75, v72\n"
"s_add_u32 s36, s36, s37\n"
"v_mfma_f32_16x16x32_bf16 v[120:123], a[232:235], v[56:59], v[120:123]\n"
"s_mul_i32 s38, s38, 0xc00\n"
"s_lshl_b32 s36, s36, 14\n"
"v_mfma_f32_16x16x32_bf16 v[132:135], v[76:79], v[56:59], v[132:135]\n"
"v_exp_f32 v48, v48\n"
"v_mfma_f32_16x16x32_bf16 v[144:147], v[80:83], v[56:59], v[144:147]\n"
"v_exp_f32 v49, v49\n"
"v_mfma_f32_16x16x32_bf16 v[156:159], v[84:87], v[56:59], v[156:159]\n"
"v_exp_f32 v50, v50\n"
"s_waitcnt lgkmcnt(0)\n"
"v_mfma_f32_16x16x32_bf16 v[168:171], v[88:91], v[56:59], v[168:171]\n"
"v_exp_f32 v51, v51\n"
"v_mfma_f32_16x16x32_bf16 v[180:183], v[92:95], v[56:59], v[180:183]\n"
"s_waitcnt vmcnt(11)\n"
"s_barrier\n"
"v_mov_b32 v246, s90\n"
"v_mfma_f32_16x16x32_bf16 v[56:59], a[192:195], v[32:35], v[72:75]\n"
"v_mov_b32 v247, s91\n"
"buffer_store_dwordx2 v[246:247], v223, s[64:67], 0 offen sc1\n"
"v_mfma_f32_16x16x32_bf16 v[56:59], a[196:199], v[36:39], v[56:59]\n"
"s_add_u32 s64, s64, 0x800\n"
"s_addc_u32 s65, s65, 0\n"
"v_mfma_f32_16x16x32_bf16 v[56:59], a[200:203], v[40:43], v[56:59]\n"
"v_exp_f32 v52, v52\n"
"v_mfma_f32_16x16x32_bf16 v[56:59], a[204:207], v[44:47], v[56:59]\n"
"v_exp_f32 v53, v53\n"
"v_mfma_f32_16x16x32_bf16 v[60:63], a[208:211], v[32:35], v[72:75]\n"
"v_exp_f32 v54, v54\n"
"v_mfma_f32_16x16x32_bf16 v[60:63], a[212:215], v[36:39], v[60:63]\n"
"v_exp_f32 v55, v55\n"
"v_mfma_f32_16x16x32_bf16 v[60:63], a[216:219], v[40:43], v[60:63]\n"
"v_cvt_pk_bf16_f32 v48, v48, v49\n"
"v_cvt_pk_bf16_f32 v49, v50, v51\n"
"v_cvt_pk_bf16_f32 v50, v52, v53\n"
"v_mfma_f32_16x16x32_bf16 v[60:63], a[220:223], v[44:47], v[60:63]\n"
"v_cvt_pk_bf16_f32 v51, v54, v55\n"
"ds_read_b128 a[192:195], v210 offset:0\n"
"s_add_u32 m0, s77, 0x16000\n"
"v_mfma_f32_16x16x32_bf16 v[196:199], v[204:207], v[48:51], v[196:199]\n"
"buffer_load_dwordx4 v220, s[48:51], s79 offen nt lds\n"
"v_mfma_f32_16x16x32_bf16 v[100:103], a[224:227], v[48:51], v[100:103]\n"
"ds_read_b128 a[196:199], v210 offset:512\n"
"ds_read_b128 a[200:203], v210 offset:1024\n"
"v_mfma_f32_16x16x32_bf16 v[112:115], a[228:231], v[48:51], v[112:115]\n"
"ds_read_b128 a[204:207], v210 offset:1536\n"
"ds_read_b128 a[208:211], v210 offset:4096\n"
"v_mfma_f32_16x16x32_bf16 v[124:127], a[232:235], v[48:51], v[124:127]\n"
"s_lshl_b32 s37, s37, 15\n"
"ds_read_b128 a[212:215], v210 offset:4608\n"
"v_mfma_f32_16x16x32_bf16 v[136:139], v[76:79], v[48:51], v[136:139]\n"
"s_add_u32 s36, s36, s38\n"
"ds_read_b128 a[216:219], v210 offset:5120\n"
"v_mfma_f32_16x16x32_bf16 v[148:151], v[80:83], v[48:51], v[148:151]\n"
"ds_read_b128 a[220:223], v210 offset:5632\n"
"s_mul_i32 s38, s76, 0xc00\n"
"v_mfma_f32_16x16x32_bf16 v[160:163], v[84:87], v[48:51], v[160:163]\n"
"v_exp_f32 v56, v56\n"
"v_mfma_f32_16x16x32_bf16 v[172:175], v[88:91], v[48:51], v[172:175]\n"
"v_exp_f32 v57, v57\n"
"v_mfma_f32_16x16x32_bf16 v[184:187], v[92:95], v[48:51], v[184:187]\n"
"v_exp_f32 v58, v58\n"
"s_waitcnt lgkmcnt(1)\n"
"v_mfma_f32_16x16x32_bf16 v[48:51], a[192:195], v[0:3], v[64:67]\n"
"v_exp_f32 v59, v59\n"
"v_mfma_f32_16x16x32_bf16 v[48:51], a[196:199], v[4:7], v[48:51]\n"
"v_exp_f32 v60, v60\n"
"v_mfma_f32_16x16x32_bf16 v[48:51], a[200:203], v[8:11], v[48:51]\n"
"v_exp_f32 v61, v61\n"
"v_mfma_f32_16x16x32_bf16 v[48:51], a[204:207], v[12:15], v[48:51]\n"
"v_exp_f32 v62, v62\n"
"v_mfma_f32_16x16x32_bf16 v[52:55], a[208:211], v[0:3], v[64:67]\n"
"v_exp_f32 v63, v63\n"
"v_mfma_f32_16x16x32_bf16 v[52:55], a[212:215], v[4:7], v[52:55]\n"
"v_cvt_pk_bf16_f32 v56, v56, v57\n"
"v_cvt_pk_bf16_f32 v57, v58, v59\n"
"v_mfma_f32_16x16x32_bf16 v[52:55], a[216:219], v[8:11], v[52:55]\n"
"v_cvt_pk_bf16_f32 v58, v60, v61\n"
"v_cvt_pk_bf16_f32 v59, v62, v63\n"
"s_waitcnt lgkmcnt(0)\n"
"v_mfma_f32_16x16x32_bf16 v[52:55], a[220:223], v[12:15], v[52:55]\n"
"buffer_load_dwordx4 v220, s[48:51], s79 offen offset:2048 nt lds\n"
"s_xor_b32 s79, s100, 0x212000\n"
"v_mfma_f32_16x16x32_bf16 v[200:203], v[204:207], v[56:59], v[200:203]\n"
"ds_read_b64_tr_b16 a[236:237], v216 offset:512\n"
"ds_read_b64_tr_b16 a[238:239], v216 offset:4608\n"
"v_mfma_f32_16x16x32_bf16 v[104:107], a[224:227], v[56:59], v[104:107]\n"
"ds_read_b64_tr_b16 a[224:225], v213 offset:0\n"
"ds_read_b64_tr_b16 a[226:227], v213 offset:4096\n"
"v_mfma_f32_16x16x32_bf16 v[116:119], a[228:231], v[56:59], v[116:119]\n"
"ds_read_b64_tr_b16 a[228:229], v216 offset:0\n"
"ds_read_b64_tr_b16 a[230:231], v216 offset:4096\n"
"v_mfma_f32_16x16x32_bf16 v[128:131], a[232:235], v[56:59], v[128:131]\n"
"ds_read_b64_tr_b16 a[232:233], v213 offset:512\n"
"ds_read_b64_tr_b16 a[234:235], v213 offset:4608\n"
"v_mfma_f32_16x16x32_bf16 v[140:143], v[76:79], v[56:59], v[140:143]\n"
"ds_read_b64_tr_b16 a[240:241], v213 offset:1024\n"
"ds_read_b64_tr_b16 a[242:243], v213 offset:5120\n"
"v_mfma_f32_16x16x32_bf16 v[152:155], v[80:83], v[56:59], v[152:155]\n"
"ds_read_b64_tr_b16 a[244:245], v216 offset:1024\n"
"ds_read_b64_tr_b16 a[246:247], v216 offset:5120\n"
"v_mfma_f32_16x16x32_bf16 v[164:167], v[84:87], v[56:59], v[164:167]\n"
"v_exp_f32 v48, v48\n"
"v_mfma_f32_16x16x32_bf16 v[176:179], v[88:91], v[56:59], v[176:179]\n"
"v_exp_f32 v49, v49\n"
"v_mfma_f32_16x16x32_bf16 v[188:191], v[92:95], v[56:59], v[188:191]\n"
"v_exp_f32 v50, v50\n"
"v_mfma_f32_16x16x32_bf16 v[56:59], a[192:195], v[16:19], v[68:71]\n"
"v_exp_f32 v51, v51\n"
"v_mfma_f32_16x16x32_bf16 v[56:59], a[196:199], v[20:23], v[56:59]\n"
"v_exp_f32 v52, v52\n"
"v_mfma_f32_16x16x32_bf16 v[56:59], a[200:203], v[24:27], v[56:59]\n"
"v_exp_f32 v53, v53\n"
"v_mfma_f32_16x16x32_bf16 v[56:59], a[204:207], v[28:31], v[56:59]\n"
"v_exp_f32 v54, v54\n"
"v_mfma_f32_16x16x32_bf16 v[60:63], a[208:211], v[16:19], v[68:71]\n"
"v_exp_f32 v55, v55\n"
"v_mfma_f32_16x16x32_bf16 v[60:63], a[212:215], v[20:23], v[60:63]\n"
"ds_read_b64_tr_b16 a[248:249], v213 offset:1536\n"
"ds_read_b64_tr_b16 a[250:251], v213 offset:5632\n"
"v_mfma_f32_16x16x32_bf16 v[60:63], a[216:219], v[24:27], v[60:63]\n"
"v_cvt_pk_bf16_f32 v48, v48, v49\n"
"v_cvt_pk_bf16_f32 v49, v50, v51\n"
"v_cvt_pk_bf16_f32 v50, v52, v53\n"
"v_mfma_f32_16x16x32_bf16 v[60:63], a[220:223], v[28:31], v[60:63]\n"
"v_cvt_pk_bf16_f32 v51, v54, v55\n"
"ds_read_b64_tr_b16 a[252:253], v216 offset:1536\n"
"ds_read_b64_tr_b16 a[254:255], v216 offset:5632\n"
"s_add_u32 m0, s77, 0x18000\n"
"v_mfma_f32_16x16x32_bf16 v[192:195], v[204:207], v[48:51], v[192:195]\n"
"buffer_load_dwordx4 v220, s[44:47], s79 offen nt lds\n"
"s_waitcnt lgkmcnt(4)\n"
"v_mfma_f32_16x16x32_bf16 v[96:99], a[224:227], v[48:51], v[96:99]\n"
"v_exp_f32 v56, v56\n"
"v_mfma_f32_16x16x32_bf16 v[108:111], a[228:231], v[48:51], v[108:111]\n"
"v_exp_f32 v57, v57\n"
"v_mfma_f32_16x16x32_bf16 v[120:123], a[232:235], v[48:51], v[120:123]\n"
"v_exp_f32 v58, v58\n"
"v_mfma_f32_16x16x32_bf16 v[132:135], a[236:239], v[48:51], v[132:135]\n"
"v_exp_f32 v59, v59\n"
"v_mfma_f32_16x16x32_bf16 v[144:147], a[240:243], v[48:51], v[144:147]\n"
"v_exp_f32 v60, v60\n"
"v_mfma_f32_16x16x32_bf16 v[156:159], a[244:247], v[48:51], v[156:159]\n"
"v_exp_f32 v61, v61\n"
"s_waitcnt lgkmcnt(0)\n"
"v_mfma_f32_16x16x32_bf16 v[168:171], a[248:251], v[48:51], v[168:171]\n"
"v_exp_f32 v62, v62\n"
"v_mfma_f32_16x16x32_bf16 v[180:183], a[252:255], v[48:51], v[180:183]\n"
"s_barrier\n"
"v_mfma_f32_16x16x32_bf16 v[48:51], a[192:195], v[32:35], v[72:75]\n"
"v_exp_f32 v63, v63\n"
"v_mfma_f32_16x16x32_bf16 v[48:51], a[196:199], v[36:39], v[48:51]\n"
"v_cvt_pk_bf16_f32 v56, v56, v57\n"
"v_cvt_pk_bf16_f32 v57, v58, v59\n"
"v_mfma_f32_16x16x32_bf16 v[48:51], a[200:203], v[40:43], v[48:51]\n"
"v_cvt_pk_bf16_f32 v58, v60, v61\n"
"v_cvt_pk_bf16_f32 v59, v62, v63\n"
"v_mfma_f32_16x16x32_bf16 v[48:51], a[204:207], v[44:47], v[48:51]\n"
"ds_read_b128 a[192:195], v210 offset:16384\n"
"ds_read_b128 a[196:199], v210 offset:16896\n"
"v_mfma_f32_16x16x32_bf16 v[52:55], a[208:211], v[32:35], v[72:75]\n"
"ds_read_b128 a[200:203], v210 offset:17408\n"
"ds_read_b128 a[204:207], v210 offset:17920\n"
"v_mfma_f32_16x16x32_bf16 v[52:55], a[212:215], v[36:39], v[52:55]\n"
"ds_read_b128 a[208:211], v210 offset:20480\n"
"ds_read_b128 a[212:215], v210 offset:20992\n"
"v_mfma_f32_16x16x32_bf16 v[52:55], a[216:219], v[40:43], v[52:55]\n"
"ds_read_b128 a[216:219], v210 offset:21504\n"
"buffer_load_dwordx4 v220, s[44:47], s79 offen offset:2048 nt lds\n"
"v_mfma_f32_16x16x32_bf16 v[52:55], a[220:223], v[44:47], v[52:55]\n"
"ds_read_b128 a[220:223], v210 offset:22016\n"
"ds_read_b64_tr_b16 v[76:77], v216 offset:16896\n"
"v_mfma_f32_16x16x32_bf16 v[196:199], v[204:207], v[56:59], v[196:199]\n"
"v_exp_f32 v48, v48\n"
"v_mfma_f32_16x16x32_bf16 v[100:103], a[224:227], v[56:59], v[100:103]\n"
"v_exp_f32 v49, v49\n"
"v_mfma_f32_16x16x32_bf16 v[112:115], a[228:231], v[56:59], v[112:115]\n"
"v_exp_f32 v50, v50\n"
"v_mfma_f32_16x16x32_bf16 v[124:127], a[232:235], v[56:59], v[124:127]\n"
"v_exp_f32 v51, v51\n"
"v_mfma_f32_16x16x32_bf16 v[136:139], a[236:239], v[56:59], v[136:139]\n"
"v_exp_f32 v52, v52\n"
"v_mfma_f32_16x16x32_bf16 v[148:151], a[240:243], v[56:59], v[148:151]\n"
"v_exp_f32 v53, v53\n"
"v_mfma_f32_16x16x32_bf16 v[160:163], a[244:247], v[56:59], v[160:163]\n"
"v_exp_f32 v54, v54\n"
"v_mfma_f32_16x16x32_bf16 v[172:175], a[248:251], v[56:59], v[172:175]\n"
"v_exp_f32 v55, v55\n"
"v_mfma_f32_16x16x32_bf16 v[184:187], a[252:255], v[56:59], v[184:187]\n"
"v_cvt_pk_bf16_f32 v48, v48, v49\n"
"v_cvt_pk_bf16_f32 v49, v50, v51\n"
"s_waitcnt lgkmcnt(0)\n"
"v_mfma_f32_16x16x32_bf16 v[56:59], a[192:195], v[0:3], v[64:67]\n"
"v_cvt_pk_bf16_f32 v50, v52, v53\n"
"v_cvt_pk_bf16_f32 v51, v54, v55\n"
"v_mfma_f32_16x16x32_bf16 v[56:59], a[196:199], v[4:7], v[56:59]\n"
"ds_read_b64_tr_b16 v[78:79], v216 offset:20992\n"
"ds_read_b64_tr_b16 v[80:81], v213 offset:17408\n"
"v_mfma_f32_16x16x32_bf16 v[56:59], a[200:203], v[8:11], v[56:59]\n"
"ds_read_b64_tr_b16 v[82:83], v213 offset:21504\n"
"ds_read_b64_tr_b16 v[84:85], v216 offset:17408\n"
"s_add_u32 m0, s77, 0x1a000\n"
"v_mfma_f32_16x16x32_bf16 v[56:59], a[204:207], v[12:15], v[56:59]\n"
"buffer_load_dwordx4 v220, s[48:51], s79 offen nt lds\n"
"v_mfma_f32_16x16x32_bf16 v[60:63], a[208:211], v[0:3], v[64:67]\n"
"ds_read_b64_tr_b16 v[86:87], v216 offset:21504\n"
"ds_read_b64_tr_b16 v[88:89], v213 offset:17920\n"
"v_mfma_f32_16x16x32_bf16 v[60:63], a[212:215], v[4:7], v[60:63]\n"
"ds_read_b64_tr_b16 v[90:91], v213 offset:22016\n"
"ds_read_b64_tr_b16 v[92:93], v216 offset:17920\n"
"v_mfma_f32_16x16x32_bf16 v[60:63], a[216:219], v[8:11], v[60:63]\n"
"ds_read_b64_tr_b16 v[94:95], v216 offset:22016\n"
"v_mfma_f32_16x16x32_bf16 v[60:63], a[220:223], v[12:15], v[60:63]\n"
"v_mfma_f32_16x16x32_bf16 v[200:203], v[204:207], v[48:51], v[200:203]\n"
"v_exp_f32 v56, v56\n"
"v_mfma_f32_16x16x32_bf16 v[104:107], a[224:227], v[48:51], v[104:107]\n"
"ds_read_b64_tr_b16 a[224:225], v213 offset:16384\n"
"ds_read_b64_tr_b16 a[226:227], v213 offset:20480\n"
"v_mfma_f32_16x16x32_bf16 v[116:119], a[228:231], v[48:51], v[116:119]\n"
"ds_read_b64_tr_b16 a[228:229], v216 offset:16384\n"
"ds_read_b64_tr_b16 a[230:231], v216 offset:20480\n"
"v_mfma_f32_16x16x32_bf16 v[128:131], a[232:235], v[48:51], v[128:131]\n"
"ds_read_b64_tr_b16 a[232:233], v213 offset:16896\n"
"ds_read_b64_tr_b16 a[234:235], v213 offset:20992\n"
"v_mfma_f32_16x16x32_bf16 v[140:143], a[236:239], v[48:51], v[140:143]\n"
"v_exp_f32 v57, v57\n"
"v_mfma_f32_16x16x32_bf16 v[152:155], a[240:243], v[48:51], v[152:155]\n"
"v_exp_f32 v58, v58\n"
"v_mfma_f32_16x16x32_bf16 v[164:167], a[244:247], v[48:51], v[164:167]\n"
"v_exp_f32 v59, v59\n"
"v_mfma_f32_16x16x32_bf16 v[176:179], a[248:251], v[48:51], v[176:179]\n"
"v_exp_f32 v60, v60\n"
"v_mfma_f32_16x16x32_bf16 v[188:191], a[252:255], v[48:51], v[188:191]\n"
"v_exp_f32 v61, v61\n"
"v_mfma_f32_16x16x32_bf16 v[48:51], a[192:195], v[16:19], v[68:71]\n"
"v_exp_f32 v62, v62\n"
"v_mfma_f32_16x16x32_bf16 v[48:51], a[196:199], v[20:23], v[48:51]\n"
"v_exp_f32 v63, v63\n"
"v_mfma_f32_16x16x32_bf16 v[48:51], a[200:203], v[24:27], v[48:51]\n"
"v_cvt_pk_bf16_f32 v56, v56, v57\n"
"v_cvt_pk_bf16_f32 v57, v58, v59\n"
"v_mfma_f32_16x16x32_bf16 v[48:51], a[204:207], v[28:31], v[48:51]\n"
"v_cvt_pk_bf16_f32 v58, v60, v61\n"
"v_cvt_pk_bf16_f32 v59, v62, v63\n"
"v_mfma_f32_16x16x32_bf16 v[52:55], a[208:211], v[16:19], v[68:71]\n"
"buffer_load_dwordx4 v220, s[48:51], s79 offen offset:2048 nt lds\n"
"s_xor_b32 s79, s100, 0x214000\n"
"v_mfma_f32_16x16x32_bf16 v[52:55], a[212:215], v[20:23], v[52:55]\n"
"v_mfma_f32_16x16x32_bf16 v[52:55], a[216:219], v[24:27], v[52:55]\n"
"v_mfma_f32_16x16x32_bf16 v[52:55], a[220:223], v[28:31], v[52:55]\n"
"v_mfma_f32_16x16x32_bf16 v[192:195], v[204:207], v[56:59], v[192:195]\n"
"v_exp_f32 v48, v48\n"
"s_waitcnt lgkmcnt(0)\n"
"v_mfma_f32_16x16x32_bf16 v[96:99], a[224:227], v[56:59], v[96:99]\n"
"v_exp_f32 v49, v49\n"
"v_mfma_f32_16x16x32_bf16 v[108:111], a[228:231], v[56:59], v[108:111]\n"
"v_exp_f32 v50, v50\n"
"v_mfma_f32_16x16x32_bf16 v[120:123], a[232:235], v[56:59], v[120:123]\n"
"v_exp_f32 v51, v51\n"
"v_mfma_f32_16x16x32_bf16 v[132:135], v[76:79], v[56:59], v[132:135]\n"
"v_exp_f32 v52, v52\n"
"v_mfma_f32_16x16x32_bf16 v[144:147], v[80:83], v[56:59], v[144:147]\n"
"v_exp_f32 v53, v53\n"
"v_mfma_f32_16x16x32_bf16 v[156:159], v[84:87], v[56:59], v[156:159]\n"
"v_exp_f32 v54, v54\n"
"v_mfma_f32_16x16x32_bf16 v[168:171], v[88:91], v[56:59], v[168:171]\n"
"v_exp_f32 v55, v55\n"
"v_mfma_f32_16x16x32_bf16 v[180:183], v[92:95], v[56:59], v[180:183]\n"
"s_barrier\n"
"v_mfma_f32_16x16x32_bf16 v[56:59], a[192:195], v[32:35], v[72:75]\n"
"v_cvt_pk_bf16_f32 v48, v48, v49\n"
"v_cvt_pk_bf16_f32 v49, v50, v51\n"
"v_mfma_f32_16x16x32_bf16 v[56:59], a[196:199], v[36:39], v[56:59]\n"
"v_cvt_pk_bf16_f32 v50, v52, v53\n"
"v_cvt_pk_bf16_f32 v51, v54, v55\n"
"v_mfma_f32_16x16x32_bf16 v[56:59], a[200:203], v[40:43], v[56:59]\n"
"ds_read_b128 a[192:195], v208 offset:16384\n"
"ds_read_b128 a[196:199], v208 offset:16896\n"
"v_mfma_f32_16x16x32_bf16 v[56:59], a[204:207], v[44:47], v[56:59]\n"
"ds_read_b128 a[200:203], v208 offset:17408\n"
"ds_read_b128 a[204:207], v208 offset:17920\n"
"v_mfma_f32_16x16x32_bf16 v[60:63], a[208:211], v[32:35], v[72:75]\n"
"ds_read_b128 a[208:211], v208 offset:20480\n"
"ds_read_b64_tr_b16 a[236:237], v214 offset:16896\n"
"s_add_u32 m0, s77, 0xc000\n"
"v_mfma_f32_16x16x32_bf16 v[60:63], a[212:215], v[36:39], v[60:63]\n"
"buffer_load_dwordx4 v220, s[44:47], s79 offen nt lds\n"
"v_mfma_f32_16x16x32_bf16 v[60:63], a[216:219], v[40:43], v[60:63]\n"
"ds_read_b128 a[212:215], v208 offset:20992\n"
"ds_read_b128 a[216:219], v208 offset:21504\n"
"v_mfma_f32_16x16x32_bf16 v[60:63], a[220:223], v[44:47], v[60:63]\n"
"ds_read_b128 a[220:223], v208 offset:22016\n"
"ds_read_b64_tr_b16 a[238:239], v214 offset:20992\n"
"v_mfma_f32_16x16x32_bf16 v[196:199], v[204:207], v[48:51], v[196:199]\n"
"v_exp_f32 v56, v56\n"
"v_mfma_f32_16x16x32_bf16 v[100:103], a[224:227], v[48:51], v[100:103]\n"
"v_exp_f32 v57, v57\n"
"v_mfma_f32_16x16x32_bf16 v[112:115], a[228:231], v[48:51], v[112:115]\n"
"v_exp_f32 v58, v58\n"
"v_mfma_f32_16x16x32_bf16 v[124:127], a[232:235], v[48:51], v[124:127]\n"
"v_exp_f32 v59, v59\n"
"v_mfma_f32_16x16x32_bf16 v[136:139], v[76:79], v[48:51], v[136:139]\n"
"v_exp_f32 v60, v60\n"
"v_mfma_f32_16x16x32_bf16 v[148:151], v[80:83], v[48:51], v[148:151]\n"
"v_exp_f32 v61, v61\n"
"v_mfma_f32_16x16x32_bf16 v[160:163], v[84:87], v[48:51], v[160:163]\n"
"v_exp_f32 v62, v62\n"
"v_mfma_f32_16x16x32_bf16 v[172:175], v[88:91], v[48:51], v[172:175]\n"
"v_exp_f32 v63, v63\n"
"v_mfma_f32_16x16x32_bf16 v[184:187], v[92:95], v[48:51], v[184:187]\n"
"v_cvt_pk_bf16_f32 v56, v56, v57\n"
"v_cvt_pk_bf16_f32 v57, v58, v59\n"
"s_waitcnt lgkmcnt(0)\n"
"v_mfma_f32_16x16x32_bf16 v[48:51], a[192:195], v[0:3], v[64:67]\n"
"v_cvt_pk_bf16_f32 v58, v60, v61\n"
"v_cvt_pk_bf16_f32 v59, v62, v63\n"
"v_mfma_f32_16x16x32_bf16 v[48:51], a[196:199], v[4:7], v[48:51]\n"
"ds_read_b64_tr_b16 a[240:241], v211 offset:17408\n"
"ds_read_b64_tr_b16 a[242:243], v211 offset:21504\n"
"v_mfma_f32_16x16x32_bf16 v[48:51], a[200:203], v[8:11], v[48:51]\n"
"buffer_load_dwordx4 v220, s[44:47], s79 offen offset:2048 nt lds\n"
"ds_read_b64_tr_b16 a[244:245], v214 offset:17408\n"
"v_mfma_f32_16x16x32_bf16 v[48:51], a[204:207], v[12:15], v[48:51]\n"
"ds_read_b64_tr_b16 a[246:247], v214 offset:21504\n"
"ds_read_b64_tr_b16 a[248:249], v211 offset:17920\n"
"v_mfma_f32_16x16x32_bf16 v[52:55], a[208:211], v[0:3], v[64:67]\n"
"ds_read_b64_tr_b16 a[250:251], v211 offset:22016\n"
"ds_read_b64_tr_b16 a[252:253], v214 offset:17920\n"
"v_mfma_f32_16x16x32_bf16 v[52:55], a[212:215], v[4:7], v[52:55]\n"
"ds_read_b64_tr_b16 a[254:255], v214 offset:22016\n"
"v_mfma_f32_16x16x32_bf16 v[52:55], a[216:219], v[8:11], v[52:55]\n"
"v_mfma_f32_16x16x32_bf16 v[52:55], a[220:223], v[12:15], v[52:55]\n"
"v_mfma_f32_16x16x32_bf16 v[200:203], v[204:207], v[56:59], v[200:203]\n"
"v_exp_f32 v48, v48\n"
"v_mfma_f32_16x16x32_bf16 v[104:107], a[224:227], v[56:59], v[104:107]\n"
"ds_read_b64_tr_b16 a[224:225], v211 offset:16384\n"
"ds_read_b64_tr_b16 a[226:227], v211 offset:20480\n"
"v_mfma_f32_16x16x32_bf16 v[116:119], a[228:231], v[56:59], v[116:119]\n"
"ds_read_b64_tr_b16 a[228:229], v214 offset:16384\n"
"ds_read_b64_tr_b16 a[230:231], v214 offset:20480\n"
"v_mfma_f32_16x16x32_bf16 v[128:131], a[232:235], v[56:59], v[128:131]\n"
"ds_read_b64_tr_b16 a[232:233], v211 offset:16896\n"
"ds_read_b64_tr_b16 a[234:235], v211 offset:20992\n"
"v_mfma_f32_16x16x32_bf16 v[140:143], v[76:79], v[56:59], v[140:143]\n"
"v_exp_f32 v49, v49\n"
"v_mfma_f32_16x16x32_bf16 v[152:155], v[80:83], v[56:59], v[152:155]\n"
"v_exp_f32 v50, v50\n"
"v_mfma_f32_16x16x32_bf16 v[164:167], v[84:87], v[56:59], v[164:167]\n"
"v_exp_f32 v51, v51\n"
"v_mfma_f32_16x16x32_bf16 v[176:179], v[88:91], v[56:59], v[176:179]\n"
"v_exp_f32 v52, v52\n"
"v_mfma_f32_16x16x32_bf16 v[188:191], v[92:95], v[56:59], v[188:191]\n"
"v_exp_f32 v53, v53\n"
"v_mfma_f32_16x16x32_bf16 v[56:59], a[192:195], v[16:19], v[68:71]\n"
"v_exp_f32 v54, v54\n"
"v_mfma_f32_16x16x32_bf16 v[56:59], a[196:199], v[20:23], v[56:59]\n"
"v_exp_f32 v55, v55\n"
"v_mfma_f32_16x16x32_bf16 v[56:59], a[200:203], v[24:27], v[56:59]\n"
"v_cvt_pk_bf16_f32 v48, v48, v49\n"
"v_cvt_pk_bf16_f32 v49, v50, v51\n"
"v_mfma_f32_16x16x32_bf16 v[56:59], a[204:207], v[28:31], v[56:59]\n"
"v_cvt_pk_bf16_f32 v50, v52, v53\n"
"v_cvt_pk_bf16_f32 v51, v54, v55\n"
"s_add_u32 m0, s77, 0xe000\n"
"v_mfma_f32_16x16x32_bf16 v[60:63], a[208:211], v[16:19], v[68:71]\n"
"buffer_load_dwordx4 v220, s[48:51], s79 offen nt lds\n"
"v_mfma_f32_16x16x32_bf16 v[60:63], a[212:215], v[20:23], v[60:63]\n"
"v_mfma_f32_16x16x32_bf16 v[60:63], a[216:219], v[24:27], v[60:63]\n"
"v_mfma_f32_16x16x32_bf16 v[60:63], a[220:223], v[28:31], v[60:63]\n"
"v_mfma_f32_16x16x32_bf16 v[192:195], v[204:207], v[48:51], v[192:195]\n"
"v_exp_f32 v56, v56\n"
"s_waitcnt lgkmcnt(0)\n"
"v_mfma_f32_16x16x32_bf16 v[96:99], a[224:227], v[48:51], v[96:99]\n"
"v_exp_f32 v57, v57\n"
"v_mfma_f32_16x16x32_bf16 v[108:111], a[228:231], v[48:51], v[108:111]\n"
"v_exp_f32 v58, v58\n"
"v_mfma_f32_16x16x32_bf16 v[120:123], a[232:235], v[48:51], v[120:123]\n"
"v_exp_f32 v59, v59\n"
"v_mfma_f32_16x16x32_bf16 v[132:135], a[236:239], v[48:51], v[132:135]\n"
"v_exp_f32 v60, v60\n"
"v_mfma_f32_16x16x32_bf16 v[144:147], a[240:243], v[48:51], v[144:147]\n"
"v_exp_f32 v61, v61\n"
"v_mfma_f32_16x16x32_bf16 v[156:159], a[244:247], v[48:51], v[156:159]\n"
"v_lshlrev_b32 v242, 3, v223\n"
"v_and_b32 v244, 0x1000, v242\n"
"v_mfma_f32_16x16x32_bf16 v[168:171], a[248:251], v[48:51], v[168:171]\n"
"v_and_b32 v242, 0xf80, v242\n"
"s_lshr_b32 s83, s20, 3\n"
"v_mfma_f32_16x16x32_bf16 v[180:183], a[252:255], v[48:51], v[180:183]\n"
"s_waitcnt vmcnt(20)\n"
"s_barrier\n"
"v_mfma_f32_16x16x32_bf16 v[48:51], a[192:195], v[32:35], v[72:75]\n"
"v_lshl_add_u32 v242, v244, 9, v242\n"
"s_lshl2_add_u32 s83, s83, s76\n"
"v_mfma_f32_16x16x32_bf16 v[48:51], a[196:199], v[36:39], v[48:51]\n"
"s_lshl_b32 s83, s83, 12\n"
"s_add_u32 s83, s83, s21\n"
"v_mfma_f32_16x16x32_bf16 v[48:51], a[200:203], v[40:43], v[48:51]\n"
"v_exp_f32 v62, v62\n"
"v_mfma_f32_16x16x32_bf16 v[48:51], a[204:207], v[44:47], v[48:51]\n"
"v_exp_f32 v63, v63\n"
"v_mfma_f32_16x16x32_bf16 v[52:55], a[208:211], v[32:35], v[72:75]\n"
"v_cvt_pk_bf16_f32 v56, v56, v57\n"
"v_cvt_pk_bf16_f32 v57, v58, v59\n"
"v_mfma_f32_16x16x32_bf16 v[52:55], a[212:215], v[36:39], v[52:55]\n"
"v_cvt_pk_bf16_f32 v58, v60, v61\n"
"v_cvt_pk_bf16_f32 v59, v62, v63\n"
"v_mfma_f32_16x16x32_bf16 v[52:55], a[216:219], v[40:43], v[52:55]\n"
"ds_read_b128 a[192:195], v209 offset:0\n"
"ds_read_b128 a[196:199], v209 offset:512\n"
"v_mfma_f32_16x16x32_bf16 v[52:55], a[220:223], v[44:47], v[52:55]\n"
"ds_read_b128 a[200:203], v209 offset:1024\n"
"ds_read_b128 a[204:207], v209 offset:1536\n"
"v_mfma_f32_16x16x32_bf16 v[196:199], v[204:207], v[56:59], v[196:199]\n"
"buffer_load_dword v253, v242, s[52:55], s83 offen\n"
"s_add_u32 s83, s82, 0x100000\n"
"v_mfma_f32_16x16x32_bf16 v[100:103], a[224:227], v[56:59], v[100:103]\n"
"ds_read_b128 a[208:211], v209 offset:4096\n"
"buffer_load_dwordx4 v220, s[48:51], s79 offen offset:2048 nt lds\n"
"v_mfma_f32_16x16x32_bf16 v[112:115], a[228:231], v[56:59], v[112:115]\n"
"s_xor_b32 s79, s100, 0x216000\n"
"ds_read_b128 a[212:215], v209 offset:4608\n"
"v_mfma_f32_16x16x32_bf16 v[124:127], a[232:235], v[56:59], v[124:127]\n"
"ds_read_b128 a[216:219], v209 offset:5120\n"
"ds_read_b128 a[220:223], v209 offset:5632\n"
"v_mfma_f32_16x16x32_bf16 v[136:139], a[236:239], v[56:59], v[136:139]\n"
"v_exp_f32 v48, v48\n"
"v_mfma_f32_16x16x32_bf16 v[148:151], a[240:243], v[56:59], v[148:151]\n"
"v_exp_f32 v49, v49\n"
"v_mfma_f32_16x16x32_bf16 v[160:163], a[244:247], v[56:59], v[160:163]\n"
"v_exp_f32 v50, v50\n"
"v_mfma_f32_16x16x32_bf16 v[172:175], a[248:251], v[56:59], v[172:175]\n"
"v_exp_f32 v51, v51\n"
"v_mfma_f32_16x16x32_bf16 v[184:187], a[252:255], v[56:59], v[184:187]\n"
"v_exp_f32 v52, v52\n"
"s_waitcnt lgkmcnt(0)\n"
"v_mfma_f32_16x16x32_bf16 v[56:59], a[192:195], v[0:3], v[64:67]\n"
"v_exp_f32 v53, v53\n"
"v_mfma_f32_16x16x32_bf16 v[56:59], a[196:199], v[4:7], v[56:59]\n"
"v_exp_f32 v54, v54\n"
"v_mfma_f32_16x16x32_bf16 v[56:59], a[200:203], v[8:11], v[56:59]\n"
"v_exp_f32 v55, v55\n"
"v_mfma_f32_16x16x32_bf16 v[56:59], a[204:207], v[12:15], v[56:59]\n"
"v_cvt_pk_bf16_f32 v48, v48, v49\n"
"v_cvt_pk_bf16_f32 v49, v50, v51\n"
"v_mfma_f32_16x16x32_bf16 v[60:63], a[208:211], v[0:3], v[64:67]\n"
"v_cvt_pk_bf16_f32 v50, v52, v53\n"
"v_cvt_pk_bf16_f32 v51, v54, v55\n"
"v_mfma_f32_16x16x32_bf16 v[60:63], a[212:215], v[4:7], v[60:63]\n"
"ds_read_b64_tr_b16 v[76:77], v215 offset:512\n"
"ds_read_b64_tr_b16 v[78:79], v215 offset:4608\n"
"s_add_u32 m0, s77, 0x1c000\n"
"v_mfma_f32_16x16x32_bf16 v[60:63], a[216:219], v[8:11], v[60:63]\n"
"buffer_load_dwordx4 v220, s[44:47], s79 offen nt lds\n"
"v_mfma_f32_16x16x32_bf16 v[60:63], a[220:223], v[12:15], v[60:63]\n"
"ds_read_b64_tr_b16 v[80:81], v212 offset:1024\n"
"ds_read_b64_tr_b16 v[82:83], v212 offset:5120\n"
"v_mfma_f32_16x16x32_bf16 v[200:203], v[204:207], v[48:51], v[200:203]\n"
"ds_read_b64_tr_b16 v[84:85], v215 offset:1024\n"
"ds_read_b64_tr_b16 v[86:87], v215 offset:5120\n"
"v_mfma_f32_16x16x32_bf16 v[104:107], a[224:227], v[48:51], v[104:107]\n"
"ds_read_b64_tr_b16 a[224:225], v212 offset:0\n"
"ds_read_b64_tr_b16 a[226:227], v212 offset:4096\n"
"v_mfma_f32_16x16x32_bf16 v[116:119], a[228:231], v[48:51], v[116:119]\n"
"buffer_load_dwordx2 v[232:233], v225, s[68:71], s92 offen sc1\n"
"ds_read_b64_tr_b16 a[228:229], v215 offset:0\n"
"v_mfma_f32_16x16x32_bf16 v[128:131], a[232:235], v[48:51], v[128:131]\n"
"ds_read_b64_tr_b16 a[230:231], v215 offset:4096\n"
"ds_read_b64_tr_b16 a[232:233], v212 offset:512\n"
"v_mfma_f32_16x16x32_bf16 v[140:143], a[236:239], v[48:51], v[140:143]\n"
"ds_read_b64_tr_b16 a[234:235], v212 offset:4608\n"
"ds_read_b64_tr_b16 v[88:89], v212 offset:1536\n"
"v_mfma_f32_16x16x32_bf16 v[152:155], a[240:243], v[48:51], v[152:155]\n"
"v_exp_f32 v56, v56\n"
"v_mfma_f32_16x16x32_bf16 v[164:167], a[244:247], v[48:51], v[164:167]\n"
"v_exp_f32 v57, v57\n"
"v_mfma_f32_16x16x32_bf16 v[176:179], a[248:251], v[48:51], v[176:179]\n"
"v_exp_f32 v58, v58\n"
"v_mfma_f32_16x16x32_bf16 v[188:191], a[252:255], v[48:51], v[188:191]\n"
"v_exp_f32 v59, v59\n"
"v_mfma_f32_16x16x32_bf16 v[48:51], a[192:195], v[16:19], v[68:71]\n"
"v_exp_f32 v60, v60\n"
"v_mfma_f32_16x16x32_bf16 v[48:51], a[196:199], v[20:23], v[48:51]\n"
"v_exp_f32 v61, v61\n"
"v_mfma_f32_16x16x32_bf16 v[48:51], a[200:203], v[24:27], v[48:51]\n"
"v_exp_f32 v62, v62\n"
"v_mfma_f32_16x16x32_bf16 v[48:51], a[204:207], v[28:31], v[48:51]\n"
"v_exp_f32 v63, v63\n"
"v_mfma_f32_16x16x32_bf16 v[52:55], a[208:211], v[16:19], v[68:71]\n"
"ds_read_b64_tr_b16 v[90:91], v212 offset:5632\n"
"v_cvt_pk_bf16_f32 v56, v56, v57\n"
"v_mfma_f32_16x16x32_bf16 v[52:55], a[212:215], v[20:23], v[52:55]\n"
"v_cvt_pk_bf16_f32 v57, v58, v59\n"
"v_cvt_pk_bf16_f32 v58, v60, v61\n"
"v_mfma_f32_16x16x32_bf16 v[52:55], a[216:219], v[24:27], v[52:55]\n"
"v_cvt_pk_bf16_f32 v59, v62, v63\n"
"ds_read_b64_tr_b16 v[92:93], v215 offset:1536\n"
"v_mfma_f32_16x16x32_bf16 v[52:55], a[220:223], v[28:31], v[52:55]\n"
"ds_read_b64_tr_b16 v[94:95], v215 offset:5632\n"
"buffer_load_dwordx4 v220, s[44:47], s79 offen offset:2048 nt lds\n"
"v_mfma_f32_16x16x32_bf16 v[192:195], v[204:207], v[56:59], v[192:195]\n"
"v_exp_f32 v48, v48\n"
"s_waitcnt lgkmcnt(2)\n"
"v_mfma_f32_16x16x32_bf16 v[96:99], a[224:227], v[56:59], v[96:99]\n"
"v_exp_f32 v49, v49\n"
"v_mfma_f32_16x16x32_bf16 v[108:111], a[228:231], v[56:59], v[108:111]\n"
"v_exp_f32 v50, v50\n"
"v_mfma_f32_16x16x32_bf16 v[120:123], a[232:235], v[56:59], v[120:123]\n"
"v_exp_f32 v51, v51\n"
"v_mfma_f32_16x16x32_bf16 v[132:135], v[76:79], v[56:59], v[132:135]\n"
"v_exp_f32 v52, v52\n"
"v_mfma_f32_16x16x32_bf16 v[144:147], v[80:83], v[56:59], v[144:147]\n"
"v_exp_f32 v53, v53\n"
"v_mfma_f32_16x16x32_bf16 v[156:159], v[84:87], v[56:59], v[156:159]\n"
"v_exp_f32 v54, v54\n"
"v_mfma_f32_16x16x32_bf16 v[168:171], v[88:91], v[56:59], v[168:171]\n"
"v_exp_f32 v55, v55\n"
"s_waitcnt lgkmcnt(0)\n"
"v_mfma_f32_16x16x32_bf16 v[180:183], v[92:95], v[56:59], v[180:183]\n"
"s_waitcnt vmcnt(21)\n"
"s_barrier\n"
"v_mfma_f32_16x16x32_bf16 v[56:59], a[192:195], v[32:35], v[72:75]\n"
"v_cvt_pk_bf16_f32 v48, v48, v49\n"
"v_cvt_pk_bf16_f32 v49, v50, v51\n"
"v_mfma_f32_16x16x32_bf16 v[56:59], a[196:199], v[36:39], v[56:59]\n"
"v_cvt_pk_bf16_f32 v50, v52, v53\n"
"v_cvt_pk_bf16_f32 v51, v54, v55\n"
"v_mfma_f32_16x16x32_bf16 v[56:59], a[200:203], v[40:43], v[56:59]\n"
"ds_read_b128 a[192:195], v208 offset:0\n"
"ds_read_b128 a[196:199], v208 offset:512\n"
"v_mfma_f32_16x16x32_bf16 v[56:59], a[204:207], v[44:47], v[56:59]\n"
"ds_read_b128 a[200:203], v208 offset:1024\n"
"ds_read_b128 a[204:207], v208 offset:1536\n"
"v_mfma_f32_16x16x32_bf16 v[60:63], a[208:211], v[32:35], v[72:75]\n"
"ds_read_b128 a[208:211], v208 offset:4096\n"
"ds_read_b64_tr_b16 a[236:237], v214 offset:512\n"
"v_mfma_f32_16x16x32_bf16 v[60:63], a[212:215], v[36:39], v[60:63]\n"
"ds_read_b128 a[212:215], v208 offset:4608\n"
"ds_read_b64_tr_b16 a[238:239], v214 offset:4608\n"
"v_mfma_f32_16x16x32_bf16 v[60:63], a[216:219], v[40:43], v[60:63]\n"
"ds_read_b128 a[216:219], v208 offset:5120\n"
"ds_read_b64_tr_b16 a[240:241], v211 offset:1024\n"
"v_mfma_f32_16x16x32_bf16 v[60:63], a[220:223], v[44:47], v[60:63]\n"
"ds_read_b128 a[220:223], v208 offset:5632\n"
"ds_read_b64_tr_b16 a[242:243], v211 offset:5120\n"
"s_add_u32 m0, s77, 0x1e000\n"
"v_mfma_f32_16x16x32_bf16 v[196:199], v[204:207], v[48:51], v[196:199]\n"
"buffer_load_dwordx4 v220, s[48:51], s79 offen nt lds\n"
"v_mfma_f32_16x16x32_bf16 v[100:103], a[224:227], v[48:51], v[100:103]\n"
"v_exp_f32 v56, v56\n"
"v_mfma_f32_16x16x32_bf16 v[112:115], a[228:231], v[48:51], v[112:115]\n"
"v_exp_f32 v57, v57\n"
"v_mfma_f32_16x16x32_bf16 v[124:127], a[232:235], v[48:51], v[124:127]\n"
"v_exp_f32 v58, v58\n"
"v_mfma_f32_16x16x32_bf16 v[136:139], v[76:79], v[48:51], v[136:139]\n"
"v_exp_f32 v59, v59\n"
"v_mfma_f32_16x16x32_bf16 v[148:151], v[80:83], v[48:51], v[148:151]\n"
"v_exp_f32 v60, v60\n"
"v_mfma_f32_16x16x32_bf16 v[160:163], v[84:87], v[48:51], v[160:163]\n"
"v_exp_f32 v61, v61\n"
"v_mfma_f32_16x16x32_bf16 v[172:175], v[88:91], v[48:51], v[172:175]\n"
"v_exp_f32 v62, v62\n"
"v_mfma_f32_16x16x32_bf16 v[184:187], v[92:95], v[48:51], v[184:187]\n"
"v_exp_f32 v63, v63\n"
"s_waitcnt lgkmcnt(0)\n"
"v_mfma_f32_16x16x32_bf16 v[48:51], a[192:195], v[0:3], v[64:67]\n"
"v_cvt_pk_bf16_f32 v56, v56, v57\n"
"v_cvt_pk_bf16_f32 v57, v58, v59\n"
"v_mfma_f32_16x16x32_bf16 v[48:51], a[196:199], v[4:7], v[48:51]\n"
"v_cvt_pk_bf16_f32 v58, v60, v61\n"
"v_cvt_pk_bf16_f32 v59, v62, v63\n"
"v_mfma_f32_16x16x32_bf16 v[48:51], a[200:203], v[8:11], v[48:51]\n"
"ds_read_b64_tr_b16 a[244:245], v214 offset:1024\n"
"ds_read_b64_tr_b16 a[246:247], v214 offset:5120\n"
"v_mfma_f32_16x16x32_bf16 v[48:51], a[204:207], v[12:15], v[48:51]\n"
"ds_read_b64_tr_b16 a[248:249], v211 offset:1536\n"
"ds_read_b64_tr_b16 a[250:251], v211 offset:5632\n"
"v_mfma_f32_16x16x32_bf16 v[52:55], a[208:211], v[0:3], v[64:67]\n"
"ds_read_b64_tr_b16 a[252:253], v214 offset:1536\n"
"ds_read_b64_tr_b16 a[254:255], v214 offset:5632\n"
"v_mfma_f32_16x16x32_bf16 v[52:55], a[212:215], v[4:7], v[52:55]\n"
"buffer_load_dwordx4 v220, s[48:51], s79 offen offset:2048 nt lds\n"
"s_xor_b32 s79, s100, 0x218000\n"
"v_mfma_f32_16x16x32_bf16 v[52:55], a[216:219], v[8:11], v[52:55]\n"
"v_mfma_f32_16x16x32_bf16 v[52:55], a[220:223], v[12:15], v[52:55]\n"
"v_mfma_f32_16x16x32_bf16 v[200:203], v[204:207], v[56:59], v[200:203]\n"
"v_exp_f32 v48, v48\n"
"v_mfma_f32_16x16x32_bf16 v[104:107], a[224:227], v[56:59], v[104:107]\n"
"ds_read_b64_tr_b16 a[224:225], v211 offset:0\n"
"ds_read_b64_tr_b16 a[226:227], v211 offset:4096\n"
"v_mfma_f32_16x16x32_bf16 v[116:119], a[228:231], v[56:59], v[116:119]\n"
"ds_read_b64_tr_b16 a[228:229], v214 offset:0\n"
"ds_read_b64_tr_b16 a[230:231], v214 offset:4096\n"
"v_mfma_f32_16x16x32_bf16 v[128:131], a[232:235], v[56:59], v[128:131]\n"
"ds_read_b64_tr_b16 a[232:233], v211 offset:512\n"
"ds_read_b64_tr_b16 a[234:235], v211 offset:4608\n"
"v_mfma_f32_16x16x32_bf16 v[140:143], v[76:79], v[56:59], v[140:143]\n"
"v_exp_f32 v49, v49\n"
"v_mfma_f32_16x16x32_bf16 v[152:155], v[80:83], v[56:59], v[152:155]\n"
"v_exp_f32 v50, v50\n"
"v_mfma_f32_16x16x32_bf16 v[164:167], v[84:87], v[56:59], v[164:167]\n"
"v_exp_f32 v51, v51\n"
"v_mfma_f32_16x16x32_bf16 v[176:179], v[88:91], v[56:59], v[176:179]\n"
"v_exp_f32 v52, v52\n"
"v_mfma_f32_16x16x32_bf16 v[188:191], v[92:95], v[56:59], v[188:191]\n"
"v_exp_f32 v53, v53\n"
"v_mfma_f32_16x16x32_bf16 v[56:59], a[192:195], v[16:19], v[68:71]\n"
"v_exp_f32 v54, v54\n"
"v_mfma_f32_16x16x32_bf16 v[56:59], a[196:199], v[20:23], v[56:59]\n"
"v_exp_f32 v55, v55\n"
"v_mfma_f32_16x16x32_bf16 v[56:59], a[200:203], v[24:27], v[56:59]\n"
"v_cvt_pk_bf16_f32 v48, v48, v49\n"
"v_cvt_pk_bf16_f32 v49, v50, v51\n"
"v_mfma_f32_16x16x32_bf16 v[56:59], a[204:207], v[28:31], v[56:59]\n"
"v_cvt_pk_bf16_f32 v50, v52, v53\n"
"v_cvt_pk_bf16_f32 v51, v54, v55\n"
"s_add_u32 m0, s77, 0x20000\n"
"v_mfma_f32_16x16x32_bf16 v[60:63], a[208:211], v[16:19], v[68:71]\n"
"buffer_load_dwordx4 v220, s[44:47], s79 offen nt lds\n"
"v_mfma_f32_16x16x32_bf16 v[60:63], a[212:215], v[20:23], v[60:63]\n"
"v_mfma_f32_16x16x32_bf16 v[60:63], a[216:219], v[24:27], v[60:63]\n"
"v_mfma_f32_16x16x32_bf16 v[60:63], a[220:223], v[28:31], v[60:63]\n"
"v_mfma_f32_16x16x32_bf16 v[192:195], v[204:207], v[48:51], v[192:195]\n"
"v_exp_f32 v56, v56\n"
"s_waitcnt lgkmcnt(0)\n"
"v_mfma_f32_16x16x32_bf16 v[96:99], a[224:227], v[48:51], v[96:99]\n"
"v_exp_f32 v57, v57\n"
"v_mfma_f32_16x16x32_bf16 v[108:111], a[228:231], v[48:51], v[108:111]\n"
"v_exp_f32 v58, v58\n"
"v_mfma_f32_16x16x32_bf16 v[120:123], a[232:235], v[48:51], v[120:123]\n"
"v_exp_f32 v59, v59\n"
"v_mfma_f32_16x16x32_bf16 v[132:135], a[236:239], v[48:51], v[132:135]\n"
"v_exp_f32 v60, v60\n"
"v_mfma_f32_16x16x32_bf16 v[144:147], a[240:243], v[48:51], v[144:147]\n"
"v_exp_f32 v61, v61\n"
"v_mfma_f32_16x16x32_bf16 v[156:159], a[244:247], v[48:51], v[156:159]\n"
"v_exp_f32 v62, v62\n"
"v_mfma_f32_16x16x32_bf16 v[168:171], a[248:251], v[48:51], v[168:171]\n"
"v_exp_f32 v63, v63\n"
"v_mfma_f32_16x16x32_bf16 v[180:183], a[252:255], v[48:51], v[180:183]\n"
"s_waitcnt vmcnt(20)\n"
"s_barrier\n"
"v_mfma_f32_16x16x32_bf16 v[48:51], a[192:195], v[32:35], v[72:75]\n"
"s_xor_b32 s81, s37, 0x0\n"
"v_cvt_pk_bf16_f32 v56, v56, v57\n"
"v_mfma_f32_16x16x32_bf16 v[48:51], a[196:199], v[36:39], v[48:51]\n"
"v_cvt_pk_bf16_f32 v57, v58, v59\n"
"v_cvt_pk_bf16_f32 v58, v60, v61\n"
"v_mfma_f32_16x16x32_bf16 v[48:51], a[200:203], v[40:43], v[48:51]\n"
"v_cvt_pk_bf16_f32 v59, v62, v63\n"
"ds_read_b128 a[192:195], v208 offset:32768\n"
"v_mfma_f32_16x16x32_bf16 v[48:51], a[204:207], v[44:47], v[48:51]\n"
"ds_read_b128 a[196:199], v208 offset:33280\n"
"ds_read_b128 a[200:203], v208 offset:33792\n"
"v_mfma_f32_16x16x32_bf16 v[52:55], a[208:211], v[32:35], v[72:75]\n"
"ds_read_b128 a[204:207], v208 offset:34304\n"
"ds_read_b128 a[208:211], v208 offset:36864\n"
"v_mfma_f32_16x16x32_bf16 v[52:55], a[212:215], v[36:39], v[52:55]\n"
"buffer_load_dwordx4 v220, s[44:47], s79 offen offset:2048 nt lds\n"
"ds_read_b128 a[212:215], v208 offset:37376\n"
"v_mfma_f32_16x16x32_bf16 v[52:55], a[216:219], v[40:43], v[52:55]\n"
"ds_read_b128 a[216:219], v208 offset:37888\n"
"ds_read_b64_tr_b16 v[76:77], v214 offset:33280\n"
"v_mfma_f32_16x16x32_bf16 v[52:55], a[220:223], v[44:47], v[52:55]\n"
"ds_read_b128 a[220:223], v208 offset:38400\n"
"ds_read_b64_tr_b16 v[78:79], v214 offset:37376\n"
"v_mfma_f32_16x16x32_bf16 v[196:199], v[204:207], v[56:59], v[196:199]\n"
"v_exp_f32 v48, v48\n"
"v_mfma_f32_16x16x32_bf16 v[100:103], a[224:227], v[56:59], v[100:103]\n"
"v_exp_f32 v49, v49\n"
"v_mfma_f32_16x16x32_bf16 v[112:115], a[228:231], v[56:59], v[112:115]\n"
"v_exp_f32 v50, v50\n"
"v_mfma_f32_16x16x32_bf16 v[124:127], a[232:235], v[56:59], v[124:127]\n"
"v_exp_f32 v51, v51\n"
"v_mfma_f32_16x16x32_bf16 v[136:139], a[236:239], v[56:59], v[136:139]\n"
"v_exp_f32 v52, v52\n"
"v_mfma_f32_16x16x32_bf16 v[148:151], a[240:243], v[56:59], v[148:151]\n"
"v_exp_f32 v53, v53\n"
"v_mfma_f32_16x16x32_bf16 v[160:163], a[244:247], v[56:59], v[160:163]\n"
"v_exp_f32 v54, v54\n"
"v_mfma_f32_16x16x32_bf16 v[172:175], a[248:251], v[56:59], v[172:175]\n"
"v_exp_f32 v55, v55\n"
"v_mfma_f32_16x16x32_bf16 v[184:187], a[252:255], v[56:59], v[184:187]\n"
"v_cvt_pk_bf16_f32 v48, v48, v49\n"
"v_cvt_pk_bf16_f32 v49, v50, v51\n"
"s_waitcnt lgkmcnt(0)\n"
"v_mfma_f32_16x16x32_bf16 v[56:59], a[192:195], v[0:3], v[64:67]\n"
"v_cvt_pk_bf16_f32 v50, v52, v53\n"
"v_cvt_pk_bf16_f32 v51, v54, v55\n"
"v_mfma_f32_16x16x32_bf16 v[56:59], a[196:199], v[4:7], v[56:59]\n"
"ds_read_b64_tr_b16 v[80:81], v211 offset:33792\n"
"ds_read_b64_tr_b16 v[82:83], v211 offset:37888\n"
"s_add_u32 m0, s77, 0x22000\n"
"v_mfma_f32_16x16x32_bf16 v[56:59], a[200:203], v[8:11], v[56:59]\n"
"buffer_load_dwordx4 v220, s[48:51], s79 offen nt lds\n"
"v_mfma_f32_16x16x32_bf16 v[56:59], a[204:207], v[12:15], v[56:59]\n"
"ds_read_b64_tr_b16 v[84:85], v214 offset:33792\n"
"ds_read_b64_tr_b16 v[86:87], v214 offset:37888\n"
"v_mfma_f32_16x16x32_bf16 v[60:63], a[208:211], v[0:3], v[64:67]\n"
"ds_read_b64_tr_b16 v[88:89], v211 offset:34304\n"
"ds_read_b64_tr_b16 v[90:91], v211 offset:38400\n"
"v_mfma_f32_16x16x32_bf16 v[60:63], a[212:215], v[4:7], v[60:63]\n"
"ds_read_b64_tr_b16 v[92:93], v214 offset:34304\n"
"ds_read_b64_tr_b16 v[94:95], v214 offset:38400\n"
"v_mfma_f32_16x16x32_bf16 v[60:63], a[216:219], v[8:11], v[60:63]\n"
"v_mfma_f32_16x16x32_bf16 v[60:63], a[220:223], v[12:15], v[60:63]\n"
"v_mfma_f32_16x16x32_bf16 v[200:203], v[204:207], v[48:51], v[200:203]\n"
"v_exp_f32 v56, v56\n"
"v_mfma_f32_16x16x32_bf16 v[104:107], a[224:227], v[48:51], v[104:107]\n"
"ds_read_b64_tr_b16 a[224:225], v211 offset:32768\n"
"ds_read_b64_tr_b16 a[226:227], v211 offset:36864\n"
"v_mfma_f32_16x16x32_bf16 v[116:119], a[228:231], v[48:51], v[116:119]\n"
"ds_read_b64_tr_b16 a[228:229], v214 offset:32768\n"
"ds_read_b64_tr_b16 a[230:231], v214 offset:36864\n"
"v_mfma_f32_16x16x32_bf16 v[128:131], a[232:235], v[48:51], v[128:131]\n"
"ds_read_b64_tr_b16 a[232:233], v211 offset:33280\n"
"ds_read_b64_tr_b16 a[234:235], v211 offset:37376\n"
"v_mfma_f32_16x16x32_bf16 v[140:143], a[236:239], v[48:51], v[140:143]\n"
"v_exp_f32 v57, v57\n"
"v_mfma_f32_16x16x32_bf16 v[152:155], a[240:243], v[48:51], v[152:155]\n"
"v_exp_f32 v58, v58\n"
"v_mfma_f32_16x16x32_bf16 v[164:167], a[244:247], v[48:51], v[164:167]\n"
"v_exp_f32 v59, v59\n"
"v_mfma_f32_16x16x32_bf16 v[176:179], a[248:251], v[48:51], v[176:179]\n"
"v_exp_f32 v60, v60\n"
"v_mfma_f32_16x16x32_bf16 v[188:191], a[252:255], v[48:51], v[188:191]\n"
"v_exp_f32 v61, v61\n"
"v_mfma_f32_16x16x32_bf16 v[48:51], a[192:195], v[16:19], v[68:71]\n"
"v_exp_f32 v62, v62\n"
"v_mfma_f32_16x16x32_bf16 v[48:51], a[196:199], v[20:23], v[48:51]\n"
"v_exp_f32 v63, v63\n"
"v_mfma_f32_16x16x32_bf16 v[48:51], a[200:203], v[24:27], v[48:51]\n"
"v_cvt_pk_bf16_f32 v56, v56, v57\n"
"v_cvt_pk_bf16_f32 v57, v58, v59\n"
"v_mfma_f32_16x16x32_bf16 v[48:51], a[204:207], v[28:31], v[48:51]\n"
"v_cvt_pk_bf16_f32 v58, v60, v61\n"
"v_cvt_pk_bf16_f32 v59, v62, v63\n"
"v_mfma_f32_16x16x32_bf16 v[52:55], a[208:211], v[16:19], v[68:71]\n"
"buffer_load_dwordx4 v220, s[48:51], s79 offen offset:2048 nt lds\n"
"s_xor_b32 s79, s100, 0x21a000\n"
"v_mfma_f32_16x16x32_bf16 v[52:55], a[212:215], v[20:23], v[52:55]\n"
"v_mfma_f32_16x16x32_bf16 v[52:55], a[216:219], v[24:27], v[52:55]\n"
"v_mfma_f32_16x16x32_bf16 v[52:55], a[220:223], v[28:31], v[52:55]\n"
"v_mfma_f32_16x16x32_bf16 v[192:195], v[204:207], v[56:59], v[192:195]\n"
"v_exp_f32 v48, v48\n"
"s_waitcnt lgkmcnt(0)\n"
"v_mfma_f32_16x16x32_bf16 v[96:99], a[224:227], v[56:59], v[96:99]\n"
"v_exp_f32 v49, v49\n"
"v_mfma_f32_16x16x32_bf16 v[108:111], a[228:231], v[56:59], v[108:111]\n"
"v_exp_f32 v50, v50\n"
"v_mfma_f32_16x16x32_bf16 v[120:123], a[232:235], v[56:59], v[120:123]\n"
"v_exp_f32 v51, v51\n"
"v_mfma_f32_16x16x32_bf16 v[132:135], v[76:79], v[56:59], v[132:135]\n"
"v_exp_f32 v52, v52\n"
"v_mfma_f32_16x16x32_bf16 v[144:147], v[80:83], v[56:59], v[144:147]\n"
"v_exp_f32 v53, v53\n"
"v_mfma_f32_16x16x32_bf16 v[156:159], v[84:87], v[56:59], v[156:159]\n"
"v_exp_f32 v54, v54\n"
"v_mfma_f32_16x16x32_bf16 v[168:171], v[88:91], v[56:59], v[168:171]\n"
"v_exp_f32 v55, v55\n"
"v_mfma_f32_16x16x32_bf16 v[180:183], v[92:95], v[56:59], v[180:183]\n"
"s_waitcnt vmcnt(18)\n"
"s_barrier\n"
"v_mfma_f32_16x16x32_bf16 v[56:59], a[192:195], v[32:35], v[72:75]\n"
"v_cvt_pk_bf16_f32 v48, v48, v49\n"
"v_cvt_pk_bf16_f32 v49, v50, v51\n"
"v_mfma_f32_16x16x32_bf16 v[56:59], a[196:199], v[36:39], v[56:59]\n"
"v_cvt_pk_bf16_f32 v50, v52, v53\n"
"v_cvt_pk_bf16_f32 v51, v54, v55\n"
"v_mfma_f32_16x16x32_bf16 v[56:59], a[200:203], v[40:43], v[56:59]\n"
"ds_read_b128 a[192:195], v209 offset:16384\n"
"ds_read_b128 a[196:199], v209 offset:16896\n"
"v_mfma_f32_16x16x32_bf16 v[56:59], a[204:207], v[44:47], v[56:59]\n"
"ds_read_b128 a[200:203], v209 offset:17408\n"
"ds_read_b128 a[204:207], v209 offset:17920\n"
"v_mfma_f32_16x16x32_bf16 v[60:63], a[208:211], v[32:35], v[72:75]\n"
"ds_read_b128 a[208:211], v209 offset:20480\n"
"ds_read_b64_tr_b16 a[236:237], v215 offset:16896\n"
"s_add_u32 m0, s77, 0x24000\n"
"v_mfma_f32_16x16x32_bf16 v[60:63], a[212:215], v[36:39], v[60:63]\n"
"buffer_load_dwordx4 v220, s[44:47], s79 offen nt lds\n"
"v_mfma_f32_16x16x32_bf16 v[60:63], a[216:219], v[40:43], v[60:63]\n"
"ds_read_b128 a[212:215], v209 offset:20992\n"
"ds_read_b128 a[216:219], v209 offset:21504\n"
"v_mfma_f32_16x16x32_bf16 v[60:63], a[220:223], v[44:47], v[60:63]\n"
"ds_read_b128 a[220:223], v209 offset:22016\n"
"ds_read_b64_tr_b16 a[238:239], v215 offset:20992\n"
"v_mfma_f32_16x16x32_bf16 v[196:199], v[204:207], v[48:51], v[196:199]\n"
"v_exp_f32 v56, v56\n"
"v_mfma_f32_16x16x32_bf16 v[100:103], a[224:227], v[48:51], v[100:103]\n"
"v_exp_f32 v57, v57\n"
"v_mfma_f32_16x16x32_bf16 v[112:115], a[228:231], v[48:51], v[112:115]\n"
"v_exp_f32 v58, v58\n"
"v_mfma_f32_16x16x32_bf16 v[124:127], a[232:235], v[48:51], v[124:127]\n"
"v_exp_f32 v59, v59\n"
"v_mfma_f32_16x16x32_bf16 v[136:139], v[76:79], v[48:51], v[136:139]\n"
"v_exp_f32 v60, v60\n"
"v_mfma_f32_16x16x32_bf16 v[148:151], v[80:83], v[48:51], v[148:151]\n"
"v_exp_f32 v61, v61\n"
"v_mfma_f32_16x16x32_bf16 v[160:163], v[84:87], v[48:51], v[160:163]\n"
"v_exp_f32 v62, v62\n"
"v_mfma_f32_16x16x32_bf16 v[172:175], v[88:91], v[48:51], v[172:175]\n"
"v_exp_f32 v63, v63\n"
"v_mfma_f32_16x16x32_bf16 v[184:187], v[92:95], v[48:51], v[184:187]\n"
"v_cvt_pk_bf16_f32 v56, v56, v57\n"
"v_cvt_pk_bf16_f32 v57, v58, v59\n"
"s_waitcnt lgkmcnt(0)\n"
"v_mfma_f32_16x16x32_bf16 v[48:51], a[192:195], v[0:3], v[64:67]\n"
"v_cvt_pk_bf16_f32 v58, v60, v61\n"
"v_cvt_pk_bf16_f32 v59, v62, v63\n"
"v_mfma_f32_16x16x32_bf16 v[48:51], a[196:199], v[4:7], v[48:51]\n"
"ds_read_b64_tr_b16 a[240:241], v212 offset:17408\n"
"ds_read_b64_tr_b16 a[242:243], v212 offset:21504\n"
"v_mfma_f32_16x16x32_bf16 v[48:51], a[200:203], v[8:11], v[48:51]\n"
"buffer_load_dwordx4 v220, s[44:47], s79 offen offset:2048 nt lds\n"
"ds_read_b64_tr_b16 a[244:245], v215 offset:17408\n"
"v_mfma_f32_16x16x32_bf16 v[48:51], a[204:207], v[12:15], v[48:51]\n"
"ds_read_b64_tr_b16 a[246:247], v215 offset:21504\n"
"ds_read_b64_tr_b16 a[248:249], v212 offset:17920\n"
"v_mfma_f32_16x16x32_bf16 v[52:55], a[208:211], v[0:3], v[64:67]\n"
"s_waitcnt vmcnt(9)\n"
"v_cmp_ne_u64 vcc, s[90:91], v[232:233]\n"
"ds_read_b64_tr_b16 a[250:251], v212 offset:22016\n"
"v_mfma_f32_16x16x32_bf16 v[52:55], a[212:215], v[4:7], v[52:55]\n"
"ds_read_b64_tr_b16 a[252:253], v215 offset:17920\n"
"ds_read_b64_tr_b16 a[254:255], v215 offset:22016\n"
"v_mfma_f32_16x16x32_bf16 v[52:55], a[216:219], v[8:11], v[52:55]\n"
"v_mfma_f32_16x16x32_bf16 v[52:55], a[220:223], v[12:15], v[52:55]\n"
"s_cmp_lg_u32 s76, 0\n"
"s_cbranch_scc1 LretA_%=\n"
"s_cbranch_vccnz LspinA_%=\n"
"buffer_inv sc1\n"
"LretA_%=:\n"
"v_mfma_f32_16x16x32_bf16 v[200:203], v[204:207], v[56:59], v[200:203]\n"
"v_exp_f32 v48, v48\n"
"v_mfma_f32_16x16x32_bf16 v[104:107], a[224:227], v[56:59], v[104:107]\n"
"ds_read_b64_tr_b16 a[224:225], v212 offset:16384\n"
"ds_read_b64_tr_b16 a[226:227], v212 offset:20480\n"
"v_mfma_f32_16x16x32_bf16 v[116:119], a[228:231], v[56:59], v[116:119]\n"
"ds_read_b64_tr_b16 a[228:229], v215 offset:16384\n"
"ds_read_b64_tr_b16 a[230:231], v215 offset:20480\n"
"v_mfma_f32_16x16x32_bf16 v[128:131], a[232:235], v[56:59], v[128:131]\n"
"ds_read_b64_tr_b16 a[232:233], v212 offset:16896\n"
"ds_read_b64_tr_b16 a[234:235], v212 offset:20992\n"
"v_mfma_f32_16x16x32_bf16 v[140:143], v[76:79], v[56:59], v[140:143]\n"
"v_exp_f32 v49, v49\n"
"v_mfma_f32_16x16x32_bf16 v[152:155], v[80:83], v[56:59], v[152:155]\n"
"v_exp_f32 v50, v50\n"
"v_mfma_f32_16x16x32_bf16 v[164:167], v[84:87], v[56:59], v[164:167]\n"
"v_exp_f32 v51, v51\n"
"v_mfma_f32_16x16x32_bf16 v[176:179], v[88:91], v[56:59], v[176:179]\n"
"v_exp_f32 v52, v52\n"
"v_mfma_f32_16x16x32_bf16 v[188:191], v[92:95], v[56:59], v[188:191]\n"
"v_exp_f32 v53, v53\n"
"v_mfma_f32_16x16x32_bf16 v[56:59], a[192:195], v[16:19], v[68:71]\n"
"v_exp_f32 v54, v54\n"
"v_mfma_f32_16x16x32_bf16 v[56:59], a[196:199], v[20:23], v[56:59]\n"
"v_exp_f32 v55, v55\n"
"v_mfma_f32_16x16x32_bf16 v[56:59], a[200:203], v[24:27], v[56:59]\n"
"v_cvt_pk_bf16_f32 v48, v48, v49\n"
"v_cvt_pk_bf16_f32 v49, v50, v51\n"
"v_mfma_f32_16x16x32_bf16 v[56:59], a[204:207], v[28:31], v[56:59]\n"
"v_cvt_pk_bf16_f32 v50, v52, v53\n"
"v_cvt_pk_bf16_f32 v51, v54, v55\n"
"s_add_u32 m0, s77, 0x26000\n"
"v_mfma_f32_16x16x32_bf16 v[60:63], a[208:211], v[16:19], v[68:71]\n"
"buffer_load_dwordx4 v220, s[48:51], s79 offen nt lds\n"
"v_mfma_f32_16x16x32_bf16 v[60:63], a[212:215], v[20:23], v[60:63]\n"
"v_lshrrev_b32 v225, 1, v223\n"
"v_and_b32 v225, 0x18, v225\n"
"v_mfma_f32_16x16x32_bf16 v[60:63], a[216:219], v[24:27], v[60:63]\n"
"v_mfma_f32_16x16x32_bf16 v[60:63], a[220:223], v[28:31], v[60:63]\n"
"v_mfma_f32_16x16x32_bf16 v[192:195], v[204:207], v[48:51], v[192:195]\n"
"v_exp_f32 v56, v56\n"
"s_waitcnt lgkmcnt(0)\n"
"v_mfma_f32_16x16x32_bf16 v[96:99], a[224:227], v[48:51], v[96:99]\n"
"v_exp_f32 v57, v57\n"
"v_mfma_f32_16x16x32_bf16 v[108:111], a[228:231], v[48:51], v[108:111]\n"
"v_exp_f32 v58, v58\n"
"v_mfma_f32_16x16x32_bf16 v[120:123], a[232:235], v[48:51], v[120:123]\n"
"v_exp_f32 v59, v59\n"
"v_mfma_f32_16x16x32_bf16 v[132:135], a[236:239], v[48:51], v[132:135]\n"
"v_exp_f32 v60, v60\n"
"v_mfma_f32_16x16x32_bf16 v[144:147], a[240:243], v[48:51], v[144:147]\n"
"v_exp_f32 v61, v61\n"
"v_mfma_f32_16x16x32_bf16 v[156:159], a[244:247], v[48:51], v[156:159]\n"
"v_exp_f32 v62, v62\n"
"v_mfma_f32_16x16x32_bf16 v[168:171], a[248:251], v[48:51], v[168:171]\n"
"v_exp_f32 v63, v63\n"
"v_mfma_f32_16x16x32_bf16 v[180:183], a[252:255], v[48:51], v[180:183]\n"
"s_barrier\n"
"v_mfma_f32_16x16x32_bf16 v[48:51], a[192:195], v[32:35], v[72:75]\n"
"v_cvt_pk_bf16_f32 v56, v56, v57\n"
"v_cvt_pk_bf16_f32 v57, v58, v59\n"
"v_mfma_f32_16x16x32_bf16 v[48:51], a[196:199], v[36:39], v[48:51]\n"
"v_cvt_pk_bf16_f32 v58, v60, v61\n"
"v_cvt_pk_bf16_f32 v59, v62, v63\n"
"v_mfma_f32_16x16x32_bf16 v[48:51], a[200:203], v[40:43], v[48:51]\n"
"ds_read_b128 a[192:195], v209 offset:32768\n"
"ds_read_b128 a[196:199], v209 offset:33280\n"
"v_mfma_f32_16x16x32_bf16 v[48:51], a[204:207], v[44:47], v[48:51]\n"
"ds_read_b128 a[200:203], v209 offset:33792\n"
"ds_read_b128 a[204:207], v209 offset:34304\n"
"v_mfma_f32_16x16x32_bf16 v[52:55], a[208:211], v[32:35], v[72:75]\n"
"ds_read_b128 a[208:211], v209 offset:36864\n"
"buffer_load_dwordx4 v220, s[48:51], s79 offen offset:2048 nt lds\n"
"v_mfma_f32_16x16x32_bf16 v[52:55], a[212:215], v[36:39], v[52:55]\n"
"s_xor_b32 s79, s100, 0x21c000\n"
"ds_read_b128 a[212:215], v209 offset:37376\n"
"v_mfma_f32_16x16x32_bf16 v[52:55], a[216:219], v[40:43], v[52:55]\n"
"ds_read_b128 a[216:219], v209 offset:37888\n"
"ds_read_b64_tr_b16 v[76:77], v215 offset:33280\n"
"v_mfma_f32_16x16x32_bf16 v[52:55], a[220:223], v[44:47], v[52:55]\n"
"ds_read_b128 a[220:223], v209 offset:38400\n"
"ds_read_b64_tr_b16 v[78:79], v215 offset:37376\n"
"v_mfma_f32_16x16x32_bf16 v[196:199], v[204:207], v[56:59], v[196:199]\n"
"v_exp_f32 v48, v48\n"
"v_mfma_f32_16x16x32_bf16 v[100:103], a[224:227], v[56:59], v[100:103]\n"
"v_exp_f32 v49, v49\n"
"v_mfma_f32_16x16x32_bf16 v[112:115], a[228:231], v[56:59], v[112:115]\n"
"v_exp_f32 v50, v50\n"
"v_mfma_f32_16x16x32_bf16 v[124:127], a[232:235], v[56:59], v[124:127]\n"
"v_exp_f32 v51, v51\n"
"v_mfma_f32_16x16x32_bf16 v[136:139], a[236:239], v[56:59], v[136:139]\n"
"v_exp_f32 v52, v52\n"
"v_mfma_f32_16x16x32_bf16 v[148:151], a[240:243], v[56:59], v[148:151]\n"
"v_exp_f32 v53, v53\n"
"v_mfma_f32_16x16x32_bf16 v[160:163], a[244:247], v[56:59], v[160:163]\n"
"v_exp_f32 v54, v54\n"
"v_mfma_f32_16x16x32_bf16 v[172:175], a[248:251], v[56:59], v[172:175]\n"
"v_exp_f32 v55, v55\n"
"v_mfma_f32_16x16x32_bf16 v[184:187], a[252:255], v[56:59], v[184:187]\n"
"v_cvt_pk_bf16_f32 v48, v48, v49\n"
"v_cvt_pk_bf16_f32 v49, v50, v51\n"
"s_waitcnt lgkmcnt(0)\n"
"v_mfma_f32_16x16x32_bf16 v[56:59], a[192:195], v[0:3], v[64:67]\n"
"v_cvt_pk_bf16_f32 v50, v52, v53\n"
"v_cvt_pk_bf16_f32 v51, v54, v55\n"
"s_add_u32 m0, s77, 0x4000\n"
"v_mfma_f32_16x16x32_bf16 v[56:59], a[196:199], v[4:7], v[56:59]\n"
"buffer_load_dwordx4 v220, s[44:47], s79 offen nt lds\n"
"v_mfma_f32_16x16x32_bf16 v[56:59], a[200:203], v[8:11], v[56:59]\n"
"ds_read_b64_tr_b16 v[80:81], v212 offset:33792\n"
"ds_read_b64_tr_b16 v[82:83], v212 offset:37888\n"
"v_mfma_f32_16x16x32_bf16 v[56:59], a[204:207], v[12:15], v[56:59]\n"
"ds_read_b64_tr_b16 v[84:85], v215 offset:33792\n"
"ds_read_b64_tr_b16 v[86:87], v215 offset:37888\n"
"v_mfma_f32_16x16x32_bf16 v[60:63], a[208:211], v[0:3], v[64:67]\n"
"ds_read_b64_tr_b16 v[88:89], v212 offset:34304\n"
"ds_read_b64_tr_b16 v[90:91], v212 offset:38400\n"
"v_mfma_f32_16x16x32_bf16 v[60:63], a[212:215], v[4:7], v[60:63]\n"
"ds_read_b64_tr_b16 v[92:93], v215 offset:34304\n"
"ds_read_b64_tr_b16 v[94:95], v215 offset:38400\n"
"v_mfma_f32_16x16x32_bf16 v[60:63], a[216:219], v[8:11], v[60:63]\n"
"v_mfma_f32_16x16x32_bf16 v[60:63], a[220:223], v[12:15], v[60:63]\n"
"v_mfma_f32_16x16x32_bf16 v[200:203], v[204:207], v[48:51], v[200:203]\n"
"v_exp_f32 v56, v56\n"
"v_mfma_f32_16x16x32_bf16 v[104:107], a[224:227], v[48:51], v[104:107]\n"
"ds_read_b64_tr_b16 a[224:225], v212 offset:32768\n"
"ds_read_b64_tr_b16 a[226:227], v212 offset:36864\n"
"v_mfma_f32_16x16x32_bf16 v[116:119], a[228:231], v[48:51], v[116:119]\n"
"ds_read_b64_tr_b16 a[228:229], v215 offset:32768\n"
"ds_read_b64_tr_b16 a[230:231], v215 offset:36864\n"
"v_mfma_f32_16x16x32_bf16 v[128:131], a[232:235], v[48:51], v[128:131]\n"
"ds_read_b64_tr_b16 a[232:233], v212 offset:33280\n"
"ds_read_b64_tr_b16 a[234:235], v212 offset:37376\n"
"v_mfma_f32_16x16x32_bf16 v[140:143], a[236:239], v[48:51], v[140:143]\n"
"v_exp_f32 v57, v57\n"
"v_mfma_f32_16x16x32_bf16 v[152:155], a[240:243], v[48:51], v[152:155]\n"
"v_exp_f32 v58, v58\n"
"v_mfma_f32_16x16x32_bf16 v[164:167], a[244:247], v[48:51], v[164:167]\n"
"v_exp_f32 v59, v59\n"
"v_mfma_f32_16x16x32_bf16 v[176:179], a[248:251], v[48:51], v[176:179]\n"
"v_exp_f32 v60, v60\n"
"v_mfma_f32_16x16x32_bf16 v[188:191], a[252:255], v[48:51], v[188:191]\n"
"v_exp_f32 v61, v61\n"
"v_mfma_f32_16x16x32_bf16 v[48:51], a[192:195], v[16:19], v[68:71]\n"
"v_exp_f32 v62, v62\n"
"v_mfma_f32_16x16x32_bf16 v[48:51], a[196:199], v[20:23], v[48:51]\n"
"v_exp_f32 v63, v63\n"
"v_mfma_f32_16x16x32_bf16 v[48:51], a[200:203], v[24:27], v[48:51]\n"
"v_cvt_pk_bf16_f32 v56, v56, v57\n"
"v_cvt_pk_bf16_f32 v57, v58, v59\n"
"v_mfma_f32_16x16x32_bf16 v[48:51], a[204:207], v[28:31], v[48:51]\n"
"v_cvt_pk_bf16_f32 v58, v60, v61\n"
"v_cvt_pk_bf16_f32 v59, v62, v63\n"
"v_mfma_f32_16x16x32_bf16 v[52:55], a[208:211], v[16:19], v[68:71]\n"
"buffer_load_dwordx4 v220, s[44:47], s79 offen offset:2048 nt lds\n"
"v_mfma_f32_16x16x32_bf16 v[52:55], a[212:215], v[20:23], v[52:55]\n"
"v_mfma_f32_16x16x32_bf16 v[52:55], a[216:219], v[24:27], v[52:55]\n"
"v_mfma_f32_16x16x32_bf16 v[52:55], a[220:223], v[28:31], v[52:55]\n"
"v_mfma_f32_16x16x32_bf16 v[192:195], v[204:207], v[56:59], v[192:195]\n"
"v_exp_f32 v48, v48\n"
"s_waitcnt lgkmcnt(0)\n"
"v_mfma_f32_16x16x32_bf16 v[96:99], a[224:227], v[56:59], v[96:99]\n"
"v_exp_f32 v49, v49\n"
"v_mfma_f32_16x16x32_bf16 v[108:111], a[228:231], v[56:59], v[108:111]\n"
"v_exp_f32 v50, v50\n"
"v_mfma_f32_16x16x32_bf16 v[120:123], a[232:235], v[56:59], v[120:123]\n"
"v_exp_f32 v51, v51\n"
"v_mfma_f32_16x16x32_bf16 v[132:135], v[76:79], v[56:59], v[132:135]\n"
"v_exp_f32 v52, v52\n"
"v_mfma_f32_16x16x32_bf16 v[144:147], v[80:83], v[56:59], v[144:147]\n"
"v_exp_f32 v53, v53\n"
"v_mfma_f32_16x16x32_bf16 v[156:159], v[84:87], v[56:59], v[156:159]\n"
"v_exp_f32 v54, v54\n"
"v_mfma_f32_16x16x32_bf16 v[168:171], v[88:91], v[56:59], v[168:171]\n"
"v_exp_f32 v55, v55\n"
"v_mfma_f32_16x16x32_bf16 v[180:183], v[92:95], v[56:59], v[180:183]\n"
"s_barrier\n"
"v_mfma_f32_16x16x32_bf16 v[56:59], a[192:195], v[32:35], v[72:75]\n"
"v_cvt_pk_bf16_f32 v48, v48, v49\n"
"v_cvt_pk_bf16_f32 v49, v50, v51\n"
"v_mfma_f32_16x16x32_bf16 v[56:59], a[196:199], v[36:39], v[56:59]\n"
"v_cvt_pk_bf16_f32 v50, v52, v53\n"
"v_cvt_pk_bf16_f32 v51, v54, v55\n"
"v_mfma_f32_16x16x32_bf16 v[56:59], a[200:203], v[40:43], v[56:59]\n"
"ds_read_b128 a[192:195], v208 offset:49152\n"
"ds_read_b128 a[196:199], v208 offset:49664\n"
"v_mfma_f32_16x16x32_bf16 v[56:59], a[204:207], v[44:47], v[56:59]\n"
"ds_read_b128 a[200:203], v208 offset:50176\n"
"ds_read_b128 a[204:207], v208 offset:50688\n"
"v_mfma_f32_16x16x32_bf16 v[60:63], a[208:211], v[32:35], v[72:75]\n"
"ds_read_b128 a[208:211], v208 offset:53248\n"
"ds_read_b64_tr_b16 a[236:237], v214 offset:49664\n"
"s_add_u32 m0, s77, 0x6000\n"
"v_mfma_f32_16x16x32_bf16 v[60:63], a[212:215], v[36:39], v[60:63]\n"
"buffer_load_dwordx4 v220, s[48:51], s79 offen nt lds\n"
"v_mfma_f32_16x16x32_bf16 v[60:63], a[216:219], v[40:43], v[60:63]\n"
"ds_read_b128 a[212:215], v208 offset:53760\n"
"ds_read_b128 a[216:219], v208 offset:54272\n"
"v_mfma_f32_16x16x32_bf16 v[60:63], a[220:223], v[44:47], v[60:63]\n"
"ds_read_b128 a[220:223], v208 offset:54784\n"
"ds_read_b64_tr_b16 a[238:239], v214 offset:53760\n"
"v_mfma_f32_16x16x32_bf16 v[196:199], v[204:207], v[48:51], v[196:199]\n"
"v_exp_f32 v56, v56\n"
"v_mfma_f32_16x16x32_bf16 v[100:103], a[224:227], v[48:51], v[100:103]\n"
"v_exp_f32 v57, v57\n"
"v_mfma_f32_16x16x32_bf16 v[112:115], a[228:231], v[48:51], v[112:115]\n"
"v_exp_f32 v58, v58\n"
"v_mfma_f32_16x16x32_bf16 v[124:127], a[232:235], v[48:51], v[124:127]\n"
"v_exp_f32 v59, v59\n"
"v_mfma_f32_16x16x32_bf16 v[136:139], v[76:79], v[48:51], v[136:139]\n"
"v_exp_f32 v60, v60\n"
"v_mfma_f32_16x16x32_bf16 v[148:151], v[80:83], v[48:51], v[148:151]\n"
"v_exp_f32 v61, v61\n"
"v_mfma_f32_16x16x32_bf16 v[160:163], v[84:87], v[48:51], v[160:163]\n"
"v_exp_f32 v62, v62\n"
"v_mfma_f32_16x16x32_bf16 v[172:175], v[88:91], v[48:51], v[172:175]\n"
"v_exp_f32 v63, v63\n"
"v_mfma_f32_16x16x32_bf16 v[184:187], v[92:95], v[48:51], v[184:187]\n"
"v_cvt_pk_bf16_f32 v56, v56, v57\n"
"v_cvt_pk_bf16_f32 v57, v58, v59\n"
"s_waitcnt lgkmcnt(0)\n"
"v_mfma_f32_16x16x32_bf16 v[48:51], a[192:195], v[0:3], v[64:67]\n"
"v_cvt_pk_bf16_f32 v58, v60, v61\n"
"v_cvt_pk_bf16_f32 v59, v62, v63\n"
"v_mfma_f32_16x16x32_bf16 v[48:51], a[196:199], v[4:7], v[48:51]\n"
"ds_read_b64_tr_b16 a[240:241], v211 offset:50176\n"
"ds_read_b64_tr_b16 a[242:243], v211 offset:54272\n"
"v_mfma_f32_16x16x32_bf16 v[48:51], a[200:203], v[8:11], v[48:51]\n"
"buffer_load_dwordx4 v220, s[48:51], s79 offen offset:2048 nt lds\n"
"s_xor_b32 s79, s100, 0x21e000\n"
"v_mfma_f32_16x16x32_bf16 v[48:51], a[204:207], v[12:15], v[48:51]\n"
"ds_read_b64_tr_b16 a[244:245], v214 offset:50176\n"
"ds_read_b64_tr_b16 a[246:247], v214 offset:54272\n"
"v_mfma_f32_16x16x32_bf16 v[52:55], a[208:211], v[0:3], v[64:67]\n"
"ds_read_b64_tr_b16 a[248:249], v211 offset:50688\n"
"ds_read_b64_tr_b16 a[250:251], v211 offset:54784\n"
"v_mfma_f32_16x16x32_bf16 v[52:55], a[212:215], v[4:7], v[52:55]\n"
"ds_read_b64_tr_b16 a[252:253], v214 offset:50688\n"
"ds_read_b64_tr_b16 a[254:255], v214 offset:54784\n"
"v_mfma_f32_16x16x32_bf16 v[52:55], a[216:219], v[8:11], v[52:55]\n"
"v_mfma_f32_16x16x32_bf16 v[52:55], a[220:223], v[12:15], v[52:55]\n"
"v_mfma_f32_16x16x32_bf16 v[200:203], v[204:207], v[56:59], v[200:203]\n"
"v_exp_f32 v48, v48\n"
"v_mfma_f32_16x16x32_bf16 v[104:107], a[224:227], v[56:59], v[104:107]\n"
"ds_read_b64_tr_b16 a[224:225], v211 offset:49152\n"
"ds_read_b64_tr_b16 a[226:227], v211 offset:53248\n"
"v_mfma_f32_16x16x32_bf16 v[116:119], a[228:231], v[56:59], v[116:119]\n"
"ds_read_b64_tr_b16 a[228:229], v214 offset:49152\n"
"ds_read_b64_tr_b16 a[230:231], v214 offset:53248\n"
"v_mfma_f32_16x16x32_bf16 v[128:131], a[232:235], v[56:59], v[128:131]\n"
"ds_read_b64_tr_b16 a[232:233], v211 offset:49664\n"
"ds_read_b64_tr_b16 a[234:235], v211 offset:53760\n"
"v_mfma_f32_16x16x32_bf16 v[140:143], v[76:79], v[56:59], v[140:143]\n"
"v_exp_f32 v49, v49\n"
"v_mfma_f32_16x16x32_bf16 v[152:155], v[80:83], v[56:59], v[152:155]\n"
"v_exp_f32 v50, v50\n"
"v_mfma_f32_16x16x32_bf16 v[164:167], v[84:87], v[56:59], v[164:167]\n"
"v_exp_f32 v51, v51\n"
"v_mfma_f32_16x16x32_bf16 v[176:179], v[88:91], v[56:59], v[176:179]\n"
"v_exp_f32 v52, v52\n"
"v_mfma_f32_16x16x32_bf16 v[188:191], v[92:95], v[56:59], v[188:191]\n"
"v_exp_f32 v53, v53\n"
"v_mfma_f32_16x16x32_bf16 v[56:59], a[192:195], v[16:19], v[68:71]\n"
"v_exp_f32 v54, v54\n"
"v_mfma_f32_16x16x32_bf16 v[56:59], a[196:199], v[20:23], v[56:59]\n"
"v_exp_f32 v55, v55\n"
"v_mfma_f32_16x16x32_bf16 v[56:59], a[200:203], v[24:27], v[56:59]\n"
"v_cvt_pk_bf16_f32 v48, v48, v49\n"
"v_cvt_pk_bf16_f32 v49, v50, v51\n"
"v_mfma_f32_16x16x32_bf16 v[56:59], a[204:207], v[28:31], v[56:59]\n"
"v_cvt_pk_bf16_f32 v50, v52, v53\n"
"v_cvt_pk_bf16_f32 v51, v54, v55\n"
"s_add_u32 m0, s77, 0x10000\n"
"v_mfma_f32_16x16x32_bf16 v[60:63], a[208:211], v[16:19], v[68:71]\n"
"buffer_load_dwordx4 v220, s[44:47], s79 offen nt lds\n"
"v_mfma_f32_16x16x32_bf16 v[60:63], a[212:215], v[20:23], v[60:63]\n"
"v_mfma_f32_16x16x32_bf16 v[60:63], a[216:219], v[24:27], v[60:63]\n"
"v_mfma_f32_16x16x32_bf16 v[60:63], a[220:223], v[28:31], v[60:63]\n"
"v_mfma_f32_16x16x32_bf16 v[192:195], v[204:207], v[48:51], v[192:195]\n"
"v_exp_f32 v56, v56\n"
"s_waitcnt lgkmcnt(0)\n"
"v_mfma_f32_16x16x32_bf16 v[96:99], a[224:227], v[48:51], v[96:99]\n"
"v_exp_f32 v57, v57\n"
"v_mfma_f32_16x16x32_bf16 v[108:111], a[228:231], v[48:51], v[108:111]\n"
"v_exp_f32 v58, v58\n"
"v_mfma_f32_16x16x32_bf16 v[120:123], a[232:235], v[48:51], v[120:123]\n"
"v_exp_f32 v59, v59\n"
"v_mfma_f32_16x16x32_bf16 v[132:135], a[236:239], v[48:51], v[132:135]\n"
"v_exp_f32 v60, v60\n"
"v_mfma_f32_16x16x32_bf16 v[144:147], a[240:243], v[48:51], v[144:147]\n"
"v_exp_f32 v61, v61\n"
"v_mfma_f32_16x16x32_bf16 v[156:159], a[244:247], v[48:51], v[156:159]\n"
"v_exp_f32 v62, v62\n"
"v_mfma_f32_16x16x32_bf16 v[168:171], a[248:251], v[48:51], v[168:171]\n"
"v_exp_f32 v63, v63\n"
"v_mfma_f32_16x16x32_bf16 v[180:183], a[252:255], v[48:51], v[180:183]\n"
"s_waitcnt vmcnt(13)\n"
"s_barrier\n"
"v_mfma_f32_16x16x32_bf16 v[48:51], a[192:195], v[32:35], v[72:75]\n"
"v_cvt_pk_bf16_f32 v56, v56, v57\n"
"v_cvt_pk_bf16_f32 v57, v58, v59\n"
"v_mfma_f32_16x16x32_bf16 v[48:51], a[196:199], v[36:39], v[48:51]\n"
"v_cvt_pk_bf16_f32 v58, v60, v61\n"
"v_cvt_pk_bf16_f32 v59, v62, v63\n"
"v_mfma_f32_16x16x32_bf16 v[48:51], a[200:203], v[40:43], v[48:51]\n"
"ds_read_b128 a[192:195], v209 offset:49152\n"
"ds_read_b128 a[196:199], v209 offset:49664\n"
"v_mfma_f32_16x16x32_bf16 v[48:51], a[204:207], v[44:47], v[48:51]\n"
"ds_read_b128 a[200:203], v209 offset:50176\n"
"ds_read_b128 a[204:207], v209 offset:50688\n"
"v_mfma_f32_16x16x32_bf16 v[52:55], a[208:211], v[32:35], v[72:75]\n"
"ds_read_b128 a[208:211], v209 offset:53248\n"
"buffer_load_dwordx4 v220, s[44:47], s79 offen offset:2048 nt lds\n"
"v_mfma_f32_16x16x32_bf16 v[52:55], a[212:215], v[36:39], v[52:55]\n"
"ds_read_b128 a[212:215], v209 offset:53760\n"
"ds_read_b64_tr_b16 v[76:77], v215 offset:49664\n"
"v_mfma_f32_16x16x32_bf16 v[52:55], a[216:219], v[40:43], v[52:55]\n"
"ds_read_b128 a[216:219], v209 offset:54272\n"
"ds_read_b64_tr_b16 v[78:79], v215 offset:53760\n"
"v_mfma_f32_16x16x32_bf16 v[52:55], a[220:223], v[44:47], v[52:55]\n"
"ds_read_b128 a[220:223], v209 offset:54784\n"
"ds_read_b64_tr_b16 v[80:81], v212 offset:50176\n"
"v_mfma_f32_16x16x32_bf16 v[196:199], v[204:207], v[56:59], v[196:199]\n"
"v_exp_f32 v48, v48\n"
"v_mfma_f32_16x16x32_bf16 v[100:103], a[224:227], v[56:59], v[100:103]\n"
"v_exp_f32 v49, v49\n"
"v_mfma_f32_16x16x32_bf16 v[112:115], a[228:231], v[56:59], v[112:115]\n"
"v_exp_f32 v50, v50\n"
"v_mfma_f32_16x16x32_bf16 v[124:127], a[232:235], v[56:59], v[124:127]\n"
"v_exp_f32 v51, v51\n"
"v_mfma_f32_16x16x32_bf16 v[136:139], a[236:239], v[56:59], v[136:139]\n"
"v_exp_f32 v52, v52\n"
"v_mfma_f32_16x16x32_bf16 v[148:151], a[240:243], v[56:59], v[148:151]\n"
"v_exp_f32 v53, v53\n"
"v_mfma_f32_16x16x32_bf16 v[160:163], a[244:247], v[56:59], v[160:163]\n"
"v_exp_f32 v54, v54\n"
"v_mfma_f32_16x16x32_bf16 v[172:175], a[248:251], v[56:59], v[172:175]\n"
"v_exp_f32 v55, v55\n"
"v_mfma_f32_16x16x32_bf16 v[184:187], a[252:255], v[56:59], v[184:187]\n"
"v_cvt_pk_bf16_f32 v48, v48, v49\n"
"v_cvt_pk_bf16_f32 v49, v50, v51\n"
"s_waitcnt lgkmcnt(0)\n"
"v_mfma_f32_16x16x32_bf16 v[56:59], a[192:195], v[0:3], v[64:67]\n"
"v_cvt_pk_bf16_f32 v50, v52, v53\n"
"v_cvt_pk_bf16_f32 v51, v54, v55\n"
"s_add_u32 m0, s77, 0x12000\n"
"v_mfma_f32_16x16x32_bf16 v[56:59], a[196:199], v[4:7], v[56:59]\n"
"buffer_load_dwordx4 v220, s[48:51], s79 offen nt lds\n"
"v_mfma_f32_16x16x32_bf16 v[56:59], a[200:203], v[8:11], v[56:59]\n"
"ds_read_b64_tr_b16 v[82:83], v212 offset:54272\n"
"ds_read_b64_tr_b16 v[84:85], v215 offset:50176\n"
"v_mfma_f32_16x16x32_bf16 v[56:59], a[204:207], v[12:15], v[56:59]\n"
"ds_read_b64_tr_b16 v[86:87], v215 offset:54272\n"
"ds_read_b64_tr_b16 v[88:89], v212 offset:50688\n"
"v_mfma_f32_16x16x32_bf16 v[60:63], a[208:211], v[0:3], v[64:67]\n"
"ds_read_b64_tr_b16 v[90:91], v212 offset:54784\n"
"ds_read_b64_tr_b16 v[92:93], v215 offset:50688\n"
"v_mfma_f32_16x16x32_bf16 v[60:63], a[212:215], v[4:7], v[60:63]\n"
"ds_read_b64_tr_b16 v[94:95], v215 offset:54784\n"
"v_mfma_f32_16x16x32_bf16 v[60:63], a[216:219], v[8:11], v[60:63]\n"
"v_mfma_f32_16x16x32_bf16 v[60:63], a[220:223], v[12:15], v[60:63]\n"
"v_mfma_f32_16x16x32_bf16 v[200:203], v[204:207], v[48:51], v[200:203]\n"
"v_exp_f32 v56, v56\n"
"v_mfma_f32_16x16x32_bf16 v[104:107], a[224:227], v[48:51], v[104:107]\n"
"ds_read_b64_tr_b16 a[224:225], v212 offset:49152\n"
"ds_read_b64_tr_b16 a[226:227], v212 offset:53248\n"
"v_mfma_f32_16x16x32_bf16 v[116:119], a[228:231], v[48:51], v[116:119]\n"
"ds_read_b64_tr_b16 a[228:229], v215 offset:49152\n"
"ds_read_b64_tr_b16 a[230:231], v215 offset:53248\n"
"v_mfma_f32_16x16x32_bf16 v[128:131], a[232:235], v[48:51], v[128:131]\n"
"ds_read_b64_tr_b16 a[232:233], v212 offset:49664\n"
"ds_read_b64_tr_b16 a[234:235], v212 offset:53760\n"
"v_mfma_f32_16x16x32_bf16 v[140:143], a[236:239], v[48:51], v[140:143]\n"
"v_exp_f32 v57, v57\n"
"v_mfma_f32_16x16x32_bf16 v[152:155], a[240:243], v[48:51], v[152:155]\n"
"v_exp_f32 v58, v58\n"
"v_mfma_f32_16x16x32_bf16 v[164:167], a[244:247], v[48:51], v[164:167]\n"
"v_exp_f32 v59, v59\n"
"v_mfma_f32_16x16x32_bf16 v[176:179], a[248:251], v[48:51], v[176:179]\n"
"v_exp_f32 v60, v60\n"
"v_mfma_f32_16x16x32_bf16 v[188:191], a[252:255], v[48:51], v[188:191]\n"
"v_exp_f32 v61, v61\n"
"v_mfma_f32_16x16x32_bf16 v[48:51], a[192:195], v[16:19], v[68:71]\n"
"v_exp_f32 v62, v62\n"
"v_mfma_f32_16x16x32_bf16 v[48:51], a[196:199], v[20:23], v[48:51]\n"
"v_exp_f32 v63, v63\n"
"v_mfma_f32_16x16x32_bf16 v[48:51], a[200:203], v[24:27], v[48:51]\n"
"v_cvt_pk_bf16_f32 v56, v56, v57\n"
"v_cvt_pk_bf16_f32 v57, v58, v59\n"
"v_mfma_f32_16x16x32_bf16 v[48:51], a[204:207], v[28:31], v[48:51]\n"
"v_cvt_pk_bf16_f32 v58, v60, v61\n"
"v_cvt_pk_bf16_f32 v59, v62, v63\n"
"v_mfma_f32_16x16x32_bf16 v[52:55], a[208:211], v[16:19], v[68:71]\n"
"buffer_load_dwordx4 v220, s[48:51], s79 offen offset:2048 nt lds\n"
"v_mfma_f32_16x16x32_bf16 v[52:55], a[212:215], v[20:23], v[52:55]\n"
"v_mfma_f32_16x16x32_bf16 v[52:55], a[216:219], v[24:27], v[52:55]\n"
"v_mfma_f32_16x16x32_bf16 v[52:55], a[220:223], v[28:31], v[52:55]\n"
"v_mfma_f32_16x16x32_bf16 v[192:195], v[204:207], v[56:59], v[192:195]\n"
"v_exp_f32 v48, v48\n"
"s_waitcnt lgkmcnt(0)\n"
"v_mfma_f32_16x16x32_bf16 v[96:99], a[224:227], v[56:59], v[96:99]\n"
"v_exp_f32 v49, v49\n"
"v_mfma_f32_16x16x32_bf16 v[108:111], a[228:231], v[56:59], v[108:111]\n"
"v_exp_f32 v50, v50\n"
"v_mfma_f32_16x16x32_bf16 v[120:123], a[232:235], v[56:59], v[120:123]\n"
"v_exp_f32 v51, v51\n"
"v_mfma_f32_16x16x32_bf16 v[132:135], v[76:79], v[56:59], v[132:135]\n"
"v_exp_f32 v52, v52\n"
"v_mfma_f32_16x16x32_bf16 v[144:147], v[80:83], v[56:59], v[144:147]\n"
"v_exp_f32 v53, v53\n"
"v_mfma_f32_16x16x32_bf16 v[156:159], v[84:87], v[56:59], v[156:159]\n"
"v_exp_f32 v54, v54\n"
"v_mfma_f32_16x16x32_bf16 v[168:171], v[88:91], v[56:59], v[168:171]\n"
"v_exp_f32 v55, v55\n"
"v_mfma_f32_16x16x32_bf16 v[180:183], v[92:95], v[56:59], v[180:183]\n"
"s_waitcnt vmcnt(12)\n"
"s_barrier\n"
"v_mfma_f32_16x16x32_bf16 v[56:59], a[192:195], v[32:35], v[72:75]\n"
"v_cvt_pk_bf16_f32 v48, v48, v49\n"
"v_cvt_pk_bf16_f32 v49, v50, v51\n"
"v_mfma_f32_16x16x32_bf16 v[56:59], a[196:199], v[36:39], v[56:59]\n"
"v_cvt_pk_bf16_f32 v50, v52, v53\n"
"v_cvt_pk_bf16_f32 v51, v54, v55\n"
"v_mfma_f32_16x16x32_bf16 v[56:59], a[200:203], v[40:43], v[56:59]\n"
"ds_read_b128 a[192:195], v210 offset:0\n"
"ds_read_b128 a[196:199], v210 offset:512\n"
"v_mfma_f32_16x16x32_bf16 v[56:59], a[204:207], v[44:47], v[56:59]\n"
"ds_read_b128 a[200:203], v210 offset:1024\n"
"ds_read_b128 a[204:207], v210 offset:1536\n"
"v_mfma_f32_16x16x32_bf16 v[60:63], a[208:211], v[32:35], v[72:75]\n"
"ds_read_b128 a[208:211], v210 offset:4096\n"
"ds_read_b64_tr_b16 a[236:237], v216 offset:512\n"
"v_mfma_f32_16x16x32_bf16 v[60:63], a[212:215], v[36:39], v[60:63]\n"
"ds_read_b128 a[212:215], v210 offset:4608\n"
"ds_read_b64_tr_b16 a[238:239], v216 offset:4608\n"
"v_mfma_f32_16x16x32_bf16 v[60:63], a[216:219], v[40:43], v[60:63]\n"
"ds_read_b128 a[216:219], v210 offset:5120\n"
"ds_read_b64_tr_b16 a[240:241], v213 offset:1024\n"
"v_mfma_f32_16x16x32_bf16 v[60:63], a[220:223], v[44:47], v[60:63]\n"
"ds_read_b128 a[220:223], v210 offset:5632\n"
"ds_read_b64_tr_b16 a[242:243], v213 offset:5120\n"
"v_mfma_f32_16x16x32_bf16 v[196:199], v[204:207], v[48:51], v[196:199]\n"
"v_exp_f32 v56, v56\n"
"v_mfma_f32_16x16x32_bf16 v[100:103], a[224:227], v[48:51], v[100:103]\n"
"v_exp_f32 v57, v57\n"
"v_mfma_f32_16x16x32_bf16 v[112:115], a[228:231], v[48:51], v[112:115]\n"
"v_exp_f32 v58, v58\n"
"v_mfma_f32_16x16x32_bf16 v[124:127], a[232:235], v[48:51], v[124:127]\n"
"v_exp_f32 v59, v59\n"
"v_mfma_f32_16x16x32_bf16 v[136:139], v[76:79], v[48:51], v[136:139]\n"
"v_exp_f32 v60, v60\n"
"v_mfma_f32_16x16x32_bf16 v[148:151], v[80:83], v[48:51], v[148:151]\n"
"v_exp_f32 v61, v61\n"
"v_mfma_f32_16x16x32_bf16 v[160:163], v[84:87], v[48:51], v[160:163]\n"
"v_exp_f32 v62, v62\n"
"v_mfma_f32_16x16x32_bf16 v[172:175], v[88:91], v[48:51], v[172:175]\n"
"v_exp_f32 v63, v63\n"
"v_mfma_f32_16x16x32_bf16 v[184:187], v[92:95], v[48:51], v[184:187]\n"
"v_cvt_pk_bf16_f32 v56, v56, v57\n"
"v_cvt_pk_bf16_f32 v57, v58, v59\n"
"s_waitcnt lgkmcnt(0)\n"
"v_mfma_f32_16x16x32_bf16 v[48:51], a[192:195], v[0:3], v[64:67]\n"
"v_cvt_pk_bf16_f32 v58, v60, v61\n"
"v_cvt_pk_bf16_f32 v59, v62, v63\n"
"v_mfma_f32_16x16x32_bf16 v[48:51], a[196:199], v[4:7], v[48:51]\n"
"ds_read_b64_tr_b16 a[244:245], v216 offset:1024\n"
"ds_read_b64_tr_b16 a[246:247], v216 offset:5120\n"
"v_mfma_f32_16x16x32_bf16 v[48:51], a[200:203], v[8:11], v[48:51]\n"
"ds_read_b64_tr_b16 a[248:249], v213 offset:1536\n"
"ds_read_b64_tr_b16 a[250:251], v213 offset:5632\n"
"v_mfma_f32_16x16x32_bf16 v[48:51], a[204:207], v[12:15], v[48:51]\n"
"ds_read_b64_tr_b16 a[252:253], v216 offset:1536\n"
"ds_read_b64_tr_b16 a[254:255], v216 offset:5632\n"
"v_mfma_f32_16x16x32_bf16 v[52:55], a[208:211], v[0:3], v[64:67]\n"
"v_mfma_f32_16x16x32_bf16 v[52:55], a[212:215], v[4:7], v[52:55]\n"
"v_mfma_f32_16x16x32_bf16 v[52:55], a[216:219], v[8:11], v[52:55]\n"
"v_mfma_f32_16x16x32_bf16 v[52:55], a[220:223], v[12:15], v[52:55]\n"
"v_mfma_f32_16x16x32_bf16 v[200:203], v[204:207], v[56:59], v[200:203]\n"
"s_nop 0\n"
"v_exp_f32 v48, v48\n"
"v_mfma_f32_16x16x32_bf16 v[104:107], a[224:227], v[56:59], v[104:107]\n"
"ds_read_b64_tr_b16 a[224:225], v213 offset:0\n"
"ds_read_b64_tr_b16 a[226:227], v213 offset:4096\n"
"v_mfma_f32_16x16x32_bf16 v[116:119], a[228:231], v[56:59], v[116:119]\n"
"ds_read_b64_tr_b16 a[228:229], v216 offset:0\n"
"ds_read_b64_tr_b16 a[230:231], v216 offset:4096\n"
"v_mfma_f32_16x16x32_bf16 v[128:131], a[232:235], v[56:59], v[128:131]\n"
"ds_read_b64_tr_b16 a[232:233], v213 offset:512\n"
"ds_read_b64_tr_b16 a[234:235], v213 offset:4608\n"
"v_mfma_f32_16x16x32_bf16 v[140:143], v[76:79], v[56:59], v[140:143]\n"
"v_exp_f32 v49, v49\n"
"v_mfma_f32_16x16x32_bf16 v[152:155], v[80:83], v[56:59], v[152:155]\n"
"v_exp_f32 v50, v50\n"
"v_mfma_f32_16x16x32_bf16 v[164:167], v[84:87], v[56:59], v[164:167]\n"
"v_exp_f32 v51, v51\n"
"v_mfma_f32_16x16x32_bf16 v[176:179], v[88:91], v[56:59], v[176:179]\n"
"v_exp_f32 v52, v52\n"
"v_mfma_f32_16x16x32_bf16 v[188:191], v[92:95], v[56:59], v[188:191]\n"
"v_exp_f32 v53, v53\n"
"v_mfma_f32_16x16x32_bf16 v[56:59], a[192:195], v[16:19], v[68:71]\n"
"v_exp_f32 v54, v54\n"
"v_mfma_f32_16x16x32_bf16 v[56:59], a[196:199], v[20:23], v[56:59]\n"
"v_exp_f32 v55, v55\n"
"v_mfma_f32_16x16x32_bf16 v[56:59], a[200:203], v[24:27], v[56:59]\n"
"v_cvt_pk_bf16_f32 v48, v48, v49\n"
"v_cvt_pk_bf16_f32 v49, v50, v51\n"
"v_mfma_f32_16x16x32_bf16 v[56:59], a[204:207], v[28:31], v[56:59]\n"
"v_cvt_pk_bf16_f32 v50, v52, v53\n"
"v_cvt_pk_bf16_f32 v51, v54, v55\n"
"v_mfma_f32_16x16x32_bf16 v[60:63], a[208:211], v[16:19], v[68:71]\n"
"v_mfma_f32_16x16x32_bf16 v[60:63], a[212:215], v[20:23], v[60:63]\n"
"v_mfma_f32_16x16x32_bf16 v[60:63], a[216:219], v[24:27], v[60:63]\n"
"v_mfma_f32_16x16x32_bf16 v[60:63], a[220:223], v[28:31], v[60:63]\n"
"v_mfma_f32_16x16x32_bf16 v[192:195], v[204:207], v[48:51], v[192:195]\n"
"s_nop 0\n"
"v_exp_f32 v56, v56\n"
"s_waitcnt lgkmcnt(0)\n"
"v_mfma_f32_16x16x32_bf16 v[96:99], a[224:227], v[48:51], v[96:99]\n"
"v_exp_f32 v57, v57\n"
"v_mfma_f32_16x16x32_bf16 v[108:111], a[228:231], v[48:51], v[108:111]\n"
"v_exp_f32 v58, v58\n"
"v_mfma_f32_16x16x32_bf16 v[120:123], a[232:235], v[48:51], v[120:123]\n"
"v_exp_f32 v59, v59\n"
"v_mfma_f32_16x16x32_bf16 v[132:135], a[236:239], v[48:51], v[132:135]\n"
"v_exp_f32 v60, v60\n"
"v_mfma_f32_16x16x32_bf16 v[144:147], a[240:243], v[48:51], v[144:147]\n"
"v_exp_f32 v61, v61\n"
"v_mfma_f32_16x16x32_bf16 v[156:159], a[244:247], v[48:51], v[156:159]\n"
"v_exp_f32 v62, v62\n"
"v_mfma_f32_16x16x32_bf16 v[168:171], a[248:251], v[48:51], v[168:171]\n"
"v_exp_f32 v63, v63\n"
"v_mfma_f32_16x16x32_bf16 v[180:183], a[252:255], v[48:51], v[180:183]\n"
"s_waitcnt vmcnt(0)\n"
"s_barrier\n"
"v_mfma_f32_16x16x32_bf16 v[48:51], a[192:195], v[32:35], v[72:75]\n"
"s_xor_b32 s39, s36, 0x0\n"
"v_cvt_pk_bf16_f32 v56, v56, v57\n"
"v_mfma_f32_16x16x32_bf16 v[48:51], a[196:199], v[36:39], v[48:51]\n"
"v_cvt_pk_bf16_f32 v57, v58, v59\n"
"v_cvt_pk_bf16_f32 v58, v60, v61\n"
"v_mfma_f32_16x16x32_bf16 v[48:51], a[200:203], v[40:43], v[48:51]\n"
"v_cvt_pk_bf16_f32 v59, v62, v63\n"
"ds_read_b128 a[192:195], v210 offset:16384\n"
"s_add_u32 m0, s77, 0x0\n"
"v_mfma_f32_16x16x32_bf16 v[48:51], a[204:207], v[44:47], v[48:51]\n"
"buffer_load_dwordx4 v224, s[52:55], s81 offen lds\n"
"v_mfma_f32_16x16x32_bf16 v[52:55], a[208:211], v[32:35], v[72:75]\n"
"s_xor_b32 s81, s37, 0x2000\n"
"ds_read_b128 a[196:199], v210 offset:16896\n"
"v_mfma_f32_16x16x32_bf16 v[52:55], a[212:215], v[36:39], v[52:55]\n"
"ds_read_b128 a[200:203], v210 offset:17408\n"
"ds_read_b128 a[204:207], v210 offset:17920\n"
"v_mfma_f32_16x16x32_bf16 v[52:55], a[216:219], v[40:43], v[52:55]\n"
"ds_read_b128 a[208:211], v210 offset:20480\n"
"ds_read_b128 a[212:215], v210 offset:20992\n"
"s_add_u32 m0, s77, 0x800\n"
"v_mfma_f32_16x16x32_bf16 v[52:55], a[220:223], v[44:47], v[52:55]\n"
"buffer_load_dwordx4 v224, s[52:55], s81 offen lds\n"
"v_mfma_f32_16x16x32_bf16 v[196:199], v[204:207], v[56:59], v[196:199]\n"
"s_xor_b32 s81, s37, 0x8000\n"
"ds_read_b128 a[216:219], v210 offset:21504\n"
"v_mfma_f32_16x16x32_bf16 v[100:103], a[224:227], v[56:59], v[100:103]\n"
"ds_read_b128 a[220:223], v210 offset:22016\n"
"ds_read_b64_tr_b16 v[76:77], v216 offset:16896\n"
"v_mfma_f32_16x16x32_bf16 v[112:115], a[228:231], v[56:59], v[112:115]\n"
"v_exp_f32 v48, v48\n"
"s_add_u32 m0, s77, 0x2000\n"
"v_mfma_f32_16x16x32_bf16 v[124:127], a[232:235], v[56:59], v[124:127]\n"
"buffer_load_dwordx4 v224, s[52:55], s81 offen lds\n"
"v_mfma_f32_16x16x32_bf16 v[136:139], a[236:239], v[56:59], v[136:139]\n"
"s_xor_b32 s81, s37, 0xa000\n"
"ds_read_b64_tr_b16 v[78:79], v216 offset:20992\n"
"v_mfma_f32_16x16x32_bf16 v[148:151], a[240:243], v[56:59], v[148:151]\n"
"v_exp_f32 v49, v49\n"
"v_mfma_f32_16x16x32_bf16 v[160:163], a[244:247], v[56:59], v[160:163]\n"
"v_exp_f32 v50, v50\n"
"s_add_u32 m0, s77, 0x2800\n"
"v_mfma_f32_16x16x32_bf16 v[172:175], a[248:251], v[56:59], v[172:175]\n"
"buffer_load_dwordx4 v224, s[52:55], s81 offen lds\n"
"v_mfma_f32_16x16x32_bf16 v[184:187], a[252:255], v[56:59], v[184:187]\n"
"s_xor_b32 s81, s37, 0x200000\n"
"v_cvt_pk_bf16_f32 v48, v48, v49\n"
"s_waitcnt lgkmcnt(0)\n"
"v_mfma_f32_16x16x32_bf16 v[56:59], a[192:195], v[0:3], v[64:67]\n"
"v_exp_f32 v51, v51\n"
"v_mfma_f32_16x16x32_bf16 v[56:59], a[196:199], v[4:7], v[56:59]\n"
"v_exp_f32 v52, v52\n"
"v_mfma_f32_16x16x32_bf16 v[56:59], a[200:203], v[8:11], v[56:59]\n"
"v_exp_f32 v53, v53\n"
"v_mfma_f32_16x16x32_bf16 v[56:59], a[204:207], v[12:15], v[56:59]\n"
"v_exp_f32 v54, v54\n"
"v_mfma_f32_16x16x32_bf16 v[60:63], a[208:211], v[0:3], v[64:67]\n"
"v_exp_f32 v55, v55\n"
"v_mfma_f32_16x16x32_bf16 v[60:63], a[212:215], v[4:7], v[60:63]\n"
"v_cvt_pk_bf16_f32 v49, v50, v51\n"
"v_cvt_pk_bf16_f32 v50, v52, v53\n"
"v_mfma_f32_16x16x32_bf16 v[60:63], a[216:219], v[8:11], v[60:63]\n"
"v_cvt_pk_bf16_f32 v51, v54, v55\n"
"ds_read_b64_tr_b16 v[80:81], v213 offset:17408\n"
"s_add_u32 m0, s77, 0x8000\n"
"v_mfma_f32_16x16x32_bf16 v[60:63], a[220:223], v[12:15], v[60:63]\n"
"buffer_load_dwordx4 v224, s[52:55], s81 offen lds\n"
"v_mfma_f32_16x16x32_bf16 v[200:203], v[204:207], v[48:51], v[200:203]\n"
"s_xor_b32 s81, s37, 0x202000\n"
"ds_read_b64_tr_b16 v[82:83], v213 offset:21504\n"
"v_mfma_f32_16x16x32_bf16 v[104:107], a[224:227], v[48:51], v[104:107]\n"
"ds_read_b64_tr_b16 a[224:225], v213 offset:16384\n"
"ds_read_b64_tr_b16 a[226:227], v213 offset:20480\n"
"v_mfma_f32_16x16x32_bf16 v[116:119], a[228:231], v[48:51], v[116:119]\n"
"ds_read_b64_tr_b16 a[228:229], v216 offset:16384\n"
"ds_read_b64_tr_b16 a[230:231], v216 offset:20480\n"
"s_add_u32 m0, s77, 0x8800\n"
"v_mfma_f32_16x16x32_bf16 v[128:131], a[232:235], v[48:51], v[128:131]\n"
"buffer_load_dwordx4 v224, s[52:55], s81 offen lds\n"
"v_mfma_f32_16x16x32_bf16 v[140:143], a[236:239], v[48:51], v[140:143]\n"
"s_xor_b32 s81, s37, 0x208000\n"
"ds_read_b64_tr_b16 a[232:233], v213 offset:16896\n"
"v_mfma_f32_16x16x32_bf16 v[152:155], a[240:243], v[48:51], v[152:155]\n"
"ds_read_b64_tr_b16 a[234:235], v213 offset:20992\n"
"ds_read_b64_tr_b16 v[84:85], v216 offset:17408\n"
"v_mfma_f32_16x16x32_bf16 v[164:167], a[244:247], v[48:51], v[164:167]\n"
"ds_read_b64_tr_b16 v[86:87], v216 offset:21504\n"
"ds_read_b64_tr_b16 v[88:89], v213 offset:17920\n"
"s_add_u32 m0, s77, 0xa000\n"
"v_mfma_f32_16x16x32_bf16 v[176:179], a[248:251], v[48:51], v[176:179]\n"
"buffer_load_dwordx4 v224, s[52:55], s81 offen lds\n"
"v_mfma_f32_16x16x32_bf16 v[188:191], a[252:255], v[48:51], v[188:191]\n"
"s_xor_b32 s81, s37, 0x20a000\n"
"ds_read_b64_tr_b16 v[90:91], v213 offset:22016\n"
"v_mfma_f32_16x16x32_bf16 v[48:51], a[192:195], v[16:19], v[68:71]\n"
"v_exp_f32 v56, v56\n"
"v_mfma_f32_16x16x32_bf16 v[48:51], a[196:199], v[20:23], v[48:51]\n"
"v_exp_f32 v57, v57\n"
"s_add_u32 m0, s77, 0xa800\n"
"v_mfma_f32_16x16x32_bf16 v[48:51], a[200:203], v[24:27], v[48:51]\n"
"buffer_load_dwordx4 v224, s[52:55], s81 offen lds\n"
"v_mfma_f32_16x16x32_bf16 v[48:51], a[204:207], v[28:31], v[48:51]\n"
"s_xor_b32 s81, s37, 0x210000\n"
"v_cvt_pk_bf16_f32 v56, v56, v57\n"
"v_mfma_f32_16x16x32_bf16 v[52:55], a[208:211], v[16:19], v[68:71]\n"
"v_exp_f32 v58, v58\n"
"v_mfma_f32_16x16x32_bf16 v[52:55], a[212:215], v[20:23], v[52:55]\n"
"v_exp_f32 v59, v59\n"
"v_mfma_f32_16x16x32_bf16 v[52:55], a[216:219], v[24:27], v[52:55]\n"
"v_exp_f32 v60, v60\n"
"v_cvt_pk_bf16_f32 v57, v58, v59\n"
"v_mfma_f32_16x16x32_bf16 v[52:55], a[220:223], v[28:31], v[52:55]\n"
"v_exp_f32 v61, v61\n"
"v_exp_f32 v62, v62\n"
"v_exp_f32 v63, v63\n"
"v_cvt_pk_bf16_f32 v58, v60, v61\n"
"v_cvt_pk_bf16_f32 v59, v62, v63\n"
"s_add_u32 m0, s77, 0x14000\n"
"s_nop 0\n"
"v_mfma_f32_16x16x32_bf16 v[192:195], v[204:207], v[56:59], v[192:195]\n"
"buffer_load_dwordx4 v224, s[52:55], s81 offen lds\n"
"ds_read_b64_tr_b16 v[92:93], v216 offset:17920\n"
"s_waitcnt lgkmcnt(1)\n"
"v_mfma_f32_16x16x32_bf16 v[96:99], a[224:227], v[56:59], v[96:99]\n"
"ds_read_b64_tr_b16 v[94:95], v216 offset:22016\n"
"s_xor_b32 s81, s37, 0x212000\n"
"v_mfma_f32_16x16x32_bf16 v[108:111], a[228:231], v[56:59], v[108:111]\n"
"v_exp_f32 v48, v48\n"
"v_mfma_f32_16x16x32_bf16 v[120:123], a[232:235], v[56:59], v[120:123]\n"
"v_exp_f32 v49, v49\n"
"s_add_u32 m0, s77, 0x14800\n"
"v_mfma_f32_16x16x32_bf16 v[132:135], v[76:79], v[56:59], v[132:135]\n"
"buffer_load_dwordx4 v224, s[52:55], s81 offen lds\n"
"v_mfma_f32_16x16x32_bf16 v[144:147], v[80:83], v[56:59], v[144:147]\n"
"s_xor_b32 s81, s37, 0x218000\n"
"v_cvt_pk_bf16_f32 v48, v48, v49\n"
"v_mfma_f32_16x16x32_bf16 v[156:159], v[84:87], v[56:59], v[156:159]\n"
"v_exp_f32 v50, v50\n"
"v_mfma_f32_16x16x32_bf16 v[168:171], v[88:91], v[56:59], v[168:171]\n"
"v_exp_f32 v51, v51\n"
"s_add_u32 m0, s77, 0x16000\n"
"s_waitcnt lgkmcnt(0)\n"
"v_mfma_f32_16x16x32_bf16 v[180:183], v[92:95], v[56:59], v[180:183]\n"
"buffer_load_dwordx4 v224, s[52:55], s81 offen lds\n"
"s_barrier\n"
"v_mfma_f32_16x16x32_bf16 v[56:59], a[192:195], v[32:35], v[72:75]\n"
"s_xor_b32 s81, s37, 0x21a000\n"
"v_cvt_pk_bf16_f32 v49, v50, v51\n"
"v_mfma_f32_16x16x32_bf16 v[56:59], a[196:199], v[36:39], v[56:59]\n"
"v_exp_f32 v52, v52\n"
"v_mfma_f32_16x16x32_bf16 v[56:59], a[200:203], v[40:43], v[56:59]\n"
"v_exp_f32 v53, v53\n"
"s_add_u32 m0, s77, 0x16800\n"
"v_mfma_f32_16x16x32_bf16 v[56:59], a[204:207], v[44:47], v[56:59]\n"
"buffer_load_dwordx4 v224, s[52:55], s81 offen lds\n"
"v_mfma_f32_16x16x32_bf16 v[60:63], a[208:211], v[32:35], v[72:75]\n"
"s_xor_b32 s81, s37, 0x10000\n"
"v_cvt_pk_bf16_f32 v50, v52, v53\n"
"v_mfma_f32_16x16x32_bf16 v[60:63], a[212:215], v[36:39], v[60:63]\n"
"v_exp_f32 v54, v54\n"
"v_mfma_f32_16x16x32_bf16 v[60:63], a[216:219], v[40:43], v[60:63]\n"
"v_exp_f32 v55, v55\n"
"s_add_u32 m0, s77, 0x18000\n"
"v_mfma_f32_16x16x32_bf16 v[60:63], a[220:223], v[44:47], v[60:63]\n"
"buffer_load_dwordx4 v224, s[52:55], s81 offen lds\n"
"v_cvt_pk_bf16_f32 v51, v54, v55\n"
"s_nop 1\n"
"v_mfma_f32_16x16x32_bf16 v[196:199], v[204:207], v[48:51], v[196:199]\n"
"s_xor_b32 s81, s37, 0x12000\n"
"ds_read_b128 a[192:195], v208 offset:16384\n"
"v_mfma_f32_16x16x32_bf16 v[100:103], a[224:227], v[48:51], v[100:103]\n"
"ds_read_b128 a[196:199], v208 offset:16896\n"
"ds_read_b128 a[200:203], v208 offset:17408\n"
"v_mfma_f32_16x16x32_bf16 v[112:115], a[228:231], v[48:51], v[112:115]\n"
"ds_read_b128 a[204:207], v208 offset:17920\n"
"ds_read_b128 a[208:211], v208 offset:20480\n"
"s_add_u32 m0, s77, 0x18800\n"
"v_mfma_f32_16x16x32_bf16 v[124:127], a[232:235], v[48:51], v[124:127]\n"
"buffer_load_dwordx4 v224, s[52:55], s81 offen lds\n"
"v_mfma_f32_16x16x32_bf16 v[136:139], v[76:79], v[48:51], v[136:139]\n"
"s_xor_b32 s81, s37, 0x18000\n"
"ds_read_b128 a[212:215], v208 offset:20992\n"
"v_mfma_f32_16x16x32_bf16 v[148:151], v[80:83], v[48:51], v[148:151]\n"
"ds_read_b128 a[216:219], v208 offset:21504\n"
"ds_read_b128 a[220:223], v208 offset:22016\n"
"v_mfma_f32_16x16x32_bf16 v[160:163], v[84:87], v[48:51], v[160:163]\n"
"v_exp_f32 v56, v56\n"
"s_add_u32 m0, s77, 0x1a000\n"
"v_mfma_f32_16x16x32_bf16 v[172:175], v[88:91], v[48:51], v[172:175]\n"
"buffer_load_dwordx4 v224, s[52:55], s81 offen lds\n"
"v_mfma_f32_16x16x32_bf16 v[184:187], v[92:95], v[48:51], v[184:187]\n"
"s_xor_b32 s81, s37, 0x1a000\n"
"ds_read_b64_tr_b16 a[236:237], v214 offset:16896\n"
"s_waitcnt lgkmcnt(3)\n"
"v_mfma_f32_16x16x32_bf16 v[48:51], a[192:195], v[0:3], v[64:67]\n"
"v_exp_f32 v57, v57\n"
"v_mfma_f32_16x16x32_bf16 v[48:51], a[196:199], v[4:7], v[48:51]\n"
"v_exp_f32 v58, v58\n"
"s_add_u32 m0, s77, 0x1a800\n"
"v_mfma_f32_16x16x32_bf16 v[48:51], a[200:203], v[8:11], v[48:51]\n"
"buffer_load_dwordx4 v224, s[52:55], s81 offen lds\n"
"v_mfma_f32_16x16x32_bf16 v[48:51], a[204:207], v[12:15], v[48:51]\n"
"v_exp_f32 v59, v59\n"
"v_mfma_f32_16x16x32_bf16 v[52:55], a[208:211], v[0:3], v[64:67]\n"
"v_exp_f32 v60, v60\n"
"v_mfma_f32_16x16x32_bf16 v[52:55], a[212:215], v[4:7], v[52:55]\n"
"v_exp_f32 v61, v61\n"
"s_add_u32 m0, s38, 0xc000\n"
"s_waitcnt lgkmcnt(0)\n"
"v_mfma_f32_16x16x32_bf16 v[52:55], a[216:219], v[8:11], v[52:55]\n"
"buffer_load_dwordx4 v223, s[56:59], s39 offen lds\n"
"v_cvt_pk_bf16_f32 v56, v56, v57\n"
"v_mfma_f32_16x16x32_bf16 v[52:55], a[220:223], v[12:15], v[52:55]\n"
"v_exp_f32 v62, v62\n"
"v_exp_f32 v63, v63\n"
"v_cvt_pk_bf16_f32 v57, v58, v59\n"
"v_cvt_pk_bf16_f32 v58, v60, v61\n"
"v_cvt_pk_bf16_f32 v59, v62, v63\n"
"s_nop 1\n"
"v_mfma_f32_16x16x32_bf16 v[200:203], v[204:207], v[56:59], v[200:203]\n"
"ds_read_b64_tr_b16 a[238:239], v214 offset:20992\n"
"ds_read_b64_tr_b16 a[240:241], v211 offset:17408\n"
"v_mfma_f32_16x16x32_bf16 v[104:107], a[224:227], v[56:59], v[104:107]\n"
"ds_read_b64_tr_b16 a[224:225], v211 offset:16384\n"
"ds_read_b64_tr_b16 a[226:227], v211 offset:20480\n"
"v_mfma_f32_16x16x32_bf16 v[116:119], a[228:231], v[56:59], v[116:119]\n"
"buffer_load_dwordx4 v223, s[56:59], s39 offen offset:1024 lds\n"
"ds_read_b64_tr_b16 a[228:229], v214 offset:16384\n"
"v_mfma_f32_16x16x32_bf16 v[128:131], a[232:235], v[56:59], v[128:131]\n"
"ds_read_b64_tr_b16 a[230:231], v214 offset:20480\n"
"ds_read_b64_tr_b16 a[232:233], v211 offset:16896\n"
"v_mfma_f32_16x16x32_bf16 v[140:143], v[76:79], v[56:59], v[140:143]\n"
"ds_read_b64_tr_b16 a[234:235], v211 offset:20992\n"
"ds_read_b64_tr_b16 a[242:243], v211 offset:21504\n"
"v_mfma_f32_16x16x32_bf16 v[152:155], v[80:83], v[56:59], v[152:155]\n"
"ds_read_b64_tr_b16 a[244:245], v214 offset:17408\n"
"ds_read_b64_tr_b16 a[246:247], v214 offset:21504\n"
"v_mfma_f32_16x16x32_bf16 v[164:167], v[84:87], v[56:59], v[164:167]\n"
"buffer_load_dwordx4 v223, s[56:59], s39 offen offset:2048 lds\n"
"s_xor_b32 s39, s36, 0x4000\n"
"v_mfma_f32_16x16x32_bf16 v[176:179], v[88:91], v[56:59], v[176:179]\n"
"v_exp_f32 v48, v48\n"
"v_mfma_f32_16x16x32_bf16 v[188:191], v[92:95], v[56:59], v[188:191]\n"
"v_exp_f32 v49, v49\n"
"v_mfma_f32_16x16x32_bf16 v[56:59], a[192:195], v[16:19], v[68:71]\n"
"v_exp_f32 v50, v50\n"
"s_add_u32 m0, s38, 0x1c000\n"
"v_mfma_f32_16x16x32_bf16 v[56:59], a[196:199], v[20:23], v[56:59]\n"
"buffer_load_dwordx4 v223, s[56:59], s39 offen lds\n"
"v_mfma_f32_16x16x32_bf16 v[56:59], a[200:203], v[24:27], v[56:59]\n"
"v_exp_f32 v51, v51\n"
"v_mfma_f32_16x16x32_bf16 v[56:59], a[204:207], v[28:31], v[56:59]\n"
"v_exp_f32 v52, v52\n"
"v_mfma_f32_16x16x32_bf16 v[60:63], a[208:211], v[16:19], v[68:71]\n"
"v_exp_f32 v53, v53\n"
"v_mfma_f32_16x16x32_bf16 v[60:63], a[212:215], v[20:23], v[60:63]\n"
"buffer_load_dwordx4 v223, s[56:59], s39 offen offset:1024 lds\n"
"v_exp_f32 v54, v54\n"
"v_mfma_f32_16x16x32_bf16 v[60:63], a[216:219], v[24:27], v[60:63]\n"
"v_exp_f32 v55, v55\n"
"ds_read_b64_tr_b16 a[248:249], v211 offset:17920\n"
"v_mfma_f32_16x16x32_bf16 v[60:63], a[220:223], v[28:31], v[60:63]\n"
"ds_read_b64_tr_b16 a[250:251], v211 offset:22016\n"
"v_cvt_pk_bf16_f32 v48, v48, v49\n"
"v_cvt_pk_bf16_f32 v49, v50, v51\n"
"v_cvt_pk_bf16_f32 v50, v52, v53\n"
"v_cvt_pk_bf16_f32 v51, v54, v55\n"
"s_nop 1\n"
"v_mfma_f32_16x16x32_bf16 v[192:195], v[204:207], v[48:51], v[192:195]\n"
"ds_read_b64_tr_b16 a[252:253], v214 offset:17920\n"
"ds_read_b64_tr_b16 a[254:255], v214 offset:22016\n"
"s_waitcnt lgkmcnt(4)\n"
"v_mfma_f32_16x16x32_bf16 v[96:99], a[224:227], v[48:51], v[96:99]\n"
"buffer_load_dwordx4 v223, s[56:59], s39 offen offset:2048 lds\n"
"v_mfma_f32_16x16x32_bf16 v[108:111], a[228:231], v[48:51], v[108:111]\n"
"v_exp_f32 v56, v56\n"
"v_mfma_f32_16x16x32_bf16 v[120:123], a[232:235], v[48:51], v[120:123]\n"
"v_exp_f32 v57, v57\n"
"v_mfma_f32_16x16x32_bf16 v[132:135], a[236:239], v[48:51], v[132:135]\n"
"v_exp_f32 v58, v58\n"
"v_mfma_f32_16x16x32_bf16 v[144:147], a[240:243], v[48:51], v[144:147]\n"
"v_exp_f32 v59, v59\n"
"v_mfma_f32_16x16x32_bf16 v[156:159], a[244:247], v[48:51], v[156:159]\n"
"v_exp_f32 v60, v60\n"
"s_waitcnt lgkmcnt(0)\n"
"v_mfma_f32_16x16x32_bf16 v[168:171], a[248:251], v[48:51], v[168:171]\n"
"v_exp_f32 v61, v61\n"
"v_mfma_f32_16x16x32_bf16 v[180:183], a[252:255], v[48:51], v[180:183]\n"
"s_barrier\n"
"v_mfma_f32_16x16x32_bf16 v[48:51], a[192:195], v[32:35], v[72:75]\n"
"v_exp_f32 v62, v62\n"
"v_mfma_f32_16x16x32_bf16 v[48:51], a[196:199], v[36:39], v[48:51]\n"
"v_exp_f32 v63, v63\n"
"v_mfma_f32_16x16x32_bf16 v[48:51], a[200:203], v[40:43], v[48:51]\n"
"v_cvt_pk_bf16_f32 v56, v56, v57\n"
"v_cvt_pk_bf16_f32 v57, v58, v59\n"
"v_mfma_f32_16x16x32_bf16 v[48:51], a[204:207], v[44:47], v[48:51]\n"
"v_cvt_pk_bf16_f32 v58, v60, v61\n"
"v_cvt_pk_bf16_f32 v59, v62, v63\n"
"v_mfma_f32_16x16x32_bf16 v[52:55], a[208:211], v[32:35], v[72:75]\n"
"ds_read_b128 a[192:195], v209 offset:0\n"
"ds_read_b128 a[196:199], v209 offset:512\n"
"v_mfma_f32_16x16x32_bf16 v[52:55], a[212:215], v[36:39], v[52:55]\n"
"ds_read_b128 a[200:203], v209 offset:1024\n"
"ds_read_b128 a[204:207], v209 offset:1536\n"
"v_mfma_f32_16x16x32_bf16 v[52:55], a[216:219], v[40:43], v[52:55]\n"
"ds_read_b128 a[208:211], v209 offset:4096\n"
"ds_read_b128 a[212:215], v209 offset:4608\n"
"v_mfma_f32_16x16x32_bf16 v[52:55], a[220:223], v[44:47], v[52:55]\n"
"ds_read_b128 a[216:219], v209 offset:5120\n"
"ds_read_b128 a[220:223], v209 offset:5632\n"
"v_mfma_f32_16x16x32_bf16 v[196:199], v[204:207], v[56:59], v[196:199]\n"
"v_exp_f32 v48, v48\n"
"v_mfma_f32_16x16x32_bf16 v[100:103], a[224:227], v[56:59], v[100:103]\n"
"v_exp_f32 v49, v49\n"
"v_mfma_f32_16x16x32_bf16 v[112:115], a[228:231], v[56:59], v[112:115]\n"
"v_exp_f32 v50, v50\n"
"v_mfma_f32_16x16x32_bf16 v[124:127], a[232:235], v[56:59], v[124:127]\n"
"v_exp_f32 v51, v51\n"
"v_mfma_f32_16x16x32_bf16 v[136:139], a[236:239], v[56:59], v[136:139]\n"
"v_exp_f32 v52, v52\n"
"v_mfma_f32_16x16x32_bf16 v[148:151], a[240:243], v[56:59], v[148:151]\n"
"v_exp_f32 v53, v53\n"
"v_mfma_f32_16x16x32_bf16 v[160:163], a[244:247], v[56:59], v[160:163]\n"
"v_exp_f32 v54, v54\n"
"v_mfma_f32_16x16x32_bf16 v[172:175], a[248:251], v[56:59], v[172:175]\n"
"v_exp_f32 v55, v55\n"
"v_mfma_f32_16x16x32_bf16 v[184:187], a[252:255], v[56:59], v[184:187]\n"
"v_cvt_pk_bf16_f32 v48, v48, v49\n"
"v_cvt_pk_bf16_f32 v49, v50, v51\n"
"s_waitcnt lgkmcnt(0)\n"
"v_mfma_f32_16x16x32_bf16 v[56:59], a[192:195], v[0:3], v[64:67]\n"
"v_cvt_pk_bf16_f32 v50, v52, v53\n"
"v_cvt_pk_bf16_f32 v51, v54, v55\n"
"v_mfma_f32_16x16x32_bf16 v[56:59], a[196:199], v[4:7], v[56:59]\n"
"ds_read_b64_tr_b16 v[76:77], v215 offset:512\n"
"ds_read_b64_tr_b16 v[78:79], v215 offset:4608\n"
"v_mfma_f32_16x16x32_bf16 v[56:59], a[200:203], v[8:11], v[56:59]\n"
"ds_read_b64_tr_b16 v[80:81], v212 offset:1024\n"
"ds_read_b64_tr_b16 v[82:83], v212 offset:5120\n"
"v_mfma_f32_16x16x32_bf16 v[56:59], a[204:207], v[12:15], v[56:59]\n"
"ds_read_b64_tr_b16 v[84:85], v215 offset:1024\n"
"ds_read_b64_tr_b16 v[86:87], v215 offset:5120\n"
"v_mfma_f32_16x16x32_bf16 v[60:63], a[208:211], v[0:3], v[64:67]\n"
"ds_read_b64_tr_b16 v[88:89], v212 offset:1536\n"
"ds_read_b64_tr_b16 v[90:91], v212 offset:5632\n"
"v_mfma_f32_16x16x32_bf16 v[60:63], a[212:215], v[4:7], v[60:63]\n"
"ds_read_b64_tr_b16 v[92:93], v215 offset:1536\n"
"ds_read_b64_tr_b16 v[94:95], v215 offset:5632\n"
"v_mfma_f32_16x16x32_bf16 v[60:63], a[216:219], v[8:11], v[60:63]\n"
"v_mfma_f32_16x16x32_bf16 v[60:63], a[220:223], v[12:15], v[60:63]\n"
"v_mfma_f32_16x16x32_bf16 v[200:203], v[204:207], v[48:51], v[200:203]\n"
"v_exp_f32 v56, v56\n"
"v_mfma_f32_16x16x32_bf16 v[104:107], a[224:227], v[48:51], v[104:107]\n"
"ds_read_b64_tr_b16 a[224:225], v212 offset:0\n"
"ds_read_b64_tr_b16 a[226:227], v212 offset:4096\n"
"v_mfma_f32_16x16x32_bf16 v[116:119], a[228:231], v[48:51], v[116:119]\n"
"ds_read_b64_tr_b16 a[228:229], v215 offset:0\n"
"ds_read_b64_tr_b16 a[230:231], v215 offset:4096\n"
"v_mfma_f32_16x16x32_bf16 v[128:131], a[232:235], v[48:51], v[128:131]\n"
"ds_read_b64_tr_b16 a[232:233], v212 offset:512\n"
"ds_read_b64_tr_b16 a[234:235], v212 offset:4608\n"
"v_mfma_f32_16x16x32_bf16 v[140:143], a[236:239], v[48:51], v[140:143]\n"
"v_exp_f32 v57, v57\n"
"v_mfma_f32_16x16x32_bf16 v[152:155], a[240:243], v[48:51], v[152:155]\n"
"v_exp_f32 v58, v58\n"
"v_mfma_f32_16x16x32_bf16 v[164:167], a[244:247], v[48:51], v[164:167]\n"
"v_exp_f32 v59, v59\n"
"v_mfma_f32_16x16x32_bf16 v[176:179], a[248:251], v[48:51], v[176:179]\n"
"v_exp_f32 v60, v60\n"
"v_mfma_f32_16x16x32_bf16 v[188:191], a[252:255], v[48:51], v[188:191]\n"
"v_exp_f32 v61, v61\n"
"v_mfma_f32_16x16x32_bf16 v[48:51], a[192:195], v[16:19], v[68:71]\n"
"v_exp_f32 v62, v62\n"
"v_mfma_f32_16x16x32_bf16 v[48:51], a[196:199], v[20:23], v[48:51]\n"
"v_exp_f32 v63, v63\n"
"v_mfma_f32_16x16x32_bf16 v[48:51], a[200:203], v[24:27], v[48:51]\n"
"v_cvt_pk_bf16_f32 v56, v56, v57\n"
"v_cvt_pk_bf16_f32 v57, v58, v59\n"
"v_mfma_f32_16x16x32_bf16 v[48:51], a[204:207], v[28:31], v[48:51]\n"
"v_cvt_pk_bf16_f32 v58, v60, v61\n"
"v_cvt_pk_bf16_f32 v59, v62, v63\n"
"v_mfma_f32_16x16x32_bf16 v[52:55], a[208:211], v[16:19], v[68:71]\n"
"v_mfma_f32_16x16x32_bf16 v[52:55], a[212:215], v[20:23], v[52:55]\n"
"v_mfma_f32_16x16x32_bf16 v[52:55], a[216:219], v[24:27], v[52:55]\n"
"v_mfma_f32_16x16x32_bf16 v[52:55], a[220:223], v[28:31], v[52:55]\n"
"v_mfma_f32_16x16x32_bf16 v[192:195], v[204:207], v[56:59], v[192:195]\n"
"s_nop 0\n"
"v_exp_f32 v48, v48\n"
"s_waitcnt lgkmcnt(0)\n"
"v_mfma_f32_16x16x32_bf16 v[96:99], a[224:227], v[56:59], v[96:99]\n"
"v_exp_f32 v49, v49\n"
"v_mfma_f32_16x16x32_bf16 v[108:111], a[228:231], v[56:59], v[108:111]\n"
"v_exp_f32 v50, v50\n"
"v_mfma_f32_16x16x32_bf16 v[120:123], a[232:235], v[56:59], v[120:123]\n"
"v_exp_f32 v51, v51\n"
"v_mfma_f32_16x16x32_bf16 v[132:135], v[76:79], v[56:59], v[132:135]\n"
"v_exp_f32 v52, v52\n"
"v_mfma_f32_16x16x32_bf16 v[144:147], v[80:83], v[56:59], v[144:147]\n"
"v_exp_f32 v53, v53\n"
"v_mfma_f32_16x16x32_bf16 v[156:159], v[84:87], v[56:59], v[156:159]\n"
"v_exp_f32 v54, v54\n"
"v_mfma_f32_16x16x32_bf16 v[168:171], v[88:91], v[56:59], v[168:171]\n"
"v_exp_f32 v55, v55\n"
"v_mfma_f32_16x16x32_bf16 v[180:183], v[92:95], v[56:59], v[180:183]\n"
"s_waitcnt vmcnt(0)\n"
"s_barrier\n"
"v_mfma_f32_16x16x32_bf16 v[56:59], a[192:195], v[32:35], v[72:75]\n"
"v_cvt_pk_bf16_f32 v48, v48, v49\n"
"v_cvt_pk_bf16_f32 v49, v50, v51\n"
"v_mfma_f32_16x16x32_bf16 v[56:59], a[196:199], v[36:39], v[56:59]\n"
"v_cvt_pk_bf16_f32 v50, v52, v53\n"
"v_cvt_pk_bf16_f32 v51, v54, v55\n"
"v_mfma_f32_16x16x32_bf16 v[56:59], a[200:203], v[40:43], v[56:59]\n"
"ds_read_b128 a[84:87], v217 offset:49152\n"
"ds_read_b128 a[88:91], v217 offset:50176\n"
"v_mfma_f32_16x16x32_bf16 v[56:59], a[204:207], v[44:47], v[56:59]\n"
"ds_read_b128 a[92:95], v217 offset:51200\n"
"ds_read_b128 a[96:99], v217 offset:52224\n"
"v_mfma_f32_16x16x32_bf16 v[60:63], a[208:211], v[32:35], v[72:75]\n"
"ds_read_b128 a[100:103], v217 offset:53248\n"
"ds_read_b128 a[104:107], v217 offset:54272\n"
"v_mfma_f32_16x16x32_bf16 v[60:63], a[212:215], v[36:39], v[60:63]\n"
"ds_read_b128 a[108:111], v217 offset:55296\n"
"ds_read_b128 a[112:115], v217 offset:56320\n"
"v_mfma_f32_16x16x32_bf16 v[60:63], a[216:219], v[40:43], v[60:63]\n"
"ds_read_b128 a[116:119], v217 offset:57344\n"
"ds_read_b128 a[120:123], v217 offset:58368\n"
"v_mfma_f32_16x16x32_bf16 v[60:63], a[220:223], v[44:47], v[60:63]\n"
"ds_read_b128 a[124:127], v217 offset:59392\n"
"ds_read_b128 a[128:131], v217 offset:60416\n"
"v_mfma_f32_16x16x32_bf16 v[196:199], v[204:207], v[48:51], v[196:199]\n"
"v_exp_f32 v56, v56\n"
"v_mfma_f32_16x16x32_bf16 v[100:103], a[224:227], v[48:51], v[100:103]\n"
"v_exp_f32 v57, v57\n"
"v_mfma_f32_16x16x32_bf16 v[112:115], a[228:231], v[48:51], v[112:115]\n"
"v_exp_f32 v58, v58\n"
"v_mfma_f32_16x16x32_bf16 v[124:127], a[232:235], v[48:51], v[124:127]\n"
"v_exp_f32 v59, v59\n"
"v_mfma_f32_16x16x32_bf16 v[136:139], v[76:79], v[48:51], v[136:139]\n"
"v_exp_f32 v60, v60\n"
"v_mfma_f32_16x16x32_bf16 v[148:151], v[80:83], v[48:51], v[148:151]\n"
"v_exp_f32 v61, v61\n"
"v_mfma_f32_16x16x32_bf16 v[160:163], v[84:87], v[48:51], v[160:163]\n"
"v_exp_f32 v62, v62\n"
"v_mfma_f32_16x16x32_bf16 v[172:175], v[88:91], v[48:51], v[172:175]\n"
"v_exp_f32 v63, v63\n"
"v_cvt_pk_bf16_f32 v56, v56, v57\n"
"v_mfma_f32_16x16x32_bf16 v[184:187], v[92:95], v[48:51], v[184:187]\n"
"v_cvt_pk_bf16_f32 v57, v58, v59\n"
"v_cvt_pk_bf16_f32 v58, v60, v61\n"
"v_cvt_pk_bf16_f32 v59, v62, v63\n"
"s_nop 1\n"
"v_mfma_f32_16x16x32_bf16 v[200:203], v[204:207], v[56:59], v[200:203]\n"
"ds_read_b128 a[132:135], v218 offset:49152\n"
"ds_read_b128 a[136:139], v218 offset:50176\n"
"v_mfma_f32_16x16x32_bf16 v[104:107], a[224:227], v[56:59], v[104:107]\n"
"ds_read_b128 a[140:143], v218 offset:51200\n"
"ds_read_b128 a[80:83], v218 offset:52224\n"
"v_mfma_f32_16x16x32_bf16 v[116:119], a[228:231], v[56:59], v[116:119]\n"
"v_mfma_f32_16x16x32_bf16 v[128:131], a[232:235], v[56:59], v[128:131]\n"
"v_mfma_f32_16x16x32_bf16 v[140:143], v[76:79], v[56:59], v[140:143]\n"
"v_mfma_f32_16x16x32_bf16 v[152:155], v[80:83], v[56:59], v[152:155]\n"
"v_max3_f32 v245, v192, v196, v200\n"
"v_cmp_lt_f32 vcc, s86, v245\n"
"v_mfma_f32_16x16x32_bf16 v[164:167], v[84:87], v[56:59], v[164:167]\n"
"v_mfma_f32_16x16x32_bf16 v[176:179], v[88:91], v[56:59], v[176:179]\n"
"v_mfma_f32_16x16x32_bf16 v[188:191], v[92:95], v[56:59], v[188:191]\n"
"s_nop 0\n"
"s_cbranch_vccnz Lslow1_%=\n"
"Lback1_%=:\n"
"v_rcp_f32 v242, v192\n"
"v_rcp_f32 v243, v196\n"
"v_add_u32 v250, s80, v219\n"
"v_add_u32 v248, s80, v217\n"
"v_add_u32 v249, s80, v218\n"
"s_lshl_b32 s80, s76, 9\n"
"s_sub_u32 s80, s80, 0x2000\n"
"v_add_u32 v211, s80, v211\n"
"ds_read_b64_tr_b16 a[48:49], v211 offset:0\n"
"ds_read_b64_tr_b16 a[50:51], v211 offset:4096\n"
"s_waitcnt lgkmcnt(0)\n"
"v_mfma_f32_16x16x32_bf16 v[0:3], a[48:51], a[84:87], 0\n"
"v_mul_f32 v96, v242, v96\n"
"v_mul_f32 v97, v242, v97\n"
"v_mul_f32 v98, v242, v98\n"
"v_mul_f32 v99, v242, v99\n"
"v_cvt_pk_bf16_f32 v226, v96, v97\n"
"v_cvt_pk_bf16_f32 v227, v98, v99\n"
"v_mul_f32 v108, v242, v108\n"
"v_add_u32 v214, s80, v214\n"
"ds_read_b64_tr_b16 a[52:53], v214 offset:0\n"
"ds_read_b64_tr_b16 a[54:55], v214 offset:4096\n"
"s_waitcnt lgkmcnt(0)\n"
"v_mfma_f32_16x16x32_bf16 v[4:7], a[52:55], a[84:87], 0\n"
"v_mul_f32 v109, v242, v109\n"
"v_mul_f32 v110, v242, v110\n"
"v_mul_f32 v111, v242, v111\n"
"v_cvt_pk_bf16_f32 v228, v108, v109\n"
"v_cvt_pk_bf16_f32 v229, v110, v111\n"
"v_mul_f32 v120, v242, v120\n"
"v_mul_f32 v121, v242, v121\n"
"v_mfma_f32_16x16x32_bf16 v[8:11], a[48:51], a[88:91], 0\n"
"v_mul_f32 v122, v242, v122\n"
"v_mul_f32 v123, v242, v123\n"
"v_cvt_pk_bf16_f32 v230, v120, v121\n"
"v_cvt_pk_bf16_f32 v231, v122, v123\n"
"v_mul_f32 v132, v242, v132\n"
"v_mul_f32 v133, v242, v133\n"
"v_mul_f32 v134, v242, v134\n"
"v_mfma_f32_16x16x32_bf16 v[12:15], a[52:55], a[88:91], 0\n"
"v_mul_f32 v135, v242, v135\n"
"v_cvt_pk_bf16_f32 v232, v132, v133\n"
"v_cvt_pk_bf16_f32 v233, v134, v135\n"
"buffer_store_dwordx4 v[226:229], v223, s[56:59], s83 offen sc1\n"
"s_add_u32 s83, s82, 0x104000\n"
"buffer_store_dwordx4 v[230:233], v223, s[56:59], s83 offen sc1\n"
"v_mul_f32 v144, v242, v144\n"
"v_mfma_f32_16x16x32_bf16 v[16:19], a[48:51], a[92:95], 0\n"
"v_mul_f32 v145, v242, v145\n"
"v_mul_f32 v146, v242, v146\n"
"v_mul_f32 v147, v242, v147\n"
"v_cvt_pk_bf16_f32 v234, v144, v145\n"
"v_cvt_pk_bf16_f32 v235, v146, v147\n"
"v_mul_f32 v156, v242, v156\n"
"v_mul_f32 v157, v242, v157\n"
"v_mfma_f32_16x16x32_bf16 v[20:23], a[52:55], a[92:95], 0\n"
"v_mul_f32 v158, v242, v158\n"
"v_mul_f32 v159, v242, v159\n"
"v_cvt_pk_bf16_f32 v236, v156, v157\n"
"v_cvt_pk_bf16_f32 v237, v158, v159\n"
"v_mul_f32 v168, v242, v168\n"
"v_mul_f32 v169, v242, v169\n"
"v_mul_f32 v170, v242, v170\n"
"v_mfma_f32_16x16x32_bf16 v[24:27], a[48:51], a[96:99], 0\n"
"v_mul_f32 v171, v242, v171\n"
"v_cvt_pk_bf16_f32 v238, v168, v169\n"
"v_cvt_pk_bf16_f32 v239, v170, v171\n"
"v_mul_f32 v180, v242, v180\n"
"v_mul_f32 v181, v242, v181\n"
"v_mul_f32 v182, v242, v182\n"
"v_mul_f32 v183, v242, v183\n"
"v_mfma_f32_16x16x32_bf16 v[28:31], a[52:55], a[96:99], 0\n"
"v_cvt_pk_bf16_f32 v240, v180, v181\n"
"v_cvt_pk_bf16_f32 v241, v182, v183\n"
"v_mul_f32 v100, v243, v100\n"
"v_mul_f32 v101, v243, v101\n"
"v_mul_f32 v102, v243, v102\n"
"v_mul_f32 v103, v243, v103\n"
"v_mul_f32 v112, v243, v112\n"
"v_mfma_f32_16x16x32_bf16 v[32:35], a[48:51], a[100:103], 0\n"
"v_mul_f32 v113, v243, v113\n"
"v_mul_f32 v114, v243, v114\n"
"v_mul_f32 v115, v243, v115\n"
"v_mul_f32 v124, v243, v124\n"
"v_mul_f32 v125, v243, v125\n"
"v_mul_f32 v126, v243, v126\n"
"v_mul_f32 v127, v243, v127\n"
"v_mfma_f32_16x16x32_bf16 v[36:39], a[52:55], a[100:103], 0\n"
"v_mul_f32 v136, v243, v136\n"
"v_mul_f32 v137, v243, v137\n"
"v_mul_f32 v138, v243, v138\n"
"v_mul_f32 v139, v243, v139\n"
"ds_write_b128 v250, v[226:229] offset:0\n"
"v_cvt_pk_bf16_f32 v226, v100, v101\n"
"v_mfma_f32_16x16x32_bf16 v[40:43], a[48:51], a[104:107], 0\n"
"v_cvt_pk_bf16_f32 v227, v102, v103\n"
"v_cvt_pk_bf16_f32 v228, v112, v113\n"
"v_cvt_pk_bf16_f32 v229, v114, v115\n"
"ds_write_b128 v250, v[230:233] offset:16384\n"
"v_cvt_pk_bf16_f32 v230, v124, v125\n"
"v_cvt_pk_bf16_f32 v231, v126, v127\n"
"v_mfma_f32_16x16x32_bf16 v[44:47], a[52:55], a[104:107], 0\n"
"v_cvt_pk_bf16_f32 v232, v136, v137\n"
"v_cvt_pk_bf16_f32 v233, v138, v139\n"
"s_add_u32 s83, s82, 0x108000\n"
"buffer_store_dwordx4 v[234:237], v223, s[56:59], s83 offen sc1\n"
"s_add_u32 s83, s82, 0x10c000\n"
"buffer_store_dwordx4 v[238:241], v223, s[56:59], s83 offen sc1\n"
"s_add_u32 s83, s82, 0x100400\n"
"v_mfma_f32_16x16x32_bf16 v[48:51], a[48:51], a[108:111], 0\n"
"buffer_store_dwordx4 v[226:229], v223, s[56:59], s83 offen sc1\n"
"s_add_u32 s83, s82, 0x104400\n"
"buffer_store_dwordx4 v[230:233], v223, s[56:59], s83 offen sc1\n"
"v_rcp_f32 v244, v200\n"
"ds_write_b128 v250, v[226:229] offset:1024\n"
"v_mfma_f32_16x16x32_bf16 v[52:55], a[52:55], a[108:111], 0\n"
"v_mul_f32 v104, v244, v104\n"
"v_mul_f32 v105, v244, v105\n"
"v_mul_f32 v106, v244, v106\n"
"v_mul_f32 v107, v244, v107\n"
"v_cvt_pk_bf16_f32 v226, v104, v105\n"
"v_cvt_pk_bf16_f32 v227, v106, v107\n"
"v_mul_f32 v116, v244, v116\n"
"v_mfma_f32_16x16x32_bf16 v[56:59], a[48:51], a[112:115], 0\n"
"v_mul_f32 v117, v244, v117\n"
"v_mul_f32 v118, v244, v118\n"
"v_mul_f32 v119, v244, v119\n"
"v_cvt_pk_bf16_f32 v228, v116, v117\n"
"v_cvt_pk_bf16_f32 v229, v118, v119\n"
"v_mul_f32 v128, v244, v128\n"
"v_mul_f32 v129, v244, v129\n"
"v_mfma_f32_16x16x32_bf16 v[60:63], a[52:55], a[112:115], 0\n"
"v_mul_f32 v130, v244, v130\n"
"v_mul_f32 v131, v244, v131\n"
"v_mul_f32 v140, v244, v140\n"
"v_mul_f32 v141, v244, v141\n"
"v_mul_f32 v142, v244, v142\n"
"v_mul_f32 v143, v244, v143\n"
"v_mul_f32 v148, v243, v148\n"
"v_mfma_f32_16x16x32_bf16 v[64:67], a[48:51], a[116:119], 0\n"
"ds_write_b128 v250, v[230:233] offset:17408\n"
"v_cvt_pk_bf16_f32 v230, v128, v129\n"
"v_cvt_pk_bf16_f32 v231, v130, v131\n"
"v_cvt_pk_bf16_f32 v232, v140, v141\n"
"v_cvt_pk_bf16_f32 v233, v142, v143\n"
"v_mul_f32 v149, v243, v149\n"
"v_mfma_f32_16x16x32_bf16 v[68:71], a[52:55], a[116:119], 0\n"
"ds_write_b128 v250, v[226:229] offset:2048\n"
"ds_write_b128 v250, v[230:233] offset:18432\n"
"v_mul_f32 v150, v243, v150\n"
"v_mul_f32 v151, v243, v151\n"
"v_mul_f32 v160, v243, v160\n"
"v_mfma_f32_16x16x32_bf16 v[72:75], a[48:51], a[120:123], 0\n"
"v_mul_f32 v161, v243, v161\n"
"v_mul_f32 v162, v243, v162\n"
"v_mul_f32 v163, v243, v163\n"
"v_mul_f32 v172, v243, v172\n"
"v_mul_f32 v173, v243, v173\n"
"v_mul_f32 v174, v243, v174\n"
"v_mul_f32 v175, v243, v175\n"
"v_mfma_f32_16x16x32_bf16 v[76:79], a[52:55], a[120:123], 0\n"
"v_mul_f32 v184, v243, v184\n"
"v_mul_f32 v185, v243, v185\n"
"v_mul_f32 v186, v243, v186\n"
"v_mul_f32 v187, v243, v187\n"
"ds_write_b128 v248, v[234:237] offset:16384\n"
"v_cvt_pk_bf16_f32 v234, v148, v149\n"
"v_mfma_f32_16x16x32_bf16 v[80:83], a[48:51], a[124:127], 0\n"
"ds_write_b128 v249, v[238:241] offset:0\n"
"v_cvt_pk_bf16_f32 v235, v150, v151\n"
"v_cvt_pk_bf16_f32 v236, v160, v161\n"
"v_cvt_pk_bf16_f32 v237, v162, v163\n"
"v_cvt_pk_bf16_f32 v238, v172, v173\n"
"v_cvt_pk_bf16_f32 v239, v174, v175\n"
"v_mfma_f32_16x16x32_bf16 v[84:87], a[52:55], a[124:127], 0\n"
"v_cvt_pk_bf16_f32 v240, v184, v185\n"
"v_cvt_pk_bf16_f32 v241, v186, v187\n"
"s_add_u32 s83, s82, 0x108400\n"
"buffer_store_dwordx4 v[234:237], v223, s[56:59], s83 offen sc1\n"
"s_add_u32 s83, s82, 0x10c400\n"
"buffer_store_dwordx4 v[238:241], v223, s[56:59], s83 offen sc1\n"
"v_mul_f32 v152, v244, v152\n"
"v_mfma_f32_16x16x32_bf16 v[88:91], a[48:51], a[128:131], 0\n"
"v_mul_f32 v153, v244, v153\n"
"v_mul_f32 v154, v244, v154\n"
"v_mul_f32 v155, v244, v155\n"
"v_mul_f32 v164, v244, v164\n"
"v_mul_f32 v165, v244, v165\n"
"v_mul_f32 v166, v244, v166\n"
"v_mul_f32 v167, v244, v167\n"
"v_mfma_f32_16x16x32_bf16 v[92:95], a[52:55], a[128:131], 0\n"
"v_mul_f32 v176, v244, v176\n"
"v_mul_f32 v177, v244, v177\n"
"v_mul_f32 v178, v244, v178\n"
"v_mul_f32 v179, v244, v179\n"
"v_mul_f32 v188, v244, v188\n"
"v_mul_f32 v189, v244, v189\n"
"v_mul_f32 v190, v244, v190\n"
"ds_read_b64_tr_b16 a[56:57], v211 offset:8192\n"
"ds_read_b64_tr_b16 a[58:59], v211 offset:12288\n"
"ds_read_b64_tr_b16 a[60:61], v214 offset:8192\n"
"ds_read_b64_tr_b16 a[62:63], v214 offset:12288\n"
"s_waitcnt lgkmcnt(0)\n"
"s_barrier\n"
"v_mfma_f32_16x16x32_bf16 v[0:3], a[56:59], a[132:135], v[0:3]\n"
"v_mul_f32 v191, v244, v191\n"
"ds_write_b128 v248, v[234:237] offset:17408\n"
"v_cvt_pk_bf16_f32 v234, v152, v153\n"
"v_cvt_pk_bf16_f32 v235, v154, v155\n"
"v_cvt_pk_bf16_f32 v236, v164, v165\n"
"v_cvt_pk_bf16_f32 v237, v166, v167\n"
"v_mfma_f32_16x16x32_bf16 v[4:7], a[60:63], a[132:135], v[4:7]\n"
"ds_write_b128 v249, v[238:241] offset:1024\n"
"v_cvt_pk_bf16_f32 v238, v176, v177\n"
"v_cvt_pk_bf16_f32 v239, v178, v179\n"
"v_cvt_pk_bf16_f32 v240, v188, v189\n"
"v_cvt_pk_bf16_f32 v241, v190, v191\n"
"s_add_u32 s83, s82, 0x100800\n"
"v_mfma_f32_16x16x32_bf16 v[8:11], a[56:59], a[136:139], v[8:11]\n"
"ds_write_b128 v248, v[234:237] offset:18432\n"
"ds_write_b128 v249, v[238:241] offset:2048\n"
"ds_read_b128 a[84:87], v218 offset:53248\n"
"s_xor_b32 s39, s36, 0x8000\n"
"ds_read_b128 a[88:91], v218 offset:54272\n"
"v_mfma_f32_16x16x32_bf16 v[12:15], a[60:63], a[136:139], v[12:15]\n"
"buffer_store_dwordx4 v[226:229], v223, s[56:59], s83 offen sc1\n"
"ds_read_b128 a[92:95], v218 offset:55296\n"
"s_add_u32 s83, s82, 0x104800\n"
"buffer_store_dwordx4 v[230:233], v223, s[56:59], s83 offen sc1\n"
"s_add_u32 s83, s82, 0x108800\n"
"buffer_store_dwordx4 v[234:237], v223, s[56:59], s83 offen sc1\n"
"v_mfma_f32_16x16x32_bf16 v[16:19], a[56:59], a[140:143], v[16:19]\n"
"s_add_u32 s83, s82, 0x10c800\n"
"ds_read_b128 a[96:99], v218 offset:56320\n"
"buffer_store_dwordx4 v[238:241], v223, s[56:59], s83 offen sc1\n"
"ds_read_b128 a[100:103], v218 offset:57344\n"
"ds_read_b128 a[104:107], v218 offset:58368\n"
"ds_read_b128 a[108:111], v218 offset:59392\n"
"v_mfma_f32_16x16x32_bf16 v[20:23], a[60:63], a[140:143], v[20:23]\n"
"s_add_u32 m0, s38, 0x0\n"
"ds_read_b128 a[112:115], v218 offset:60416\n"
"buffer_load_dwordx4 v223, s[56:59], s39 offen lds\n"
"ds_read_b64_tr_b16 a[64:65], v211 offset:32768\n"
"ds_read_b64_tr_b16 a[66:67], v211 offset:36864\n"
"ds_read_b128 a[116:119], v219 offset:0\n"
"v_mfma_f32_16x16x32_bf16 v[24:27], a[56:59], a[80:83], v[24:27]\n"
"ds_read_b64_tr_b16 a[68:69], v214 offset:32768\n"
"ds_read_b64_tr_b16 a[70:71], v214 offset:36864\n"
"v_mfma_f32_16x16x32_bf16 v[28:31], a[60:63], a[80:83], v[28:31]\n"
"ds_read_b128 a[120:123], v219 offset:1024\n"
"ds_read_b128 a[124:127], v219 offset:2048\n"
"s_waitcnt lgkmcnt(12)\n"
"v_mfma_f32_16x16x32_bf16 v[32:35], a[56:59], a[84:87], v[32:35]\n"
"buffer_load_dwordx4 v223, s[56:59], s39 offen offset:1024 lds\n"
"ds_read_b128 a[128:131], v219 offset:3072\n"
"v_mfma_f32_16x16x32_bf16 v[36:39], a[60:63], a[84:87], v[36:39]\n"
"ds_read_b128 a[132:135], v219 offset:4096\n"
"ds_read_b128 a[136:139], v219 offset:5120\n"
"v_mfma_f32_16x16x32_bf16 v[40:43], a[56:59], a[88:91], v[40:43]\n"
"ds_read_b128 a[140:143], v219 offset:6144\n"
"ds_read_b128 a[80:83], v219 offset:7168\n"
"v_mfma_f32_16x16x32_bf16 v[44:47], a[60:63], a[88:91], v[44:47]\n"
"buffer_load_dwordx4 v223, s[56:59], s39 offen offset:2048 lds\n"
"s_xor_b32 s39, s36, 0xc000\n"
"v_mfma_f32_16x16x32_bf16 v[48:51], a[56:59], a[92:95], v[48:51]\n"
"ds_read_b128 a[84:87], v219 offset:8192\n"
"ds_read_b128 a[88:91], v219 offset:9216\n"
"v_mfma_f32_16x16x32_bf16 v[52:55], a[60:63], a[92:95], v[52:55]\n"
"ds_read_b128 a[92:95], v219 offset:10240\n"
"ds_read_b64_tr_b16 a[72:73], v211 offset:40960\n"
"s_add_u32 m0, s38, 0xc000\n"
"s_waitcnt lgkmcnt(6)\n"
"v_mfma_f32_16x16x32_bf16 v[56:59], a[56:59], a[96:99], v[56:59]\n"
"buffer_load_dwordx4 v223, s[56:59], s39 offen lds\n"
"v_mfma_f32_16x16x32_bf16 v[60:63], a[60:63], a[96:99], v[60:63]\n"
"ds_read_b128 a[96:99], v219 offset:11264\n"
"ds_read_b64_tr_b16 a[74:75], v211 offset:45056\n"
"v_mfma_f32_16x16x32_bf16 v[64:67], a[56:59], a[100:103], v[64:67]\n"
"ds_read_b64_tr_b16 a[76:77], v214 offset:40960\n"
"ds_read_b64_tr_b16 a[78:79], v214 offset:45056\n"
"v_mfma_f32_16x16x32_bf16 v[68:71], a[60:63], a[100:103], v[68:71]\n"
"buffer_load_dwordx4 v223, s[56:59], s39 offen offset:1024 lds\n"
"ds_read_b128 a[100:103], v219 offset:16384\n"
"v_mfma_f32_16x16x32_bf16 v[72:75], a[56:59], a[104:107], v[72:75]\n"
"v_add_u32 v212, s80, v212\n"
"v_add_u32 v215, s80, v215\n"
"v_mfma_f32_16x16x32_bf16 v[76:79], a[60:63], a[104:107], v[76:79]\n"
"ds_read_b128 a[104:107], v219 offset:17408\n"
"v_add_u32 v213, s80, v213\n"
"v_mfma_f32_16x16x32_bf16 v[80:83], a[56:59], a[108:111], v[80:83]\n"
"buffer_load_dwordx4 v223, s[56:59], s39 offen offset:2048 lds\n"
"v_add_u32 v216, s80, v216\n"
"v_mfma_f32_16x16x32_bf16 v[84:87], a[60:63], a[108:111], v[84:87]\n"
"ds_read_b128 a[108:111], v219 offset:18432\n"
"v_lshlrev_b32 v226, 6, v223\n"
"v_mfma_f32_16x16x32_bf16 v[88:91], a[56:59], a[112:115], v[88:91]\n"
"v_and_b32 v226, 0x3c00, v226\n"
"v_lshrrev_b32 v227, 8, v223\n"
"v_mfma_f32_16x16x32_bf16 v[92:95], a[60:63], a[112:115], v[92:95]\n"
"ds_read_b128 a[112:115], v219 offset:19456\n"
"v_lshl_add_u32 v226, v227, 3, v226\n"
"v_mfma_f32_16x16x32_bf16 v[0:3], a[64:67], a[116:119], v[0:3]\n"
"s_mov_b32 s22, 0x3f80\n"
"v_mfma_f32_16x16x32_bf16 v[4:7], a[68:71], a[116:119], v[4:7]\n"
"ds_read_b128 a[116:119], v219 offset:20480\n"
"v_mfma_f32_16x16x32_bf16 v[8:11], a[64:67], a[120:123], v[8:11]\n"
"v_mfma_f32_16x16x32_bf16 v[12:15], a[68:71], a[120:123], v[12:15]\n"
"ds_read_b128 a[120:123], v219 offset:21504\n"
"v_mfma_f32_16x16x32_bf16 v[16:19], a[64:67], a[124:127], v[16:19]\n"
"v_mfma_f32_16x16x32_bf16 v[20:23], a[68:71], a[124:127], v[20:23]\n"
"ds_read_b128 a[124:127], v219 offset:22528\n"
"v_mfma_f32_16x16x32_bf16 v[24:27], a[64:67], a[128:131], v[24:27]\n"
"v_mfma_f32_16x16x32_bf16 v[28:31], a[68:71], a[128:131], v[28:31]\n"
"ds_read_b128 a[128:131], v219 offset:23552\n"
"v_mfma_f32_16x16x32_bf16 v[32:35], a[64:67], a[132:135], v[32:35]\n"
"v_mfma_f32_16x16x32_bf16 v[36:39], a[68:71], a[132:135], v[36:39]\n"
"ds_read_b128 a[132:135], v219 offset:24576\n"
"v_mfma_f32_16x16x32_bf16 v[40:43], a[64:67], a[136:139], v[40:43]\n"
"v_mfma_f32_16x16x32_bf16 v[44:47], a[68:71], a[136:139], v[44:47]\n"
"ds_read_b128 a[136:139], v219 offset:25600\n"
"s_waitcnt lgkmcnt(2)\n"
"v_mfma_f32_16x16x32_bf16 v[48:51], a[64:67], a[140:143], v[48:51]\n"
"v_mfma_f32_16x16x32_bf16 v[52:55], a[68:71], a[140:143], v[52:55]\n"
"ds_read_b128 a[140:143], v219 offset:26624\n"
"v_mfma_f32_16x16x32_bf16 v[56:59], a[64:67], a[80:83], v[56:59]\n"
"v_mfma_f32_16x16x32_bf16 v[60:63], a[68:71], a[80:83], v[60:63]\n"
"ds_read_b128 a[80:83], v219 offset:27648\n"
"v_mfma_f32_16x16x32_bf16 v[64:67], a[64:67], a[84:87], v[64:67]\n"
"v_mfma_f32_16x16x32_bf16 v[68:71], a[68:71], a[84:87], v[68:71]\n"
"v_mfma_f32_16x16x32_bf16 v[72:75], a[64:67], a[88:91], v[72:75]\n"
"v_mfma_f32_16x16x32_bf16 v[76:79], a[68:71], a[88:91], v[76:79]\n"
"v_mfma_f32_16x16x32_bf16 v[80:83], a[64:67], a[92:95], v[80:83]\n"
"v_mfma_f32_16x16x32_bf16 v[84:87], a[68:71], a[92:95], v[84:87]\n"
"v_mfma_f32_16x16x32_bf16 v[88:91], a[64:67], a[96:99], v[88:91]\n"
"v_mfma_f32_16x16x32_bf16 v[92:95], a[68:71], a[96:99], v[92:95]\n"
"s_waitcnt lgkmcnt(0)\n"
"s_barrier\n"
"v_mfma_f32_16x16x32_bf16 v[0:3], a[72:75], a[100:103], v[0:3]\n"
"s_xor_b32 s81, s37, 0x20000\n"
"s_xor_b32 s39, s36, 0x10000\n"
"s_add_u32 m0, s77, 0x8000\n"
"v_mfma_f32_16x16x32_bf16 v[4:7], a[76:79], a[100:103], v[4:7]\n"
"buffer_load_dwordx4 v224, s[52:55], s81 offen lds\n"
"v_mfma_f32_16x16x32_bf16 v[8:11], a[72:75], a[104:107], v[8:11]\n"
"s_xor_b32 s81, s37, 0x22000\n"
"ds_read_b64_tr_b16 a[48:49], v212 offset:16384\n"
"v_mfma_f32_16x16x32_bf16 v[12:15], a[76:79], a[104:107], v[12:15]\n"
"ds_read_b64_tr_b16 a[50:51], v212 offset:20480\n"
"ds_read_b128 a[84:87], v217 offset:16384\n"
"s_add_u32 m0, s77, 0x8800\n"
"v_mfma_f32_16x16x32_bf16 v[16:19], a[72:75], a[108:111], v[16:19]\n"
"buffer_load_dwordx4 v224, s[52:55], s81 offen lds\n"
"v_mfma_f32_16x16x32_bf16 v[20:23], a[76:79], a[108:111], v[20:23]\n"
"s_xor_b32 s81, s37, 0x28000\n"
"ds_read_b64_tr_b16 a[52:53], v215 offset:16384\n"
"v_mfma_f32_16x16x32_bf16 v[24:27], a[72:75], a[112:115], v[24:27]\n"
"ds_read_b64_tr_b16 a[54:55], v215 offset:20480\n"
"ds_read_b128 a[88:91], v217 offset:17408\n"
"s_add_u32 m0, s77, 0xa000\n"
"v_mfma_f32_16x16x32_bf16 v[28:31], a[76:79], a[112:115], v[28:31]\n"
"buffer_load_dwordx4 v224, s[52:55], s81 offen lds\n"
"v_mfma_f32_16x16x32_bf16 v[32:35], a[72:75], a[116:119], v[32:35]\n"
"s_xor_b32 s81, s37, 0x2a000\n"
"ds_read_b128 a[92:95], v217 offset:18432\n"
"v_mfma_f32_16x16x32_bf16 v[36:39], a[76:79], a[116:119], v[36:39]\n"
"ds_read_b128 a[96:99], v217 offset:19456\n"
"ds_read_b128 a[100:103], v217 offset:20480\n"
"s_add_u32 m0, s77, 0xa800\n"
"v_mfma_f32_16x16x32_bf16 v[40:43], a[72:75], a[120:123], v[40:43]\n"
"buffer_load_dwordx4 v224, s[52:55], s81 offen lds\n"
"v_mfma_f32_16x16x32_bf16 v[44:47], a[76:79], a[120:123], v[44:47]\n"
"ds_read_b128 a[104:107], v217 offset:21504\n"
"ds_read_b128 a[108:111], v217 offset:22528\n"
"v_mfma_f32_16x16x32_bf16 v[48:51], a[72:75], a[124:127], v[48:51]\n"
"ds_read_b128 a[112:115], v217 offset:23552\n"
"ds_read_b128 a[116:119], v217 offset:24576\n"
"s_add_u32 m0, s38, 0x20000\n"
"v_mfma_f32_16x16x32_bf16 v[52:55], a[76:79], a[124:127], v[52:55]\n"
"buffer_load_dwordx4 v223, s[56:59], s39 offen lds\n"
"v_mfma_f32_16x16x32_bf16 v[56:59], a[72:75], a[128:131], v[56:59]\n"
"ds_read_b128 a[120:123], v217 offset:25600\n"
"ds_read_b128 a[124:127], v217 offset:26624\n"
"v_mfma_f32_16x16x32_bf16 v[60:63], a[76:79], a[128:131], v[60:63]\n"
"ds_read_b128 a[128:131], v217 offset:27648\n"
"ds_read_b64_tr_b16 a[56:57], v212 offset:24576\n"
"v_mfma_f32_16x16x32_bf16 v[64:67], a[72:75], a[132:135], v[64:67]\n"
"buffer_load_dwordx4 v223, s[56:59], s39 offen offset:1024 lds\n"
"ds_read_b64_tr_b16 a[58:59], v212 offset:28672\n"
"v_mfma_f32_16x16x32_bf16 v[68:71], a[76:79], a[132:135], v[68:71]\n"
"ds_read_b128 a[132:135], v218 offset:0\n"
"ds_read_b64_tr_b16 a[60:61], v215 offset:24576\n"
"v_mfma_f32_16x16x32_bf16 v[72:75], a[72:75], a[136:139], v[72:75]\n"
"ds_read_b64_tr_b16 a[62:63], v215 offset:28672\n"
"v_mfma_f32_16x16x32_bf16 v[76:79], a[76:79], a[136:139], v[76:79]\n"
"buffer_load_dwordx4 v223, s[56:59], s39 offen offset:2048 lds\n"
"s_xor_b32 s39, s36, 0x14000\n"
"v_mfma_f32_16x16x32_bf16 v[80:83], a[72:75], a[140:143], v[80:83]\n"
"ds_read_b128 a[136:139], v218 offset:1024\n"
"v_mfma_f32_16x16x32_bf16 v[84:87], a[76:79], a[140:143], v[84:87]\n"
"ds_read_b128 a[140:143], v218 offset:2048\n"
"s_add_u32 m0, s38, 0x1c000\n"
"v_mfma_f32_16x16x32_bf16 v[88:91], a[72:75], a[80:83], v[88:91]\n"
"buffer_load_dwordx4 v223, s[56:59], s39 offen lds\n"
"v_mfma_f32_16x16x32_bf16 v[92:95], a[76:79], a[80:83], v[92:95]\n"
"ds_read_b128 a[80:83], v218 offset:3072\n"
"s_waitcnt lgkmcnt(3)\n"
"v_mfma_f32_16x16x32_bf16 v[0:3], a[48:51], a[84:87], v[0:3]\n"
"v_mfma_f32_16x16x32_bf16 v[4:7], a[52:55], a[84:87], v[4:7]\n"
"buffer_load_dwordx4 v223, s[56:59], s39 offen offset:1024 lds\n"
"ds_read_b128 a[84:87], v218 offset:4096\n"
"v_mfma_f32_16x16x32_bf16 v[8:11], a[48:51], a[88:91], v[8:11]\n"
"v_mfma_f32_16x16x32_bf16 v[12:15], a[52:55], a[88:91], v[12:15]\n"
"ds_read_b128 a[88:91], v218 offset:5120\n"
"v_mfma_f32_16x16x32_bf16 v[16:19], a[48:51], a[92:95], v[16:19]\n"
"buffer_load_dwordx4 v223, s[56:59], s39 offen offset:2048 lds\n"
"v_mfma_f32_16x16x32_bf16 v[20:23], a[52:55], a[92:95], v[20:23]\n"
"ds_read_b128 a[92:95], v218 offset:6144\n"
"v_mfma_f32_16x16x32_bf16 v[24:27], a[48:51], a[96:99], v[24:27]\n"
"v_mfma_f32_16x16x32_bf16 v[28:31], a[52:55], a[96:99], v[28:31]\n"
"ds_read_b128 a[96:99], v218 offset:7168\n"
"v_mfma_f32_16x16x32_bf16 v[32:35], a[48:51], a[100:103], v[32:35]\n"
"v_mfma_f32_16x16x32_bf16 v[36:39], a[52:55], a[100:103], v[36:39]\n"
"ds_read_b128 a[100:103], v218 offset:8192\n"
"v_mfma_f32_16x16x32_bf16 v[40:43], a[48:51], a[104:107], v[40:43]\n"
"v_mfma_f32_16x16x32_bf16 v[44:47], a[52:55], a[104:107], v[44:47]\n"
"ds_read_b128 a[104:107], v218 offset:9216\n"
"v_mfma_f32_16x16x32_bf16 v[48:51], a[48:51], a[108:111], v[48:51]\n"
"v_mfma_f32_16x16x32_bf16 v[52:55], a[52:55], a[108:111], v[52:55]\n"
"ds_read_b128 a[108:111], v218 offset:10240\n"
"v_mfma_f32_16x16x32_bf16 v[56:59], a[48:51], a[112:115], v[56:59]\n"
"v_mfma_f32_16x16x32_bf16 v[60:63], a[52:55], a[112:115], v[60:63]\n"
"ds_read_b128 a[112:115], v218 offset:11264\n"
"v_mfma_f32_16x16x32_bf16 v[64:67], a[48:51], a[116:119], v[64:67]\n"
"v_mfma_f32_16x16x32_bf16 v[68:71], a[52:55], a[116:119], v[68:71]\n"
"v_mfma_f32_16x16x32_bf16 v[72:75], a[48:51], a[120:123], v[72:75]\n"
"v_mfma_f32_16x16x32_bf16 v[76:79], a[52:55], a[120:123], v[76:79]\n"
"v_mfma_f32_16x16x32_bf16 v[80:83], a[48:51], a[124:127], v[80:83]\n"
"v_mfma_f32_16x16x32_bf16 v[84:87], a[52:55], a[124:127], v[84:87]\n"
"v_mfma_f32_16x16x32_bf16 v[88:91], a[48:51], a[128:131], v[88:91]\n"
"v_mfma_f32_16x16x32_bf16 v[92:95], a[52:55], a[128:131], v[92:95]\n"
"s_waitcnt vmcnt(10) lgkmcnt(0)\n"
"s_barrier\n"
"v_mov_b32 v246, s90\n"
"v_mfma_f32_16x16x32_bf16 v[0:3], a[56:59], a[132:135], v[0:3]\n"
"v_mov_b32 v247, s91\n"
"buffer_store_dwordx2 v[246:247], v223, s[64:67], 0 offen sc1\n"
"v_mfma_f32_16x16x32_bf16 v[4:7], a[60:63], a[132:135], v[4:7]\n"
"s_xor_b32 s81, s37, 0x30000\n"
"s_xor_b32 s39, s36, 0x18000\n"
"v_mfma_f32_16x16x32_bf16 v[8:11], a[56:59], a[136:139], v[8:11]\n"
"s_add_u32 s64, s64, 0x800\n"
"s_addc_u32 s65, s65, 0\n"
"s_add_u32 m0, s77, 0x14000\n"
"v_mfma_f32_16x16x32_bf16 v[12:15], a[60:63], a[136:139], v[12:15]\n"
"buffer_load_dwordx4 v224, s[52:55], s81 offen lds\n"
"v_mfma_f32_16x16x32_bf16 v[16:19], a[56:59], a[140:143], v[16:19]\n"
"s_xor_b32 s81, s37, 0x32000\n"
"ds_read_b64_tr_b16 a[64:65], v212 offset:32768\n"
"v_mfma_f32_16x16x32_bf16 v[20:23], a[60:63], a[140:143], v[20:23]\n"
"ds_read_b64_tr_b16 a[66:67], v212 offset:36864\n"
"ds_read_b128 a[116:119], v217 offset:0\n"
"s_add_u32 m0, s77, 0x14800\n"
"v_mfma_f32_16x16x32_bf16 v[24:27], a[56:59], a[80:83], v[24:27]\n"
"buffer_load_dwordx4 v224, s[52:55], s81 offen lds\n"
"v_mfma_f32_16x16x32_bf16 v[28:31], a[60:63], a[80:83], v[28:31]\n"
"s_xor_b32 s81, s37, 0x38000\n"
"ds_read_b64_tr_b16 a[68:69], v215 offset:32768\n"
"v_mfma_f32_16x16x32_bf16 v[32:35], a[56:59], a[84:87], v[32:35]\n"
"ds_read_b64_tr_b16 a[70:71], v215 offset:36864\n"
"ds_read_b128 a[120:123], v217 offset:1024\n"
"s_add_u32 m0, s77, 0x16000\n"
"v_mfma_f32_16x16x32_bf16 v[36:39], a[60:63], a[84:87], v[36:39]\n"
"buffer_load_dwordx4 v224, s[52:55], s81 offen lds\n"
"v_mfma_f32_16x16x32_bf16 v[40:43], a[56:59], a[88:91], v[40:43]\n"
"s_xor_b32 s81, s37, 0x3a000\n"
"ds_read_b128 a[124:127], v217 offset:2048\n"
"v_mfma_f32_16x16x32_bf16 v[44:47], a[60:63], a[88:91], v[44:47]\n"
"ds_read_b128 a[128:131], v217 offset:3072\n"
"ds_read_b128 a[132:135], v217 offset:4096\n"
"s_add_u32 m0, s77, 0x16800\n"
"v_mfma_f32_16x16x32_bf16 v[48:51], a[56:59], a[92:95], v[48:51]\n"
"buffer_load_dwordx4 v224, s[52:55], s81 offen lds\n"
"v_mfma_f32_16x16x32_bf16 v[52:55], a[60:63], a[92:95], v[52:55]\n"
"ds_read_b128 a[136:139], v217 offset:5120\n"
"ds_read_b128 a[140:143], v217 offset:6144\n"
"v_mfma_f32_16x16x32_bf16 v[56:59], a[56:59], a[96:99], v[56:59]\n"
"ds_read_b128 a[80:83], v217 offset:7168\n"
"ds_read_b128 a[84:87], v217 offset:8192\n"
"s_add_u32 m0, s38, 0x4000\n"
"v_mfma_f32_16x16x32_bf16 v[60:63], a[60:63], a[96:99], v[60:63]\n"
"buffer_load_dwordx4 v223, s[56:59], s39 offen lds\n"
"v_mfma_f32_16x16x32_bf16 v[64:67], a[56:59], a[100:103], v[64:67]\n"
"ds_read_b128 a[88:91], v217 offset:9216\n"
"ds_read_b128 a[92:95], v217 offset:10240\n"
"v_mfma_f32_16x16x32_bf16 v[68:71], a[60:63], a[100:103], v[68:71]\n"
"ds_read_b128 a[96:99], v217 offset:11264\n"
"ds_read_b64_tr_b16 a[72:73], v212 offset:40960\n"
"v_mfma_f32_16x16x32_bf16 v[72:75], a[56:59], a[104:107], v[72:75]\n"
"buffer_load_dwordx4 v223, s[56:59], s39 offen offset:1024 lds\n"
"ds_read_b64_tr_b16 a[74:75], v212 offset:45056\n"
"v_mfma_f32_16x16x32_bf16 v[76:79], a[60:63], a[104:107], v[76:79]\n"
"ds_read_b128 a[100:103], v217 offset:49152\n"
"ds_read_b64_tr_b16 a[76:77], v215 offset:40960\n"
"v_mfma_f32_16x16x32_bf16 v[80:83], a[56:59], a[108:111], v[80:83]\n"
"ds_read_b64_tr_b16 a[78:79], v215 offset:45056\n"
"ds_read_b128 a[104:107], v217 offset:50176\n"
"v_mfma_f32_16x16x32_bf16 v[84:87], a[60:63], a[108:111], v[84:87]\n"
"buffer_load_dwordx4 v223, s[56:59], s39 offen offset:2048 lds\n"
"s_xor_b32 s39, s36, 0x1c000\n"
"v_mfma_f32_16x16x32_bf16 v[88:91], a[56:59], a[112:115], v[88:91]\n"
"ds_read_b128 a[108:111], v217 offset:51200\n"
"v_mfma_f32_16x16x32_bf16 v[92:95], a[60:63], a[112:115], v[92:95]\n"
"ds_read_b128 a[112:115], v217 offset:52224\n"
"s_add_u32 m0, s38, 0x24000\n"
"s_waitcnt lgkmcnt(4)\n"
"v_mfma_f32_16x16x32_bf16 v[0:3], a[64:67], a[116:119], v[0:3]\n"
"buffer_load_dwordx4 v223, s[56:59], s39 offen lds\n"
"v_mfma_f32_16x16x32_bf16 v[4:7], a[68:71], a[116:119], v[4:7]\n"
"ds_read_b128 a[116:119], v217 offset:53248\n"
"v_mfma_f32_16x16x32_bf16 v[8:11], a[64:67], a[120:123], v[8:11]\n"
"v_mfma_f32_16x16x32_bf16 v[12:15], a[68:71], a[120:123], v[12:15]\n"
"buffer_load_dwordx4 v223, s[56:59], s39 offen offset:1024 lds\n"
"ds_read_b128 a[120:123], v217 offset:54272\n"
"v_mfma_f32_16x16x32_bf16 v[16:19], a[64:67], a[124:127], v[16:19]\n"
"v_mfma_f32_16x16x32_bf16 v[20:23], a[68:71], a[124:127], v[20:23]\n"
"ds_read_b128 a[124:127], v217 offset:55296\n"
"v_mfma_f32_16x16x32_bf16 v[24:27], a[64:67], a[128:131], v[24:27]\n"
"buffer_load_dwordx4 v223, s[56:59], s39 offen offset:2048 lds\n"
"v_mfma_f32_16x16x32_bf16 v[28:31], a[68:71], a[128:131], v[28:31]\n"
"ds_read_b128 a[128:131], v217 offset:56320\n"
"v_mfma_f32_16x16x32_bf16 v[32:35], a[64:67], a[132:135], v[32:35]\n"
"v_mfma_f32_16x16x32_bf16 v[36:39], a[68:71], a[132:135], v[36:39]\n"
"ds_read_b128 a[132:135], v217 offset:57344\n"
"v_mfma_f32_16x16x32_bf16 v[40:43], a[64:67], a[136:139], v[40:43]\n"
"v_mfma_f32_16x16x32_bf16 v[44:47], a[68:71], a[136:139], v[44:47]\n"
"ds_read_b128 a[136:139], v217 offset:58368\n"
"v_mfma_f32_16x16x32_bf16 v[48:51], a[64:67], a[140:143], v[48:51]\n"
"v_mfma_f32_16x16x32_bf16 v[52:55], a[68:71], a[140:143], v[52:55]\n"
"ds_read_b128 a[140:143], v217 offset:59392\n"
"v_mfma_f32_16x16x32_bf16 v[56:59], a[64:67], a[80:83], v[56:59]\n"
"v_mfma_f32_16x16x32_bf16 v[60:63], a[68:71], a[80:83], v[60:63]\n"
"ds_read_b128 a[80:83], v217 offset:60416\n"
"v_mfma_f32_16x16x32_bf16 v[64:67], a[64:67], a[84:87], v[64:67]\n"
"v_mfma_f32_16x16x32_bf16 v[68:71], a[68:71], a[84:87], v[68:71]\n"
"v_mfma_f32_16x16x32_bf16 v[72:75], a[64:67], a[88:91], v[72:75]\n"
"v_mfma_f32_16x16x32_bf16 v[76:79], a[68:71], a[88:91], v[76:79]\n"
"v_mfma_f32_16x16x32_bf16 v[80:83], a[64:67], a[92:95], v[80:83]\n"
"v_mfma_f32_16x16x32_bf16 v[84:87], a[68:71], a[92:95], v[84:87]\n"
"v_mfma_f32_16x16x32_bf16 v[88:91], a[64:67], a[96:99], v[88:91]\n"
"v_mfma_f32_16x16x32_bf16 v[92:95], a[68:71], a[96:99], v[92:95]\n"
"s_waitcnt vmcnt(11) lgkmcnt(0)\n"
"s_barrier\n"
"v_mfma_f32_16x16x32_bf16 v[0:3], a[72:75], a[100:103], v[0:3]\n"
"s_xor_b32 s81, s37, 0x40000\n"
"s_xor_b32 s39, s36, 0x20000\n"
"s_add_u32 m0, s77, 0x18000\n"
"v_mfma_f32_16x16x32_bf16 v[4:7], a[76:79], a[100:103], v[4:7]\n"
"buffer_load_dwordx4 v224, s[52:55], s81 offen lds\n"
"v_mfma_f32_16x16x32_bf16 v[8:11], a[72:75], a[104:107], v[8:11]\n"
"s_xor_b32 s81, s37, 0x42000\n"
"ds_read_b64_tr_b16 a[48:49], v211 offset:32768\n"
"v_mfma_f32_16x16x32_bf16 v[12:15], a[76:79], a[104:107], v[12:15]\n"
"ds_read_b64_tr_b16 a[50:51], v211 offset:36864\n"
"ds_read_b128 a[84:87], v219 offset:0\n"
"s_add_u32 m0, s77, 0x18800\n"
"v_mfma_f32_16x16x32_bf16 v[16:19], a[72:75], a[108:111], v[16:19]\n"
"buffer_load_dwordx4 v224, s[52:55], s81 offen lds\n"
"v_mfma_f32_16x16x32_bf16 v[20:23], a[76:79], a[108:111], v[20:23]\n"
"s_xor_b32 s81, s37, 0x48000\n"
"ds_read_b64_tr_b16 a[52:53], v214 offset:32768\n"
"v_mfma_f32_16x16x32_bf16 v[24:27], a[72:75], a[112:115], v[24:27]\n"
"ds_read_b64_tr_b16 a[54:55], v214 offset:36864\n"
"ds_read_b128 a[88:91], v219 offset:1024\n"
"s_add_u32 m0, s77, 0x1a000\n"
"v_mfma_f32_16x16x32_bf16 v[28:31], a[76:79], a[112:115], v[28:31]\n"
"buffer_load_dwordx4 v224, s[52:55], s81 offen lds\n"
"v_mfma_f32_16x16x32_bf16 v[32:35], a[72:75], a[116:119], v[32:35]\n"
"s_xor_b32 s81, s37, 0x4a000\n"
"ds_read_b128 a[92:95], v219 offset:2048\n"
"v_mfma_f32_16x16x32_bf16 v[36:39], a[76:79], a[116:119], v[36:39]\n"
"ds_read_b128 a[96:99], v219 offset:3072\n"
"ds_read_b128 a[100:103], v219 offset:4096\n"
"s_add_u32 m0, s77, 0x1a800\n"
"v_mfma_f32_16x16x32_bf16 v[40:43], a[72:75], a[120:123], v[40:43]\n"
"buffer_load_dwordx4 v224, s[52:55], s81 offen lds\n"
"v_mfma_f32_16x16x32_bf16 v[44:47], a[76:79], a[120:123], v[44:47]\n"
"ds_read_b128 a[104:107], v219 offset:5120\n"
"ds_read_b128 a[108:111], v219 offset:6144\n"
"v_mfma_f32_16x16x32_bf16 v[48:51], a[72:75], a[124:127], v[48:51]\n"
"ds_read_b128 a[112:115], v219 offset:7168\n"
"ds_read_b128 a[116:119], v219 offset:8192\n"
"s_add_u32 m0, s38, 0x0\n"
"v_mfma_f32_16x16x32_bf16 v[52:55], a[76:79], a[124:127], v[52:55]\n"
"buffer_load_dwordx4 v223, s[56:59], s39 offen lds\n"
"v_mfma_f32_16x16x32_bf16 v[56:59], a[72:75], a[128:131], v[56:59]\n"
"ds_read_b128 a[120:123], v219 offset:9216\n"
"ds_read_b128 a[124:127], v219 offset:10240\n"
"v_mfma_f32_16x16x32_bf16 v[60:63], a[76:79], a[128:131], v[60:63]\n"
"ds_read_b128 a[128:131], v219 offset:11264\n"
"ds_read_b64_tr_b16 a[56:57], v211 offset:40960\n"
"v_mfma_f32_16x16x32_bf16 v[64:67], a[72:75], a[132:135], v[64:67]\n"
"buffer_load_dwordx4 v223, s[56:59], s39 offen offset:1024 lds\n"
"ds_read_b64_tr_b16 a[58:59], v211 offset:45056\n"
"v_mfma_f32_16x16x32_bf16 v[68:71], a[76:79], a[132:135], v[68:71]\n"
"ds_read_b128 a[132:135], v218 offset:49152\n"
"ds_read_b64_tr_b16 a[60:61], v214 offset:40960\n"
"v_mfma_f32_16x16x32_bf16 v[72:75], a[72:75], a[136:139], v[72:75]\n"
"ds_read_b64_tr_b16 a[62:63], v214 offset:45056\n"
"v_mfma_f32_16x16x32_bf16 v[76:79], a[76:79], a[136:139], v[76:79]\n"
"buffer_load_dwordx4 v223, s[56:59], s39 offen offset:2048 lds\n"
"s_xor_b32 s39, s36, 0x24000\n"
"v_mfma_f32_16x16x32_bf16 v[80:83], a[72:75], a[140:143], v[80:83]\n"
"ds_read_b128 a[136:139], v218 offset:50176\n"
"v_mfma_f32_16x16x32_bf16 v[84:87], a[76:79], a[140:143], v[84:87]\n"
"ds_read_b128 a[140:143], v218 offset:51200\n"
"s_add_u32 m0, s38, 0x10000\n"
"v_mfma_f32_16x16x32_bf16 v[88:91], a[72:75], a[80:83], v[88:91]\n"
"buffer_load_dwordx4 v223, s[56:59], s39 offen lds\n"
"v_mfma_f32_16x16x32_bf16 v[92:95], a[76:79], a[80:83], v[92:95]\n"
"ds_read_b128 a[80:83], v218 offset:52224\n"
"s_waitcnt lgkmcnt(3)\n"
"v_mfma_f32_16x16x32_bf16 v[0:3], a[48:51], a[84:87], v[0:3]\n"
"v_mfma_f32_16x16x32_bf16 v[4:7], a[52:55], a[84:87], v[4:7]\n"
"buffer_load_dwordx4 v223, s[56:59], s39 offen offset:1024 lds\n"
"ds_read_b128 a[84:87], v218 offset:53248\n"
"v_mfma_f32_16x16x32_bf16 v[8:11], a[48:51], a[88:91], v[8:11]\n"
"v_mfma_f32_16x16x32_bf16 v[12:15], a[52:55], a[88:91], v[12:15]\n"
"ds_read_b128 a[88:91], v218 offset:54272\n"
"v_mfma_f32_16x16x32_bf16 v[16:19], a[48:51], a[92:95], v[16:19]\n"
"buffer_load_dwordx4 v223, s[56:59], s39 offen offset:2048 lds\n"
"v_mfma_f32_16x16x32_bf16 v[20:23], a[52:55], a[92:95], v[20:23]\n"
"ds_read_b128 a[92:95], v218 offset:55296\n"
"v_mfma_f32_16x16x32_bf16 v[24:27], a[48:51], a[96:99], v[24:27]\n"
"v_mfma_f32_16x16x32_bf16 v[28:31], a[52:55], a[96:99], v[28:31]\n"
"ds_read_b128 a[96:99], v218 offset:56320\n"
"v_mfma_f32_16x16x32_bf16 v[32:35], a[48:51], a[100:103], v[32:35]\n"
"v_mfma_f32_16x16x32_bf16 v[36:39], a[52:55], a[100:103], v[36:39]\n"
"ds_read_b128 a[100:103], v218 offset:57344\n"
"v_mfma_f32_16x16x32_bf16 v[40:43], a[48:51], a[104:107], v[40:43]\n"
"v_mfma_f32_16x16x32_bf16 v[44:47], a[52:55], a[104:107], v[44:47]\n"
"ds_read_b128 a[104:107], v218 offset:58368\n"
"v_mfma_f32_16x16x32_bf16 v[48:51], a[48:51], a[108:111], v[48:51]\n"
"v_mfma_f32_16x16x32_bf16 v[52:55], a[52:55], a[108:111], v[52:55]\n"
"ds_read_b128 a[108:111], v218 offset:59392\n"
"v_mfma_f32_16x16x32_bf16 v[56:59], a[48:51], a[112:115], v[56:59]\n"
"v_mfma_f32_16x16x32_bf16 v[60:63], a[52:55], a[112:115], v[60:63]\n"
"ds_read_b128 a[112:115], v218 offset:60416\n"
"v_mfma_f32_16x16x32_bf16 v[64:67], a[48:51], a[116:119], v[64:67]\n"
"v_mfma_f32_16x16x32_bf16 v[68:71], a[52:55], a[116:119], v[68:71]\n"
"v_mfma_f32_16x16x32_bf16 v[72:75], a[48:51], a[120:123], v[72:75]\n"
"v_mfma_f32_16x16x32_bf16 v[76:79], a[52:55], a[120:123], v[76:79]\n"
"v_mfma_f32_16x16x32_bf16 v[80:83], a[48:51], a[124:127], v[80:83]\n"
"v_mfma_f32_16x16x32_bf16 v[84:87], a[52:55], a[124:127], v[84:87]\n"
"v_mfma_f32_16x16x32_bf16 v[88:91], a[48:51], a[128:131], v[88:91]\n"
"v_mfma_f32_16x16x32_bf16 v[92:95], a[52:55], a[128:131], v[92:95]\n"
"s_waitcnt vmcnt(10) lgkmcnt(0)\n"
"s_barrier\n"
"v_mfma_f32_16x16x32_bf16 v[0:3], a[56:59], a[132:135], v[0:3]\n"
"s_xor_b32 s81, s37, 0x50000\n"
"s_xor_b32 s39, s36, 0x28000\n"
"s_add_u32 m0, s77, 0x8000\n"
"v_mfma_f32_16x16x32_bf16 v[4:7], a[60:63], a[132:135], v[4:7]\n"
"buffer_load_dwordx4 v224, s[52:55], s81 offen lds\n"
"v_mfma_f32_16x16x32_bf16 v[8:11], a[56:59], a[136:139], v[8:11]\n"
"s_xor_b32 s81, s37, 0x52000\n"
"ds_read_b64_tr_b16 a[64:65], v212 offset:16384\n"
"v_mfma_f32_16x16x32_bf16 v[12:15], a[60:63], a[136:139], v[12:15]\n"
"ds_read_b64_tr_b16 a[66:67], v212 offset:20480\n"
"ds_read_b128 a[116:119], v217 offset:16384\n"
"s_add_u32 m0, s77, 0x8800\n"
"v_mfma_f32_16x16x32_bf16 v[16:19], a[56:59], a[140:143], v[16:19]\n"
"buffer_load_dwordx4 v224, s[52:55], s81 offen lds\n"
"v_mfma_f32_16x16x32_bf16 v[20:23], a[60:63], a[140:143], v[20:23]\n"
"s_xor_b32 s81, s37, 0x58000\n"
"ds_read_b64_tr_b16 a[68:69], v215 offset:16384\n"
"v_mfma_f32_16x16x32_bf16 v[24:27], a[56:59], a[80:83], v[24:27]\n"
"ds_read_b64_tr_b16 a[70:71], v215 offset:20480\n"
"ds_read_b128 a[120:123], v217 offset:17408\n"
"s_add_u32 m0, s77, 0xa000\n"
"v_mfma_f32_16x16x32_bf16 v[28:31], a[60:63], a[80:83], v[28:31]\n"
"buffer_load_dwordx4 v224, s[52:55], s81 offen lds\n"
"v_mfma_f32_16x16x32_bf16 v[32:35], a[56:59], a[84:87], v[32:35]\n"
"s_xor_b32 s81, s37, 0x5a000\n"
"ds_read_b128 a[124:127], v217 offset:18432\n"
"v_mfma_f32_16x16x32_bf16 v[36:39], a[60:63], a[84:87], v[36:39]\n"
"ds_read_b128 a[128:131], v217 offset:19456\n"
"ds_read_b128 a[132:135], v217 offset:20480\n"
"s_add_u32 m0, s77, 0xa800\n"
"v_mfma_f32_16x16x32_bf16 v[40:43], a[56:59], a[88:91], v[40:43]\n"
"buffer_load_dwordx4 v224, s[52:55], s81 offen lds\n"
"v_mfma_f32_16x16x32_bf16 v[44:47], a[60:63], a[88:91], v[44:47]\n"
"ds_read_b128 a[136:139], v217 offset:21504\n"
"ds_read_b128 a[140:143], v217 offset:22528\n"
"v_mfma_f32_16x16x32_bf16 v[48:51], a[56:59], a[92:95], v[48:51]\n"
"ds_read_b128 a[80:83], v217 offset:23552\n"
"ds_read_b128 a[84:87], v217 offset:24576\n"
"s_add_u32 m0, s38, 0x20000\n"
"v_mfma_f32_16x16x32_bf16 v[52:55], a[60:63], a[92:95], v[52:55]\n"
"buffer_load_dwordx4 v223, s[56:59], s39 offen lds\n"
"v_mfma_f32_16x16x32_bf16 v[56:59], a[56:59], a[96:99], v[56:59]\n"
"ds_read_b128 a[88:91], v217 offset:25600\n"
"ds_read_b128 a[92:95], v217 offset:26624\n"
"v_mfma_f32_16x16x32_bf16 v[60:63], a[60:63], a[96:99], v[60:63]\n"
"ds_read_b128 a[96:99], v217 offset:27648\n"
"ds_read_b64_tr_b16 a[72:73], v212 offset:24576\n"
"v_mfma_f32_16x16x32_bf16 v[64:67], a[56:59], a[100:103], v[64:67]\n"
"buffer_load_dwordx4 v223, s[56:59], s39 offen offset:1024 lds\n"
"ds_read_b64_tr_b16 a[74:75], v212 offset:28672\n"
"v_mfma_f32_16x16x32_bf16 v[68:71], a[60:63], a[100:103], v[68:71]\n"
"ds_read_b128 a[100:103], v219 offset:16384\n"
"ds_read_b64_tr_b16 a[76:77], v215 offset:24576\n"
"v_mfma_f32_16x16x32_bf16 v[72:75], a[56:59], a[104:107], v[72:75]\n"
"ds_read_b64_tr_b16 a[78:79], v215 offset:28672\n"
"v_mfma_f32_16x16x32_bf16 v[76:79], a[60:63], a[104:107], v[76:79]\n"
"buffer_load_dwordx4 v223, s[56:59], s39 offen offset:2048 lds\n"
"s_xor_b32 s39, s36, 0x2c000\n"
"v_mfma_f32_16x16x32_bf16 v[80:83], a[56:59], a[108:111], v[80:83]\n"
"ds_read_b128 a[104:107], v219 offset:17408\n"
"v_mfma_f32_16x16x32_bf16 v[84:87], a[60:63], a[108:111], v[84:87]\n"
"ds_read_b128 a[108:111], v219 offset:18432\n"
"s_add_u32 m0, s38, 0xc000\n"
"v_mfma_f32_16x16x32_bf16 v[88:91], a[56:59], a[112:115], v[88:91]\n"
"buffer_load_dwordx4 v223, s[56:59], s39 offen lds\n"
"v_mfma_f32_16x16x32_bf16 v[92:95], a[60:63], a[112:115], v[92:95]\n"
"ds_read_b128 a[112:115], v219 offset:19456\n"
"s_waitcnt lgkmcnt(3)\n"
"v_mfma_f32_16x16x32_bf16 v[0:3], a[64:67], a[116:119], v[0:3]\n"
"v_mfma_f32_16x16x32_bf16 v[4:7], a[68:71], a[116:119], v[4:7]\n"
"buffer_load_dwordx4 v223, s[56:59], s39 offen offset:1024 lds\n"
"ds_read_b128 a[116:119], v219 offset:20480\n"
"v_mfma_f32_16x16x32_bf16 v[8:11], a[64:67], a[120:123], v[8:11]\n"
"v_mfma_f32_16x16x32_bf16 v[12:15], a[68:71], a[120:123], v[12:15]\n"
"ds_read_b128 a[120:123], v219 offset:21504\n"
"v_mfma_f32_16x16x32_bf16 v[16:19], a[64:67], a[124:127], v[16:19]\n"
"buffer_load_dwordx4 v223, s[56:59], s39 offen offset:2048 lds\n"
"v_mfma_f32_16x16x32_bf16 v[20:23], a[68:71], a[124:127], v[20:23]\n"
"ds_read_b128 a[124:127], v219 offset:22528\n"
"v_mfma_f32_16x16x32_bf16 v[24:27], a[64:67], a[128:131], v[24:27]\n"
"v_mfma_f32_16x16x32_bf16 v[28:31], a[68:71], a[128:131], v[28:31]\n"
"ds_read_b128 a[128:131], v219 offset:23552\n"
"v_mfma_f32_16x16x32_bf16 v[32:35], a[64:67], a[132:135], v[32:35]\n"
"v_mfma_f32_16x16x32_bf16 v[36:39], a[68:71], a[132:135], v[36:39]\n"
"ds_read_b128 a[132:135], v219 offset:24576\n"
"v_mfma_f32_16x16x32_bf16 v[40:43], a[64:67], a[136:139], v[40:43]\n"
"v_mfma_f32_16x16x32_bf16 v[44:47], a[68:71], a[136:139], v[44:47]\n"
"ds_read_b128 a[136:139], v219 offset:25600\n"
"v_mfma_f32_16x16x32_bf16 v[48:51], a[64:67], a[140:143], v[48:51]\n"
"v_mfma_f32_16x16x32_bf16 v[52:55], a[68:71], a[140:143], v[52:55]\n"
"ds_read_b128 a[140:143], v219 offset:26624\n"
"v_mfma_f32_16x16x32_bf16 v[56:59], a[64:67], a[80:83], v[56:59]\n"
"v_mfma_f32_16x16x32_bf16 v[60:63], a[68:71], a[80:83], v[60:63]\n"
"ds_read_b128 a[80:83], v219 offset:27648\n"
"v_mfma_f32_16x16x32_bf16 v[64:67], a[64:67], a[84:87], v[64:67]\n"
"v_mfma_f32_16x16x32_bf16 v[68:71], a[68:71], a[84:87], v[68:71]\n"
"buffer_load_dwordx2 v[232:233], v225, s[68:71], s92 offen offset:2048 sc1\n"
"v_mfma_f32_16x16x32_bf16 v[72:75], a[64:67], a[88:91], v[72:75]\n"
"v_mfma_f32_16x16x32_bf16 v[76:79], a[68:71], a[88:91], v[76:79]\n"
"v_mfma_f32_16x16x32_bf16 v[80:83], a[64:67], a[92:95], v[80:83]\n"
"v_mfma_f32_16x16x32_bf16 v[84:87], a[68:71], a[92:95], v[84:87]\n"
"v_mfma_f32_16x16x32_bf16 v[88:91], a[64:67], a[96:99], v[88:91]\n"
"v_mfma_f32_16x16x32_bf16 v[92:95], a[68:71], a[96:99], v[92:95]\n"
"s_waitcnt vmcnt(11) lgkmcnt(0)\n"
"s_barrier\n"
"s_xor_b32 s81, s37, 0x60000\n"
"s_add_u32 m0, s77, 0x14000\n"
"v_mfma_f32_16x16x32_bf16 v[0:3], a[72:75], a[100:103], v[0:3]\n"
"buffer_load_dwordx4 v224, s[52:55], s81 offen lds\n"
"s_xor_b32 s81, s37, 0x62000\n"
"v_mfma_f32_16x16x32_bf16 v[4:7], a[76:79], a[100:103], v[4:7]\n"
"s_xor_b32 s39, s36, 0x30000\n"
"ds_read_b64_tr_b16 a[48:49], v212 offset:32768\n"
"v_mfma_f32_16x16x32_bf16 v[8:11], a[72:75], a[104:107], v[8:11]\n"
"ds_read_b64_tr_b16 a[50:51], v212 offset:36864\n"
"ds_read_b128 a[84:87], v217 offset:0\n"
"s_add_u32 m0, s77, 0x14800\n"
"v_mfma_f32_16x16x32_bf16 v[12:15], a[76:79], a[104:107], v[12:15]\n"
"buffer_load_dwordx4 v224, s[52:55], s81 offen lds\n"
"s_xor_b32 s81, s37, 0x68000\n"
"v_mfma_f32_16x16x32_bf16 v[16:19], a[72:75], a[108:111], v[16:19]\n"
"ds_read_b64_tr_b16 a[52:53], v215 offset:32768\n"
"ds_read_b64_tr_b16 a[54:55], v215 offset:36864\n"
"v_mfma_f32_16x16x32_bf16 v[20:23], a[76:79], a[108:111], v[20:23]\n"
"ds_read_b128 a[88:91], v217 offset:1024\n"
"ds_read_b128 a[92:95], v217 offset:2048\n"
"s_add_u32 m0, s77, 0x16000\n"
"v_mfma_f32_16x16x32_bf16 v[24:27], a[72:75], a[112:115], v[24:27]\n"
"buffer_load_dwordx4 v224, s[52:55], s81 offen lds\n"
"s_xor_b32 s81, s37, 0x6a000\n"
"v_mfma_f32_16x16x32_bf16 v[28:31], a[76:79], a[112:115], v[28:31]\n"
"ds_read_b128 a[96:99], v217 offset:3072\n"
"ds_read_b128 a[100:103], v217 offset:4096\n"
"v_mfma_f32_16x16x32_bf16 v[32:35], a[72:75], a[116:119], v[32:35]\n"
"ds_read_b128 a[104:107], v217 offset:5120\n"
"ds_read_b128 a[108:111], v217 offset:6144\n"
"s_add_u32 m0, s77, 0x16800\n"
"v_mfma_f32_16x16x32_bf16 v[36:39], a[76:79], a[116:119], v[36:39]\n"
"buffer_load_dwordx4 v224, s[52:55], s81 offen lds\n"
"v_mfma_f32_16x16x32_bf16 v[40:43], a[72:75], a[120:123], v[40:43]\n"
"ds_read_b128 a[112:115], v217 offset:7168\n"
"ds_read_b128 a[116:119], v217 offset:8192\n"
"v_mfma_f32_16x16x32_bf16 v[44:47], a[76:79], a[120:123], v[44:47]\n"
"ds_read_b128 a[120:123], v217 offset:9216\n"
"ds_read_b64_tr_b16 a[56:57], v212 offset:40960\n"
"s_add_u32 m0, s38, 0x4000\n"
"v_mfma_f32_16x16x32_bf16 v[48:51], a[72:75], a[124:127], v[48:51]\n"
"buffer_load_dwordx4 v223, s[56:59], s39 offen lds\n"
"v_mfma_f32_16x16x32_bf16 v[52:55], a[76:79], a[124:127], v[52:55]\n"
"ds_read_b128 a[124:127], v217 offset:10240\n"
"ds_read_b64_tr_b16 a[58:59], v212 offset:45056\n"
"v_mfma_f32_16x16x32_bf16 v[56:59], a[72:75], a[128:131], v[56:59]\n"
"ds_read_b64_tr_b16 a[60:61], v215 offset:40960\n"
"ds_read_b64_tr_b16 a[62:63], v215 offset:45056\n"
"v_mfma_f32_16x16x32_bf16 v[60:63], a[76:79], a[128:131], v[60:63]\n"
"buffer_load_dwordx4 v223, s[56:59], s39 offen offset:1024 lds\n"
"ds_read_b128 a[128:131], v217 offset:11264\n"
"v_mfma_f32_16x16x32_bf16 v[64:67], a[72:75], a[132:135], v[64:67]\n"
"v_mfma_f32_16x16x32_bf16 v[68:71], a[76:79], a[132:135], v[68:71]\n"
"ds_read_b128 a[132:135], v218 offset:0\n"
"v_mfma_f32_16x16x32_bf16 v[72:75], a[72:75], a[136:139], v[72:75]\n"
"buffer_load_dwordx4 v223, s[56:59], s39 offen offset:2048 lds\n"
"s_xor_b32 s39, s36, 0x34000\n"
"v_mfma_f32_16x16x32_bf16 v[76:79], a[76:79], a[136:139], v[76:79]\n"
"ds_read_b128 a[136:139], v218 offset:1024\n"
"v_mfma_f32_16x16x32_bf16 v[80:83], a[72:75], a[140:143], v[80:83]\n"
"s_add_u32 m0, s38, 0x1c000\n"
"v_mfma_f32_16x16x32_bf16 v[84:87], a[76:79], a[140:143], v[84:87]\n"
"buffer_load_dwordx4 v223, s[56:59], s39 offen lds\n"
"v_mfma_f32_16x16x32_bf16 v[88:91], a[72:75], a[80:83], v[88:91]\n"
"ds_read_b128 a[140:143], v218 offset:2048\n"
"v_mfma_f32_16x16x32_bf16 v[92:95], a[76:79], a[80:83], v[92:95]\n"
"buffer_load_dwordx4 v223, s[56:59], s39 offen offset:1024 lds\n"
"s_waitcnt vmcnt(9)\n"
"v_cmp_ne_u64 vcc, s[90:91], v[232:233]\n"
"buffer_load_dwordx4 v223, s[56:59], s39 offen offset:2048 lds\n"
"s_nop 2\n"
"s_cmp_lg_u32 s76, 0\n"
"s_cbranch_scc1 LretB_%=\n"
"s_cbranch_vccnz LspinB_%=\n"
"buffer_inv sc1\n"
"LretB_%=:\n"
"s_waitcnt lgkmcnt(1)\n"
"v_mfma_f32_16x16x32_bf16 v[0:3], a[48:51], a[84:87], v[0:3]\n"
"ds_read_b128 a[80:83], v218 offset:3072\n"
"v_lshlrev_b32 v225, 1, v223\n"
"v_mfma_f32_16x16x32_bf16 v[4:7], a[52:55], a[84:87], v[4:7]\n"
"ds_read_b128 a[84:87], v218 offset:4096\n"
"v_and_b32 v225, 0x60, v225\n"
"v_mfma_f32_16x16x32_bf16 v[8:11], a[48:51], a[88:91], v[8:11]\n"
"v_mfma_f32_16x16x32_bf16 v[12:15], a[52:55], a[88:91], v[12:15]\n"
"ds_read_b128 a[88:91], v218 offset:5120\n"
"v_mfma_f32_16x16x32_bf16 v[16:19], a[48:51], a[92:95], v[16:19]\n"
"v_mfma_f32_16x16x32_bf16 v[20:23], a[52:55], a[92:95], v[20:23]\n"
"ds_read_b128 a[92:95], v218 offset:6144\n"
"v_mfma_f32_16x16x32_bf16 v[24:27], a[48:51], a[96:99], v[24:27]\n"
"v_mfma_f32_16x16x32_bf16 v[28:31], a[52:55], a[96:99], v[28:31]\n"
"ds_read_b128 a[96:99], v218 offset:7168\n"
"v_mfma_f32_16x16x32_bf16 v[32:35], a[48:51], a[100:103], v[32:35]\n"
"v_mfma_f32_16x16x32_bf16 v[36:39], a[52:55], a[100:103], v[36:39]\n"
"ds_read_b128 a[100:103], v218 offset:8192\n"
"v_mfma_f32_16x16x32_bf16 v[40:43], a[48:51], a[104:107], v[40:43]\n"
"v_mfma_f32_16x16x32_bf16 v[44:47], a[52:55], a[104:107], v[44:47]\n"
"ds_read_b128 a[104:107], v218 offset:9216\n"
"v_mfma_f32_16x16x32_bf16 v[48:51], a[48:51], a[108:111], v[48:51]\n"
"v_mfma_f32_16x16x32_bf16 v[52:55], a[52:55], a[108:111], v[52:55]\n"
"ds_read_b128 a[108:111], v218 offset:10240\n"
"v_mfma_f32_16x16x32_bf16 v[56:59], a[48:51], a[112:115], v[56:59]\n"
"v_mfma_f32_16x16x32_bf16 v[60:63], a[52:55], a[112:115], v[60:63]\n"
"ds_read_b128 a[112:115], v218 offset:11264\n"
"v_mfma_f32_16x16x32_bf16 v[64:67], a[48:51], a[116:119], v[64:67]\n"
"v_mfma_f32_16x16x32_bf16 v[68:71], a[52:55], a[116:119], v[68:71]\n"
"v_mfma_f32_16x16x32_bf16 v[72:75], a[48:51], a[120:123], v[72:75]\n"
"v_mfma_f32_16x16x32_bf16 v[76:79], a[52:55], a[120:123], v[76:79]\n"
"v_mfma_f32_16x16x32_bf16 v[80:83], a[48:51], a[124:127], v[80:83]\n"
"v_mfma_f32_16x16x32_bf16 v[84:87], a[52:55], a[124:127], v[84:87]\n"
"v_mfma_f32_16x16x32_bf16 v[88:91], a[48:51], a[128:131], v[88:91]\n"
"v_mfma_f32_16x16x32_bf16 v[92:95], a[52:55], a[128:131], v[92:95]\n"
"s_waitcnt lgkmcnt(0)\n"
"s_barrier\n"
"v_mfma_f32_16x16x32_bf16 v[0:3], a[56:59], a[132:135], v[0:3]\n"
"s_xor_b32 s81, s37, 0x70000\n"
"s_xor_b32 s39, s36, 0x38000\n"
"s_add_u32 m0, s77, 0x18000\n"
"v_mfma_f32_16x16x32_bf16 v[4:7], a[60:63], a[132:135], v[4:7]\n"
"buffer_load_dwordx4 v224, s[52:55], s81 offen lds\n"
"v_mfma_f32_16x16x32_bf16 v[8:11], a[56:59], a[136:139], v[8:11]\n"
"s_xor_b32 s81, s37, 0x72000\n"
"ds_read_b64_tr_b16 a[64:65], v211 offset:32768\n"
"v_mfma_f32_16x16x32_bf16 v[12:15], a[60:63], a[136:139], v[12:15]\n"
"ds_read_b64_tr_b16 a[66:67], v211 offset:36864\n"
"ds_read_b128 a[116:119], v219 offset:0\n"
"s_add_u32 m0, s77, 0x18800\n"
"v_mfma_f32_16x16x32_bf16 v[16:19], a[56:59], a[140:143], v[16:19]\n"
"buffer_load_dwordx4 v224, s[52:55], s81 offen lds\n"
"v_mfma_f32_16x16x32_bf16 v[20:23], a[60:63], a[140:143], v[20:23]\n"
"s_xor_b32 s81, s37, 0x78000\n"
"ds_read_b64_tr_b16 a[68:69], v214 offset:32768\n"
"v_mfma_f32_16x16x32_bf16 v[24:27], a[56:59], a[80:83], v[24:27]\n"
"ds_read_b64_tr_b16 a[70:71], v214 offset:36864\n"
"ds_read_b128 a[120:123], v219 offset:1024\n"
"s_add_u32 m0, s77, 0x1a000\n"
"v_mfma_f32_16x16x32_bf16 v[28:31], a[60:63], a[80:83], v[28:31]\n"
"buffer_load_dwordx4 v224, s[52:55], s81 offen lds\n"
"v_mfma_f32_16x16x32_bf16 v[32:35], a[56:59], a[84:87], v[32:35]\n"
"s_xor_b32 s81, s37, 0x7a000\n"
"ds_read_b128 a[124:127], v219 offset:2048\n"
"v_mfma_f32_16x16x32_bf16 v[36:39], a[60:63], a[84:87], v[36:39]\n"
"ds_read_b128 a[128:131], v219 offset:3072\n"
"ds_read_b128 a[132:135], v219 offset:4096\n"
"s_add_u32 m0, s77, 0x1a800\n"
"v_mfma_f32_16x16x32_bf16 v[40:43], a[56:59], a[88:91], v[40:43]\n"
"buffer_load_dwordx4 v224, s[52:55], s81 offen lds\n"
"v_mfma_f32_16x16x32_bf16 v[44:47], a[60:63], a[88:91], v[44:47]\n"
"ds_read_b128 a[136:139], v219 offset:5120\n"
"ds_read_b128 a[140:143], v219 offset:6144\n"
"v_mfma_f32_16x16x32_bf16 v[48:51], a[56:59], a[92:95], v[48:51]\n"
"ds_read_b128 a[80:83], v219 offset:7168\n"
"ds_read_b128 a[84:87], v219 offset:8192\n"
"s_add_u32 m0, s38, 0x0\n"
"v_mfma_f32_16x16x32_bf16 v[52:55], a[60:63], a[92:95], v[52:55]\n"
"buffer_load_dwordx4 v223, s[56:59], s39 offen lds\n"
"v_mfma_f32_16x16x32_bf16 v[56:59], a[56:59], a[96:99], v[56:59]\n"
"ds_read_b128 a[88:91], v219 offset:9216\n"
"ds_read_b128 a[92:95], v219 offset:10240\n"
"v_mfma_f32_16x16x32_bf16 v[60:63], a[60:63], a[96:99], v[60:63]\n"
"ds_read_b128 a[96:99], v219 offset:11264\n"
"ds_read_b64_tr_b16 a[72:73], v211 offset:40960\n"
"v_mfma_f32_16x16x32_bf16 v[64:67], a[56:59], a[100:103], v[64:67]\n"
"buffer_load_dwordx4 v223, s[56:59], s39 offen offset:1024 lds\n"
"ds_read_b64_tr_b16 a[74:75], v211 offset:45056\n"
"v_mfma_f32_16x16x32_bf16 v[68:71], a[60:63], a[100:103], v[68:71]\n"
"ds_read_b128 a[100:103], v217 offset:49152\n"
"ds_read_b64_tr_b16 a[76:77], v214 offset:40960\n"
"v_mfma_f32_16x16x32_bf16 v[72:75], a[56:59], a[104:107], v[72:75]\n"
"ds_read_b64_tr_b16 a[78:79], v214 offset:45056\n"
"v_mfma_f32_16x16x32_bf16 v[76:79], a[60:63], a[104:107], v[76:79]\n"
"buffer_load_dwordx4 v223, s[56:59], s39 offen offset:2048 lds\n"
"s_xor_b32 s39, s36, 0x3c000\n"
"v_mfma_f32_16x16x32_bf16 v[80:83], a[56:59], a[108:111], v[80:83]\n"
"ds_read_b128 a[104:107], v217 offset:50176\n"
"v_mfma_f32_16x16x32_bf16 v[84:87], a[60:63], a[108:111], v[84:87]\n"
"ds_read_b128 a[108:111], v217 offset:51200\n"
"s_add_u32 m0, s38, 0x24000\n"
"v_mfma_f32_16x16x32_bf16 v[88:91], a[56:59], a[112:115], v[88:91]\n"
"buffer_load_dwordx4 v223, s[56:59], s39 offen lds\n"
"v_mfma_f32_16x16x32_bf16 v[92:95], a[60:63], a[112:115], v[92:95]\n"
"ds_read_b128 a[112:115], v217 offset:52224\n"
"s_waitcnt lgkmcnt(3)\n"
"v_mfma_f32_16x16x32_bf16 v[0:3], a[64:67], a[116:119], v[0:3]\n"
"v_mfma_f32_16x16x32_bf16 v[4:7], a[68:71], a[116:119], v[4:7]\n"
"buffer_load_dwordx4 v223, s[56:59], s39 offen offset:1024 lds\n"
"ds_read_b128 a[116:119], v217 offset:53248\n"
"v_mfma_f32_16x16x32_bf16 v[8:11], a[64:67], a[120:123], v[8:11]\n"
"v_mfma_f32_16x16x32_bf16 v[12:15], a[68:71], a[120:123], v[12:15]\n"
"ds_read_b128 a[120:123], v217 offset:54272\n"
"v_mfma_f32_16x16x32_bf16 v[16:19], a[64:67], a[124:127], v[16:19]\n"
"buffer_load_dwordx4 v223, s[56:59], s39 offen offset:2048 lds\n"
"v_mfma_f32_16x16x32_bf16 v[20:23], a[68:71], a[124:127], v[20:23]\n"
"ds_read_b128 a[124:127], v217 offset:55296\n"
"v_mfma_f32_16x16x32_bf16 v[24:27], a[64:67], a[128:131], v[24:27]\n"
"v_mfma_f32_16x16x32_bf16 v[28:31], a[68:71], a[128:131], v[28:31]\n"
"ds_read_b128 a[128:131], v217 offset:56320\n"
"v_mfma_f32_16x16x32_bf16 v[32:35], a[64:67], a[132:135], v[32:35]\n"
"v_mfma_f32_16x16x32_bf16 v[36:39], a[68:71], a[132:135], v[36:39]\n"
"ds_read_b128 a[132:135], v217 offset:57344\n"
"v_mfma_f32_16x16x32_bf16 v[40:43], a[64:67], a[136:139], v[40:43]\n"
"v_mfma_f32_16x16x32_bf16 v[44:47], a[68:71], a[136:139], v[44:47]\n"
"ds_read_b128 a[136:139], v217 offset:58368\n"
"v_mfma_f32_16x16x32_bf16 v[48:51], a[64:67], a[140:143], v[48:51]\n"
"v_mfma_f32_16x16x32_bf16 v[52:55], a[68:71], a[140:143], v[52:55]\n"
"ds_read_b128 a[140:143], v217 offset:59392\n"
"v_mfma_f32_16x16x32_bf16 v[56:59], a[64:67], a[80:83], v[56:59]\n"
"v_mfma_f32_16x16x32_bf16 v[60:63], a[68:71], a[80:83], v[60:63]\n"
"ds_read_b128 a[80:83], v217 offset:60416\n"
"v_mfma_f32_16x16x32_bf16 v[64:67], a[64:67], a[84:87], v[64:67]\n"
"v_mfma_f32_16x16x32_bf16 v[68:71], a[68:71], a[84:87], v[68:71]\n"
"v_mfma_f32_16x16x32_bf16 v[72:75], a[64:67], a[88:91], v[72:75]\n"
"v_mfma_f32_16x16x32_bf16 v[76:79], a[68:71], a[88:91], v[76:79]\n"
"v_mfma_f32_16x16x32_bf16 v[80:83], a[64:67], a[92:95], v[80:83]\n"
"v_mfma_f32_16x16x32_bf16 v[84:87], a[68:71], a[92:95], v[84:87]\n"
"v_mfma_f32_16x16x32_bf16 v[88:91], a[64:67], a[96:99], v[88:91]\n"
"v_mfma_f32_16x16x32_bf16 v[92:95], a[68:71], a[96:99], v[92:95]\n"
"s_waitcnt vmcnt(10) lgkmcnt(0)\n"
"s_barrier\n"
"v_mfma_f32_16x16x32_bf16 v[0:3], a[72:75], a[100:103], v[0:3]\n"
"s_xor_b32 s81, s37, 0x220000\n"
"ds_read_b64_tr_b16 a[48:49], v212 offset:16384\n"
"s_add_u32 m0, s77, 0x8000\n"
"v_mfma_f32_16x16x32_bf16 v[4:7], a[76:79], a[100:103], v[4:7]\n"
"buffer_load_dwordx4 v224, s[52:55], s81 offen lds\n"
"v_mfma_f32_16x16x32_bf16 v[8:11], a[72:75], a[104:107], v[8:11]\n"
"s_xor_b32 s81, s37, 0x222000\n"
"ds_read_b64_tr_b16 a[50:51], v212 offset:20480\n"
"v_mfma_f32_16x16x32_bf16 v[12:15], a[76:79], a[104:107], v[12:15]\n"
"ds_read_b128 v[100:103], v217 offset:16384\n"
"ds_read_b64_tr_b16 a[52:53], v215 offset:16384\n"
"s_add_u32 m0, s77, 0x8800\n"
"v_mfma_f32_16x16x32_bf16 v[16:19], a[72:75], a[108:111], v[16:19]\n"
"buffer_load_dwordx4 v224, s[52:55], s81 offen lds\n"
"v_mfma_f32_16x16x32_bf16 v[20:23], a[76:79], a[108:111], v[20:23]\n"
"s_xor_b32 s81, s37, 0x228000\n"
"ds_read_b64_tr_b16 a[54:55], v215 offset:20480\n"
"v_mfma_f32_16x16x32_bf16 v[24:27], a[72:75], a[112:115], v[24:27]\n"
"ds_read_b128 v[104:107], v217 offset:17408\n"
"ds_read_b128 v[108:111], v217 offset:18432\n"
"s_add_u32 m0, s77, 0xa000\n"
"v_mfma_f32_16x16x32_bf16 v[28:31], a[76:79], a[112:115], v[28:31]\n"
"buffer_load_dwordx4 v224, s[52:55], s81 offen lds\n"
"v_mfma_f32_16x16x32_bf16 v[32:35], a[72:75], a[116:119], v[32:35]\n"
"s_xor_b32 s81, s37, 0x22a000\n"
"ds_read_b128 v[112:115], v217 offset:19456\n"
"v_mfma_f32_16x16x32_bf16 v[36:39], a[76:79], a[116:119], v[36:39]\n"
"ds_read_b128 v[116:119], v217 offset:20480\n"
"ds_read_b128 v[120:123], v217 offset:21504\n"
"s_add_u32 m0, s77, 0xa800\n"
"v_mfma_f32_16x16x32_bf16 v[40:43], a[72:75], a[120:123], v[40:43]\n"
"buffer_load_dwordx4 v224, s[52:55], s81 offen lds\n"
"v_mfma_f32_16x16x32_bf16 v[44:47], a[76:79], a[120:123], v[44:47]\n"
"ds_read_b128 v[124:127], v217 offset:22528\n"
"ds_read_b128 v[128:131], v217 offset:23552\n"
"v_mfma_f32_16x16x32_bf16 v[48:51], a[72:75], a[124:127], v[48:51]\n"
"ds_read_b128 v[132:135], v217 offset:24576\n"
"ds_read_b128 v[136:139], v217 offset:25600\n"
"v_mfma_f32_16x16x32_bf16 v[52:55], a[76:79], a[124:127], v[52:55]\n"
"ds_read_b128 v[140:143], v217 offset:26624\n"
"ds_read_b128 v[144:147], v217 offset:27648\n"
"v_mfma_f32_16x16x32_bf16 v[56:59], a[72:75], a[128:131], v[56:59]\n"
"ds_read_b64_tr_b16 a[56:57], v212 offset:24576\n"
"ds_read_b64_tr_b16 a[58:59], v212 offset:28672\n"
"v_mfma_f32_16x16x32_bf16 v[60:63], a[76:79], a[128:131], v[60:63]\n"
"ds_read_b128 v[148:151], v218 offset:49152\n"
"ds_read_b64_tr_b16 a[60:61], v215 offset:24576\n"
"v_mfma_f32_16x16x32_bf16 v[64:67], a[72:75], a[132:135], v[64:67]\n"
"ds_read_b64_tr_b16 a[62:63], v215 offset:28672\n"
"ds_read_b128 v[152:155], v218 offset:50176\n"
"v_mfma_f32_16x16x32_bf16 v[68:71], a[76:79], a[132:135], v[68:71]\n"
"ds_read_b128 v[156:159], v218 offset:51200\n"
"ds_read_b128 v[96:99], v218 offset:52224\n"
"v_mfma_f32_16x16x32_bf16 v[72:75], a[72:75], a[136:139], v[72:75]\n"
"v_mfma_f32_16x16x32_bf16 v[76:79], a[76:79], a[136:139], v[76:79]\n"
"v_mfma_f32_16x16x32_bf16 v[80:83], a[72:75], a[140:143], v[80:83]\n"
"v_mfma_f32_16x16x32_bf16 v[84:87], a[76:79], a[140:143], v[84:87]\n"
"v_mfma_f32_16x16x32_bf16 v[88:91], a[72:75], a[80:83], v[88:91]\n"
"v_mfma_f32_16x16x32_bf16 v[92:95], a[76:79], a[80:83], v[92:95]\n"
"s_waitcnt lgkmcnt(0)\n"
"v_mfma_f32_16x16x32_bf16 v[0:3], a[48:51], v[100:103], v[0:3]\n"
"v_mfma_f32_16x16x32_bf16 v[4:7], a[52:55], v[100:103], v[4:7]\n"
"ds_read_b128 v[100:103], v218 offset:53248\n"
"v_mfma_f32_16x16x32_bf16 v[8:11], a[48:51], v[104:107], v[8:11]\n"
"v_mfma_f32_16x16x32_bf16 v[12:15], a[52:55], v[104:107], v[12:15]\n"
"ds_read_b128 v[104:107], v218 offset:54272\n"
"v_mfma_f32_16x16x32_bf16 v[16:19], a[48:51], v[108:111], v[16:19]\n"
"v_mfma_f32_16x16x32_bf16 v[20:23], a[52:55], v[108:111], v[20:23]\n"
"ds_read_b128 v[108:111], v218 offset:55296\n"
"v_mfma_f32_16x16x32_bf16 v[24:27], a[48:51], v[112:115], v[24:27]\n"
"v_mfma_f32_16x16x32_bf16 v[28:31], a[52:55], v[112:115], v[28:31]\n"
"ds_read_b128 v[112:115], v218 offset:56320\n"
"v_mfma_f32_16x16x32_bf16 v[32:35], a[48:51], v[116:119], v[32:35]\n"
"v_mfma_f32_16x16x32_bf16 v[36:39], a[52:55], v[116:119], v[36:39]\n"
"ds_read_b128 v[116:119], v218 offset:57344\n"
"v_mfma_f32_16x16x32_bf16 v[40:43], a[48:51], v[120:123], v[40:43]\n"
"v_mfma_f32_16x16x32_bf16 v[44:47], a[52:55], v[120:123], v[44:47]\n"
"ds_read_b128 v[120:123], v218 offset:58368\n"
"v_mfma_f32_16x16x32_bf16 v[48:51], a[48:51], v[124:127], v[48:51]\n"
"v_mfma_f32_16x16x32_bf16 v[52:55], a[52:55], v[124:127], v[52:55]\n"
"ds_read_b128 v[124:127], v218 offset:59392\n"
"v_mfma_f32_16x16x32_bf16 v[56:59], a[48:51], v[128:131], v[56:59]\n"
"v_mfma_f32_16x16x32_bf16 v[60:63], a[52:55], v[128:131], v[60:63]\n"
"ds_read_b128 v[128:131], v218 offset:60416\n"
"v_mfma_f32_16x16x32_bf16 v[64:67], a[48:51], v[132:135], v[64:67]\n"
"v_mfma_f32_16x16x32_bf16 v[68:71], a[52:55], v[132:135], v[68:71]\n"
"v_mfma_f32_16x16x32_bf16 v[72:75], a[48:51], v[136:139], v[72:75]\n"
"v_mfma_f32_16x16x32_bf16 v[76:79], a[52:55], v[136:139], v[76:79]\n"
"v_mfma_f32_16x16x32_bf16 v[80:83], a[48:51], v[140:143], v[80:83]\n"
"v_mfma_f32_16x16x32_bf16 v[84:87], a[52:55], v[140:143], v[84:87]\n"
"v_mfma_f32_16x16x32_bf16 v[88:91], a[48:51], v[144:147], v[88:91]\n"
"v_mfma_f32_16x16x32_bf16 v[92:95], a[52:55], v[144:147], v[92:95]\n"
"s_waitcnt vmcnt(0) lgkmcnt(0)\n"
"s_barrier\n"
"s_xor_b32 s39, s36, 0x110000\n"
"s_add_u32 m0, s38, 0x20000\n"
"v_mfma_f32_16x16x32_bf16 v[0:3], a[56:59], v[148:151], v[0:3]\n"
"buffer_load_dwordx4 v223, s[56:59], s39 offen lds\n"
"v_mfma_f32_16x16x32_bf16 v[4:7], a[60:63], v[148:151], v[4:7]\n"
"s_xor_b32 s81, s37, 0x230000\n"
"ds_read_b64_tr_b16 a[64:65], v212 offset:32768\n"
"v_mfma_f32_16x16x32_bf16 v[8:11], a[56:59], v[152:155], v[8:11]\n"
"ds_read_b64_tr_b16 a[66:67], v212 offset:36864\n"
"ds_read_b128 v[132:135], v217 offset:0\n"
"v_mfma_f32_16x16x32_bf16 v[12:15], a[60:63], v[152:155], v[12:15]\n"
"buffer_load_dwordx4 v223, s[56:59], s39 offen offset:1024 lds\n"
"ds_read_b64_tr_b16 a[68:69], v215 offset:32768\n"
"v_mfma_f32_16x16x32_bf16 v[16:19], a[56:59], v[156:159], v[16:19]\n"
"ds_read_b64_tr_b16 a[70:71], v215 offset:36864\n"
"ds_read_b128 v[136:139], v217 offset:1024\n"
"v_mfma_f32_16x16x32_bf16 v[20:23], a[60:63], v[156:159], v[20:23]\n"
"ds_read_b128 v[140:143], v217 offset:2048\n"
"ds_read_b128 v[144:147], v217 offset:3072\n"
"v_mfma_f32_16x16x32_bf16 v[24:27], a[56:59], v[96:99], v[24:27]\n"
"buffer_load_dwordx4 v223, s[56:59], s39 offen offset:2048 lds\n"
"s_xor_b32 s39, s36, 0x114000\n"
"v_mfma_f32_16x16x32_bf16 v[28:31], a[60:63], v[96:99], v[28:31]\n"
"ds_read_b128 v[148:151], v217 offset:4096\n"
"ds_read_b128 v[152:155], v217 offset:5120\n"
"v_mfma_f32_16x16x32_bf16 v[32:35], a[56:59], v[100:103], v[32:35]\n"
"ds_read_b128 v[156:159], v217 offset:6144\n"
"ds_read_b128 v[96:99], v217 offset:7168\n"
"s_add_u32 m0, s38, 0x10000\n"
"v_mfma_f32_16x16x32_bf16 v[36:39], a[60:63], v[100:103], v[36:39]\n"
"buffer_load_dwordx4 v223, s[56:59], s39 offen lds\n"
"v_mfma_f32_16x16x32_bf16 v[40:43], a[56:59], v[104:107], v[40:43]\n"
"ds_read_b128 v[100:103], v217 offset:8192\n"
"ds_read_b64_tr_b16 a[72:73], v212 offset:40960\n"
"v_mfma_f32_16x16x32_bf16 v[44:47], a[60:63], v[104:107], v[44:47]\n"
"ds_read_b128 v[104:107], v217 offset:9216\n"
"ds_read_b64_tr_b16 a[74:75], v212 offset:45056\n"
"v_mfma_f32_16x16x32_bf16 v[48:51], a[56:59], v[108:111], v[48:51]\n"
"buffer_load_dwordx4 v223, s[56:59], s39 offen offset:1024 lds\n"
"ds_read_b64_tr_b16 a[76:77], v215 offset:40960\n"
"v_mfma_f32_16x16x32_bf16 v[52:55], a[60:63], v[108:111], v[52:55]\n"
"ds_read_b128 v[108:111], v217 offset:10240\n"
"ds_read_b64_tr_b16 a[78:79], v215 offset:45056\n"
"v_mfma_f32_16x16x32_bf16 v[56:59], a[56:59], v[112:115], v[56:59]\n"
"v_mfma_f32_16x16x32_bf16 v[60:63], a[60:63], v[112:115], v[60:63]\n"
"buffer_load_dwordx4 v223, s[56:59], s39 offen offset:2048 lds\n"
"s_xor_b32 s39, s36, 0x118000\n"
"v_mfma_f32_16x16x32_bf16 v[64:67], a[56:59], v[116:119], v[64:67]\n"
"ds_read_b128 v[112:115], v217 offset:11264\n"
"v_mfma_f32_16x16x32_bf16 v[68:71], a[60:63], v[116:119], v[68:71]\n"
"ds_read_b128 v[116:119], v219 offset:16384\n"
"s_add_u32 m0, s77, 0x14000\n"
"v_mfma_f32_16x16x32_bf16 v[72:75], a[56:59], v[120:123], v[72:75]\n"
"buffer_load_dwordx4 v224, s[52:55], s81 offen lds\n"
"s_xor_b32 s81, s37, 0x232000\n"
"v_mfma_f32_16x16x32_bf16 v[76:79], a[60:63], v[120:123], v[76:79]\n"
"ds_read_b128 v[120:123], v219 offset:17408\n"
"v_mfma_f32_16x16x32_bf16 v[80:83], a[56:59], v[124:127], v[80:83]\n"
"s_add_u32 m0, s77, 0x14800\n"
"v_mfma_f32_16x16x32_bf16 v[84:87], a[60:63], v[124:127], v[84:87]\n"
"buffer_load_dwordx4 v224, s[52:55], s81 offen lds\n"
"s_xor_b32 s81, s37, 0x238000\n"
"v_mfma_f32_16x16x32_bf16 v[88:91], a[56:59], v[128:131], v[88:91]\n"
"ds_read_b128 v[124:127], v219 offset:18432\n"
"v_mfma_f32_16x16x32_bf16 v[92:95], a[60:63], v[128:131], v[92:95]\n"
"ds_read_b128 v[128:131], v219 offset:19456\n"
"s_add_u32 m0, s77, 0x16000\n"
"s_waitcnt lgkmcnt(2)\n"
"v_mfma_f32_16x16x32_bf16 v[0:3], a[64:67], v[132:135], v[0:3]\n"
"buffer_load_dwordx4 v224, s[52:55], s81 offen lds\n"
"s_xor_b32 s81, s37, 0x23a000\n"
"v_mfma_f32_16x16x32_bf16 v[4:7], a[68:71], v[132:135], v[4:7]\n"
"ds_read_b128 v[132:135], v219 offset:20480\n"
"v_mfma_f32_16x16x32_bf16 v[8:11], a[64:67], v[136:139], v[8:11]\n"
"s_add_u32 m0, s77, 0x16800\n"
"v_mfma_f32_16x16x32_bf16 v[12:15], a[68:71], v[136:139], v[12:15]\n"
"buffer_load_dwordx4 v224, s[52:55], s81 offen lds\n"
"v_mfma_f32_16x16x32_bf16 v[16:19], a[64:67], v[140:143], v[16:19]\n"
"ds_read_b128 v[136:139], v219 offset:21504\n"
"v_mfma_f32_16x16x32_bf16 v[20:23], a[68:71], v[140:143], v[20:23]\n"
"ds_read_b128 v[140:143], v219 offset:22528\n"
"s_add_u32 m0, s38, 0x4000\n"
"v_mfma_f32_16x16x32_bf16 v[24:27], a[64:67], v[144:147], v[24:27]\n"
"buffer_load_dwordx4 v223, s[56:59], s39 offen lds\n"
"v_mfma_f32_16x16x32_bf16 v[28:31], a[68:71], v[144:147], v[28:31]\n"
"ds_read_b128 v[144:147], v219 offset:23552\n"
"v_mfma_f32_16x16x32_bf16 v[32:35], a[64:67], v[148:151], v[32:35]\n"
"v_mfma_f32_16x16x32_bf16 v[36:39], a[68:71], v[148:151], v[36:39]\n"
"buffer_load_dwordx4 v223, s[56:59], s39 offen offset:1024 lds\n"
"ds_read_b128 v[148:151], v219 offset:24576\n"
"v_mfma_f32_16x16x32_bf16 v[40:43], a[64:67], v[152:155], v[40:43]\n"
"v_mfma_f32_16x16x32_bf16 v[44:47], a[68:71], v[152:155], v[44:47]\n"
"ds_read_b128 v[152:155], v219 offset:25600\n"
"v_mfma_f32_16x16x32_bf16 v[48:51], a[64:67], v[156:159], v[48:51]\n"
"buffer_load_dwordx4 v223, s[56:59], s39 offen offset:2048 lds\n"
"s_xor_b32 s39, s36, 0x11c000\n"
"v_mfma_f32_16x16x32_bf16 v[52:55], a[68:71], v[156:159], v[52:55]\n"
"ds_read_b128 v[156:159], v219 offset:26624\n"
"v_mfma_f32_16x16x32_bf16 v[56:59], a[64:67], v[96:99], v[56:59]\n"
"s_add_u32 m0, s38, 0xc000\n"
"v_mfma_f32_16x16x32_bf16 v[60:63], a[68:71], v[96:99], v[60:63]\n"
"buffer_load_dwordx4 v223, s[56:59], s39 offen lds\n"
"v_mfma_f32_16x16x32_bf16 v[64:67], a[64:67], v[100:103], v[64:67]\n"
"ds_read_b128 v[96:99], v219 offset:27648\n"
"v_mfma_f32_16x16x32_bf16 v[68:71], a[68:71], v[100:103], v[68:71]\n"
"v_mfma_f32_16x16x32_bf16 v[72:75], a[64:67], v[104:107], v[72:75]\n"
"buffer_load_dwordx4 v223, s[56:59], s39 offen offset:1024 lds\n"
"v_mfma_f32_16x16x32_bf16 v[76:79], a[68:71], v[104:107], v[76:79]\n"
"v_mfma_f32_16x16x32_bf16 v[80:83], a[64:67], v[108:111], v[80:83]\n"
"v_mfma_f32_16x16x32_bf16 v[84:87], a[68:71], v[108:111], v[84:87]\n"
"buffer_load_dwordx4 v223, s[56:59], s39 offen offset:2048 lds\n"
"v_mfma_f32_16x16x32_bf16 v[88:91], a[64:67], v[112:115], v[88:91]\n"
"v_mfma_f32_16x16x32_bf16 v[92:95], a[68:71], v[112:115], v[92:95]\n"
"s_waitcnt vmcnt(10) lgkmcnt(0)\n"
"s_barrier\n"
"s_xor_b32 s81, s37, 0x240000\n"
"s_add_u32 m0, s77, 0x18000\n"
"v_mfma_f32_16x16x32_bf16 v[0:3], a[72:75], v[116:119], v[0:3]\n"
"buffer_load_dwordx4 v224, s[52:55], s81 offen lds\n"
"s_xor_b32 s81, s37, 0x242000\n"
"v_mfma_f32_16x16x32_bf16 v[4:7], a[76:79], v[116:119], v[4:7]\n"
"s_xor_b32 s39, s36, 0x120000\n"
"ds_read_b128 a[16:19], v219 offset:0\n"
"v_mfma_f32_16x16x32_bf16 v[8:11], a[72:75], v[120:123], v[8:11]\n"
"ds_read_b128 a[20:23], v219 offset:1024\n"
"ds_read_b128 a[24:27], v219 offset:2048\n"
"s_add_u32 m0, s77, 0x18800\n"
"v_mfma_f32_16x16x32_bf16 v[12:15], a[76:79], v[120:123], v[12:15]\n"
"buffer_load_dwordx4 v224, s[52:55], s81 offen lds\n"
"s_xor_b32 s81, s37, 0x248000\n"
"v_mfma_f32_16x16x32_bf16 v[16:19], a[72:75], v[124:127], v[16:19]\n"
"ds_read_b128 a[28:31], v218 offset:0\n"
"ds_read_b128 a[32:35], v218 offset:1024\n"
"v_mfma_f32_16x16x32_bf16 v[20:23], a[76:79], v[124:127], v[20:23]\n"
"ds_read_b128 a[36:39], v218 offset:2048\n"
"ds_read_b64_tr_b16 a[0:1], v211 offset:32768\n"
"s_add_u32 m0, s77, 0x1a000\n"
"v_mfma_f32_16x16x32_bf16 v[24:27], a[72:75], v[128:131], v[24:27]\n"
"buffer_load_dwordx4 v224, s[52:55], s81 offen lds\n"
"s_xor_b32 s81, s37, 0x24a000\n"
"v_mfma_f32_16x16x32_bf16 v[28:31], a[76:79], v[128:131], v[28:31]\n"
"ds_read_b64_tr_b16 a[2:3], v211 offset:36864\n"
"ds_read_b128 v[100:103], v219 offset:3072\n"
"v_mfma_f32_16x16x32_bf16 v[32:35], a[72:75], v[132:135], v[32:35]\n"
"ds_read_b64_tr_b16 a[4:5], v214 offset:32768\n"
"ds_read_b64_tr_b16 a[6:7], v214 offset:36864\n"
"s_add_u32 m0, s77, 0x1a800\n"
"v_mfma_f32_16x16x32_bf16 v[36:39], a[76:79], v[132:135], v[36:39]\n"
"buffer_load_dwordx4 v224, s[52:55], s81 offen lds\n"
"v_mfma_f32_16x16x32_bf16 v[40:43], a[72:75], v[136:139], v[40:43]\n"
"ds_read_b128 v[104:107], v219 offset:4096\n"
"ds_read_b128 v[108:111], v219 offset:5120\n"
"v_mfma_f32_16x16x32_bf16 v[44:47], a[76:79], v[136:139], v[44:47]\n"
"ds_read_b128 v[112:115], v219 offset:6144\n"
"ds_read_b128 v[116:119], v219 offset:7168\n"
"s_add_u32 m0, s38, 0x0\n"
"v_mfma_f32_16x16x32_bf16 v[48:51], a[72:75], v[140:143], v[48:51]\n"
"buffer_load_dwordx4 v223, s[56:59], s39 offen lds\n"
"v_mfma_f32_16x16x32_bf16 v[52:55], a[76:79], v[140:143], v[52:55]\n"
"ds_read_b128 v[120:123], v219 offset:8192\n"
"ds_read_b128 v[124:127], v219 offset:9216\n"
"v_mfma_f32_16x16x32_bf16 v[56:59], a[72:75], v[144:147], v[56:59]\n"
"ds_read_b128 v[128:131], v219 offset:10240\n"
"ds_read_b128 v[132:135], v219 offset:11264\n"
"v_mfma_f32_16x16x32_bf16 v[60:63], a[76:79], v[144:147], v[60:63]\n"
"buffer_load_dwordx4 v223, s[56:59], s39 offen offset:1024 lds\n"
"ds_read_b64_tr_b16 a[8:9], v211 offset:40960\n"
"v_mfma_f32_16x16x32_bf16 v[64:67], a[72:75], v[148:151], v[64:67]\n"
"ds_read_b64_tr_b16 a[10:11], v211 offset:45056\n"
"ds_read_b128 v[136:139], v218 offset:3072\n"
"v_mfma_f32_16x16x32_bf16 v[68:71], a[76:79], v[148:151], v[68:71]\n"
"ds_read_b64_tr_b16 a[12:13], v214 offset:40960\n"
"ds_read_b64_tr_b16 a[14:15], v214 offset:45056\n"
"v_mfma_f32_16x16x32_bf16 v[72:75], a[72:75], v[152:155], v[72:75]\n"
"buffer_load_dwordx4 v223, s[56:59], s39 offen offset:2048 lds\n"
"s_xor_b32 s39, s36, 0x124000\n"
"v_mfma_f32_16x16x32_bf16 v[76:79], a[76:79], v[152:155], v[76:79]\n"
"ds_read_b128 v[140:143], v218 offset:4096\n"
"ds_read_b128 v[144:147], v218 offset:5120\n"
"v_mfma_f32_16x16x32_bf16 v[80:83], a[72:75], v[156:159], v[80:83]\n"
"ds_read_b128 v[148:151], v218 offset:6144\n"
"ds_read_b128 v[152:155], v218 offset:7168\n"
"s_add_u32 m0, s38, 0x1c000\n"
"v_mfma_f32_16x16x32_bf16 v[84:87], a[76:79], v[156:159], v[84:87]\n"
"buffer_load_dwordx4 v223, s[56:59], s39 offen lds\n"
"v_mfma_f32_16x16x32_bf16 v[88:91], a[72:75], v[96:99], v[88:91]\n"
"ds_read_b128 v[156:159], v218 offset:8192\n"
"v_mfma_f32_16x16x32_bf16 v[92:95], a[76:79], v[96:99], v[92:95]\n"
"ds_read_b128 v[96:99], v218 offset:9216\n"
"s_waitcnt lgkmcnt(4)\n"
"v_mfma_f32_16x16x32_bf16 v[24:27], a[0:3], v[100:103], v[24:27]\n"
"buffer_load_dwordx4 v223, s[56:59], s39 offen offset:1024 lds\n"
"v_mfma_f32_16x16x32_bf16 v[28:31], a[4:7], v[100:103], v[28:31]\n"
"ds_read_b128 v[100:103], v218 offset:10240\n"
"v_mfma_f32_16x16x32_bf16 v[32:35], a[0:3], v[104:107], v[32:35]\n"
"v_mfma_f32_16x16x32_bf16 v[36:39], a[4:7], v[104:107], v[36:39]\n"
"buffer_load_dwordx4 v223, s[56:59], s39 offen offset:2048 lds\n"
"ds_read_b128 v[104:107], v218 offset:11264\n"
"v_mfma_f32_16x16x32_bf16 v[40:43], a[0:3], v[108:111], v[40:43]\n"
"v_mfma_f32_16x16x32_bf16 v[44:47], a[4:7], v[108:111], v[44:47]\n"
"v_mfma_f32_16x16x32_bf16 v[48:51], a[0:3], v[112:115], v[48:51]\n"
"v_mfma_f32_16x16x32_bf16 v[52:55], a[4:7], v[112:115], v[52:55]\n"
"v_mfma_f32_16x16x32_bf16 v[56:59], a[0:3], v[116:119], v[56:59]\n"
"v_mfma_f32_16x16x32_bf16 v[60:63], a[4:7], v[116:119], v[60:63]\n"
"v_mfma_f32_16x16x32_bf16 v[64:67], a[0:3], v[120:123], v[64:67]\n"
"v_mfma_f32_16x16x32_bf16 v[68:71], a[4:7], v[120:123], v[68:71]\n"
"v_mfma_f32_16x16x32_bf16 v[72:75], a[0:3], v[124:127], v[72:75]\n"
"v_mfma_f32_16x16x32_bf16 v[76:79], a[4:7], v[124:127], v[76:79]\n"
"v_mfma_f32_16x16x32_bf16 v[80:83], a[0:3], v[128:131], v[80:83]\n"
"v_mfma_f32_16x16x32_bf16 v[84:87], a[4:7], v[128:131], v[84:87]\n"
"v_mfma_f32_16x16x32_bf16 v[88:91], a[0:3], v[132:135], v[88:91]\n"
"v_mfma_f32_16x16x32_bf16 v[92:95], a[4:7], v[132:135], v[92:95]\n"
"s_waitcnt vmcnt(10) lgkmcnt(0)\n"
"s_barrier\n"
"s_xor_b32 s81, s37, 0x250000\n"
"s_add_u32 m0, s77, 0x8000\n"
"v_mfma_f32_16x16x32_bf16 v[24:27], a[8:11], v[136:139], v[24:27]\n"
"buffer_load_dwordx4 v224, s[52:55], s81 offen lds\n"
"s_xor_b32 s81, s37, 0x252000\n"
"v_mfma_f32_16x16x32_bf16 v[28:31], a[12:15], v[136:139], v[28:31]\n"
"s_xor_b32 s39, s36, 0x128000\n"
"ds_read_b64_tr_b16 a[40:41], v212 offset:16384\n"
"v_mfma_f32_16x16x32_bf16 v[32:35], a[8:11], v[140:143], v[32:35]\n"
"ds_read_b64_tr_b16 a[42:43], v212 offset:20480\n"
"ds_read_b128 a[56:59], v217 offset:16384\n"
"s_add_u32 m0, s77, 0x8800\n"
"v_mfma_f32_16x16x32_bf16 v[36:39], a[12:15], v[140:143], v[36:39]\n"
"buffer_load_dwordx4 v224, s[52:55], s81 offen lds\n"
"s_xor_b32 s81, s37, 0x258000\n"
"v_mfma_f32_16x16x32_bf16 v[40:43], a[8:11], v[144:147], v[40:43]\n"
"ds_read_b128 a[60:63], v217 offset:17408\n"
"ds_read_b128 a[64:67], v217 offset:18432\n"
"v_mfma_f32_16x16x32_bf16 v[44:47], a[12:15], v[144:147], v[44:47]\n"
"ds_read_b128 v[108:111], v217 offset:19456\n"
"ds_read_b128 a[68:71], v217 offset:49152\n"
"s_add_u32 m0, s77, 0xa000\n"
"v_mfma_f32_16x16x32_bf16 v[48:51], a[8:11], v[148:151], v[48:51]\n"
"buffer_load_dwordx4 v224, s[52:55], s81 offen lds\n"
"s_xor_b32 s81, s37, 0x25a000\n"
"v_mfma_f32_16x16x32_bf16 v[52:55], a[12:15], v[148:151], v[52:55]\n"
"ds_read_b128 a[72:75], v217 offset:50176\n"
"ds_read_b128 a[76:79], v217 offset:51200\n"
"v_mfma_f32_16x16x32_bf16 v[56:59], a[8:11], v[152:155], v[56:59]\n"
"ds_read_b64_tr_b16 a[44:45], v215 offset:16384\n"
"ds_read_b64_tr_b16 a[46:47], v215 offset:20480\n"
"s_add_u32 m0, s77, 0xa800\n"
"v_mfma_f32_16x16x32_bf16 v[60:63], a[12:15], v[152:155], v[60:63]\n"
"buffer_load_dwordx4 v224, s[52:55], s81 offen lds\n"
"v_mfma_f32_16x16x32_bf16 v[64:67], a[8:11], v[156:159], v[64:67]\n"
"ds_read_b128 v[112:115], v217 offset:20480\n"
"ds_read_b128 v[116:119], v217 offset:21504\n"
"v_mfma_f32_16x16x32_bf16 v[68:71], a[12:15], v[156:159], v[68:71]\n"
"ds_read_b128 v[120:123], v217 offset:22528\n"
"ds_read_b128 v[124:127], v217 offset:23552\n"
"s_add_u32 m0, s38, 0x20000\n"
"v_mfma_f32_16x16x32_bf16 v[72:75], a[8:11], v[96:99], v[72:75]\n"
"buffer_load_dwordx4 v223, s[56:59], s39 offen lds\n"
"v_mfma_f32_16x16x32_bf16 v[76:79], a[12:15], v[96:99], v[76:79]\n"
"ds_read_b128 v[128:131], v217 offset:24576\n"
"ds_read_b128 v[132:135], v217 offset:25600\n"
"v_mfma_f32_16x16x32_bf16 v[80:83], a[8:11], v[100:103], v[80:83]\n"
"ds_read_b128 v[136:139], v217 offset:26624\n"
"ds_read_b128 v[140:143], v217 offset:27648\n"
"v_mfma_f32_16x16x32_bf16 v[84:87], a[12:15], v[100:103], v[84:87]\n"
"buffer_load_dwordx4 v223, s[56:59], s39 offen offset:1024 lds\n"
"ds_read_b64_tr_b16 a[48:49], v212 offset:24576\n"
"v_mfma_f32_16x16x32_bf16 v[88:91], a[8:11], v[104:107], v[88:91]\n"
"ds_read_b64_tr_b16 a[50:51], v212 offset:28672\n"
"ds_read_b128 v[144:147], v217 offset:52224\n"
"v_mfma_f32_16x16x32_bf16 v[92:95], a[12:15], v[104:107], v[92:95]\n"
"ds_read_b64_tr_b16 a[52:53], v215 offset:24576\n"
"ds_read_b64_tr_b16 a[54:55], v215 offset:28672\n"
"s_waitcnt lgkmcnt(7)\n"
"v_mfma_f32_16x16x32_bf16 v[24:27], a[40:43], v[108:111], v[24:27]\n"
"buffer_load_dwordx4 v223, s[56:59], s39 offen offset:2048 lds\n"
"s_xor_b32 s39, s36, 0x12c000\n"
"v_mfma_f32_16x16x32_bf16 v[28:31], a[44:47], v[108:111], v[28:31]\n"
"ds_read_b128 v[148:151], v217 offset:53248\n"
"ds_read_b128 v[152:155], v217 offset:54272\n"
"v_mfma_f32_16x16x32_bf16 v[32:35], a[40:43], v[112:115], v[32:35]\n"
"ds_read_b128 v[156:159], v217 offset:55296\n"
"ds_read_b128 v[96:99], v217 offset:56320\n"
"s_add_u32 m0, s38, 0x24000\n"
"v_mfma_f32_16x16x32_bf16 v[36:39], a[44:47], v[112:115], v[36:39]\n"
"buffer_load_dwordx4 v223, s[56:59], s39 offen lds\n"
"v_mfma_f32_16x16x32_bf16 v[40:43], a[40:43], v[116:119], v[40:43]\n"
"ds_read_b128 v[100:103], v217 offset:57344\n"
"ds_read_b128 v[104:107], v217 offset:58368\n"
"v_mfma_f32_16x16x32_bf16 v[44:47], a[44:47], v[116:119], v[44:47]\n"
"ds_read_b128 v[108:111], v217 offset:59392\n"
"ds_read_b128 v[112:115], v217 offset:60416\n"
"v_mfma_f32_16x16x32_bf16 v[48:51], a[40:43], v[120:123], v[48:51]\n"
"buffer_load_dwordx4 v223, s[56:59], s39 offen offset:1024 lds\n"
"v_mfma_f32_16x16x32_bf16 v[52:55], a[44:47], v[120:123], v[52:55]\n"
"v_mfma_f32_16x16x32_bf16 v[56:59], a[40:43], v[124:127], v[56:59]\n"
"v_mfma_f32_16x16x32_bf16 v[60:63], a[44:47], v[124:127], v[60:63]\n"
"buffer_load_dwordx4 v223, s[56:59], s39 offen offset:2048 lds\n"
"v_mfma_f32_16x16x32_bf16 v[64:67], a[40:43], v[128:131], v[64:67]\n"
"v_mfma_f32_16x16x32_bf16 v[68:71], a[44:47], v[128:131], v[68:71]\n"
"v_mfma_f32_16x16x32_bf16 v[72:75], a[40:43], v[132:135], v[72:75]\n"
"v_mfma_f32_16x16x32_bf16 v[76:79], a[44:47], v[132:135], v[76:79]\n"
"s_waitcnt lgkmcnt(0)\n"
"v_mfma_f32_16x16x32_bf16 v[80:83], a[40:43], v[136:139], v[80:83]\n"
"v_mfma_f32_16x16x32_bf16 v[84:87], a[44:47], v[136:139], v[84:87]\n"
"v_mfma_f32_16x16x32_bf16 v[88:91], a[40:43], v[140:143], v[88:91]\n"
"v_mfma_f32_16x16x32_bf16 v[92:95], a[44:47], v[140:143], v[92:95]\n"
"s_waitcnt vmcnt(10)\n"
"s_barrier\n"
"s_xor_b32 s81, s37, 0x260000\n"
"s_add_u32 m0, s77, 0x14000\n"
"v_mfma_f32_16x16x32_bf16 v[24:27], a[48:51], v[144:147], v[24:27]\n"
"buffer_load_dwordx4 v224, s[52:55], s81 offen lds\n"
"s_xor_b32 s81, s37, 0x262000\n"
"v_mfma_f32_16x16x32_bf16 v[28:31], a[52:55], v[144:147], v[28:31]\n"
"s_xor_b32 s39, s36, 0x130000\n"
"ds_read_b64_tr_b16 a[80:81], v212 offset:32768\n"
"v_mfma_f32_16x16x32_bf16 v[32:35], a[48:51], v[148:151], v[32:35]\n"
"ds_read_b64_tr_b16 a[82:83], v212 offset:36864\n"
"ds_read_b128 a[96:99], v217 offset:0\n"
"s_add_u32 m0, s77, 0x14800\n"
"v_mfma_f32_16x16x32_bf16 v[36:39], a[52:55], v[148:151], v[36:39]\n"
"buffer_load_dwordx4 v224, s[52:55], s81 offen lds\n"
"s_xor_b32 s81, s37, 0x268000\n"
"v_mfma_f32_16x16x32_bf16 v[40:43], a[48:51], v[152:155], v[40:43]\n"
"ds_read_b128 a[100:103], v217 offset:1024\n"
"ds_read_b128 a[104:107], v217 offset:2048\n"
"v_mfma_f32_16x16x32_bf16 v[44:47], a[52:55], v[152:155], v[44:47]\n"
"ds_read_b128 v[116:119], v217 offset:3072\n"
"ds_read_b128 a[108:111], v218 offset:49152\n"
"s_add_u32 m0, s77, 0x16000\n"
"v_mfma_f32_16x16x32_bf16 v[48:51], a[48:51], v[156:159], v[48:51]\n"
"buffer_load_dwordx4 v224, s[52:55], s81 offen lds\n"
"s_xor_b32 s81, s37, 0x26a000\n"
"v_mfma_f32_16x16x32_bf16 v[52:55], a[52:55], v[156:159], v[52:55]\n"
"ds_read_b128 a[112:115], v218 offset:50176\n"
"ds_read_b128 a[116:119], v218 offset:51200\n"
"v_mfma_f32_16x16x32_bf16 v[56:59], a[48:51], v[96:99], v[56:59]\n"
"ds_read_b64_tr_b16 a[84:85], v215 offset:32768\n"
"ds_read_b64_tr_b16 a[86:87], v215 offset:36864\n"
"s_add_u32 m0, s77, 0x16800\n"
"v_mfma_f32_16x16x32_bf16 v[60:63], a[52:55], v[96:99], v[60:63]\n"
"buffer_load_dwordx4 v224, s[52:55], s81 offen lds\n"
"v_mfma_f32_16x16x32_bf16 v[64:67], a[48:51], v[100:103], v[64:67]\n"
"ds_read_b128 v[120:123], v217 offset:4096\n"
"ds_read_b128 v[124:127], v217 offset:5120\n"
"v_mfma_f32_16x16x32_bf16 v[68:71], a[52:55], v[100:103], v[68:71]\n"
"ds_read_b128 v[128:131], v217 offset:6144\n"
"ds_read_b128 v[132:135], v217 offset:7168\n"
"s_add_u32 m0, s38, 0x4000\n"
"v_mfma_f32_16x16x32_bf16 v[72:75], a[48:51], v[104:107], v[72:75]\n"
"buffer_load_dwordx4 v223, s[56:59], s39 offen lds\n"
"v_mfma_f32_16x16x32_bf16 v[76:79], a[52:55], v[104:107], v[76:79]\n"
"ds_read_b128 v[136:139], v217 offset:8192\n"
"ds_read_b128 v[140:143], v217 offset:9216\n"
"v_mfma_f32_16x16x32_bf16 v[80:83], a[48:51], v[108:111], v[80:83]\n"
"ds_read_b128 v[144:147], v217 offset:10240\n"
"ds_read_b128 v[148:151], v217 offset:11264\n"
"v_mfma_f32_16x16x32_bf16 v[84:87], a[52:55], v[108:111], v[84:87]\n"
"buffer_load_dwordx4 v223, s[56:59], s39 offen offset:1024 lds\n"
"ds_read_b64_tr_b16 a[88:89], v212 offset:40960\n"
"v_mfma_f32_16x16x32_bf16 v[88:91], a[48:51], v[112:115], v[88:91]\n"
"ds_read_b64_tr_b16 a[90:91], v212 offset:45056\n"
"ds_read_b128 v[152:155], v218 offset:52224\n"
"v_mfma_f32_16x16x32_bf16 v[92:95], a[52:55], v[112:115], v[92:95]\n"
"ds_read_b64_tr_b16 a[92:93], v215 offset:40960\n"
"ds_read_b64_tr_b16 a[94:95], v215 offset:45056\n"
"s_waitcnt lgkmcnt(7)\n"
"v_mfma_f32_16x16x32_bf16 v[24:27], a[80:83], v[116:119], v[24:27]\n"
"buffer_load_dwordx4 v223, s[56:59], s39 offen offset:2048 lds\n"
"s_xor_b32 s39, s36, 0x134000\n"
"v_mfma_f32_16x16x32_bf16 v[28:31], a[84:87], v[116:119], v[28:31]\n"
"ds_read_b128 v[156:159], v218 offset:53248\n"
"ds_read_b128 v[96:99], v218 offset:54272\n"
"v_mfma_f32_16x16x32_bf16 v[32:35], a[80:83], v[120:123], v[32:35]\n"
"ds_read_b128 v[100:103], v218 offset:55296\n"
"ds_read_b128 v[104:107], v218 offset:56320\n"
"s_add_u32 m0, s38, 0x10000\n"
"v_mfma_f32_16x16x32_bf16 v[36:39], a[84:87], v[120:123], v[36:39]\n"
"buffer_load_dwordx4 v223, s[56:59], s39 offen lds\n"
"v_mfma_f32_16x16x32_bf16 v[40:43], a[80:83], v[124:127], v[40:43]\n"
"ds_read_b128 v[108:111], v218 offset:57344\n"
"ds_read_b128 v[112:115], v218 offset:58368\n"
"v_mfma_f32_16x16x32_bf16 v[44:47], a[84:87], v[124:127], v[44:47]\n"
"ds_read_b128 v[116:119], v218 offset:59392\n"
"ds_read_b128 v[120:123], v218 offset:60416\n"
"v_mfma_f32_16x16x32_bf16 v[48:51], a[80:83], v[128:131], v[48:51]\n"
"buffer_load_dwordx4 v223, s[56:59], s39 offen offset:1024 lds\n"
"v_mfma_f32_16x16x32_bf16 v[52:55], a[84:87], v[128:131], v[52:55]\n"
"v_mfma_f32_16x16x32_bf16 v[56:59], a[80:83], v[132:135], v[56:59]\n"
"v_mfma_f32_16x16x32_bf16 v[60:63], a[84:87], v[132:135], v[60:63]\n"
"buffer_load_dwordx4 v223, s[56:59], s39 offen offset:2048 lds\n"
"v_mfma_f32_16x16x32_bf16 v[64:67], a[80:83], v[136:139], v[64:67]\n"
"v_mfma_f32_16x16x32_bf16 v[68:71], a[84:87], v[136:139], v[68:71]\n"
"v_mfma_f32_16x16x32_bf16 v[72:75], a[80:83], v[140:143], v[72:75]\n"
"v_mfma_f32_16x16x32_bf16 v[76:79], a[84:87], v[140:143], v[76:79]\n"
"s_waitcnt lgkmcnt(0)\n"
"v_mfma_f32_16x16x32_bf16 v[80:83], a[80:83], v[144:147], v[80:83]\n"
"v_mfma_f32_16x16x32_bf16 v[84:87], a[84:87], v[144:147], v[84:87]\n"
"v_mfma_f32_16x16x32_bf16 v[88:91], a[80:83], v[148:151], v[88:91]\n"
"v_mfma_f32_16x16x32_bf16 v[92:95], a[84:87], v[148:151], v[92:95]\n"
"s_waitcnt vmcnt(10)\n"
"s_barrier\n"
"s_xor_b32 s81, s37, 0x270000\n"
"s_add_u32 m0, s77, 0x18000\n"
"v_mfma_f32_16x16x32_bf16 v[24:27], a[88:91], v[152:155], v[24:27]\n"
"buffer_load_dwordx4 v224, s[52:55], s81 offen lds\n"
"s_xor_b32 s81, s37, 0x272000\n"
"v_mfma_f32_16x16x32_bf16 v[28:31], a[92:95], v[152:155], v[28:31]\n"
"s_xor_b32 s39, s36, 0x138000\n"
"ds_read_b64_tr_b16 a[120:121], v211 offset:32768\n"
"v_mfma_f32_16x16x32_bf16 v[32:35], a[88:91], v[156:159], v[32:35]\n"
"ds_read_b64_tr_b16 a[122:123], v211 offset:36864\n"
"ds_read_b128 a[136:139], v219 offset:0\n"
"s_add_u32 m0, s77, 0x18800\n"
"v_mfma_f32_16x16x32_bf16 v[36:39], a[92:95], v[156:159], v[36:39]\n"
"buffer_load_dwordx4 v224, s[52:55], s81 offen lds\n"
"s_xor_b32 s81, s37, 0x278000\n"
"v_mfma_f32_16x16x32_bf16 v[40:43], a[88:91], v[96:99], v[40:43]\n"
"ds_read_b128 a[140:143], v219 offset:1024\n"
"ds_read_b128 a[144:147], v219 offset:2048\n"
"v_mfma_f32_16x16x32_bf16 v[44:47], a[92:95], v[96:99], v[44:47]\n"
"ds_read_b128 v[124:127], v219 offset:3072\n"
"ds_read_b128 a[148:151], v219 offset:16384\n"
"s_add_u32 m0, s77, 0x1a000\n"
"v_mfma_f32_16x16x32_bf16 v[48:51], a[88:91], v[100:103], v[48:51]\n"
"buffer_load_dwordx4 v224, s[52:55], s81 offen lds\n"
"s_xor_b32 s81, s37, 0x27a000\n"
"v_mfma_f32_16x16x32_bf16 v[52:55], a[92:95], v[100:103], v[52:55]\n"
"ds_read_b128 a[152:155], v219 offset:17408\n"
"ds_read_b128 a[156:159], v219 offset:18432\n"
"v_mfma_f32_16x16x32_bf16 v[56:59], a[88:91], v[104:107], v[56:59]\n"
"ds_read_b64_tr_b16 a[124:125], v214 offset:32768\n"
"ds_read_b64_tr_b16 a[126:127], v214 offset:36864\n"
"s_add_u32 m0, s77, 0x1a800\n"
"v_mfma_f32_16x16x32_bf16 v[60:63], a[92:95], v[104:107], v[60:63]\n"
"buffer_load_dwordx4 v224, s[52:55], s81 offen lds\n"
"v_mfma_f32_16x16x32_bf16 v[64:67], a[88:91], v[108:111], v[64:67]\n"
"ds_read_b128 v[128:131], v219 offset:4096\n"
"ds_read_b128 v[132:135], v219 offset:5120\n"
"v_mfma_f32_16x16x32_bf16 v[68:71], a[92:95], v[108:111], v[68:71]\n"
"ds_read_b128 v[136:139], v219 offset:6144\n"
"ds_read_b128 v[140:143], v219 offset:7168\n"
"s_add_u32 m0, s38, 0x0\n"
"v_mfma_f32_16x16x32_bf16 v[72:75], a[88:91], v[112:115], v[72:75]\n"
"buffer_load_dwordx4 v223, s[56:59], s39 offen lds\n"
"v_mfma_f32_16x16x32_bf16 v[76:79], a[92:95], v[112:115], v[76:79]\n"
"ds_read_b128 v[144:147], v219 offset:8192\n"
"ds_read_b128 v[148:151], v219 offset:9216\n"
"v_mfma_f32_16x16x32_bf16 v[80:83], a[88:91], v[116:119], v[80:83]\n"
"ds_read_b128 v[152:155], v219 offset:10240\n"
"ds_read_b128 v[156:159], v219 offset:11264\n"
"v_mfma_f32_16x16x32_bf16 v[84:87], a[92:95], v[116:119], v[84:87]\n"
"buffer_load_dwordx4 v223, s[56:59], s39 offen offset:1024 lds\n"
"ds_read_b64_tr_b16 a[128:129], v211 offset:40960\n"
"v_mfma_f32_16x16x32_bf16 v[88:91], a[88:91], v[120:123], v[88:91]\n"
"ds_read_b64_tr_b16 a[130:131], v211 offset:45056\n"
"ds_read_b128 v[96:99], v219 offset:19456\n"
"v_mfma_f32_16x16x32_bf16 v[92:95], a[92:95], v[120:123], v[92:95]\n"
"ds_read_b64_tr_b16 a[132:133], v214 offset:40960\n"
"ds_read_b64_tr_b16 a[134:135], v214 offset:45056\n"
"s_waitcnt lgkmcnt(7)\n"
"v_mfma_f32_16x16x32_bf16 v[24:27], a[120:123], v[124:127], v[24:27]\n"
"buffer_load_dwordx4 v223, s[56:59], s39 offen offset:2048 lds\n"
"s_xor_b32 s39, s36, 0x13c000\n"
"v_mfma_f32_16x16x32_bf16 v[28:31], a[124:127], v[124:127], v[28:31]\n"
"ds_read_b128 v[100:103], v219 offset:20480\n"
"ds_read_b128 v[104:107], v219 offset:21504\n"
"v_mfma_f32_16x16x32_bf16 v[32:35], a[120:123], v[128:131], v[32:35]\n"
"ds_read_b128 v[108:111], v219 offset:22528\n"
"ds_read_b128 v[112:115], v219 offset:23552\n"
"s_add_u32 m0, s38, 0xc000\n"
"v_mfma_f32_16x16x32_bf16 v[36:39], a[124:127], v[128:131], v[36:39]\n"
"buffer_load_dwordx4 v223, s[56:59], s39 offen lds\n"
"v_mfma_f32_16x16x32_bf16 v[40:43], a[120:123], v[132:135], v[40:43]\n"
"ds_read_b128 v[116:119], v219 offset:24576\n"
"ds_read_b128 v[120:123], v219 offset:25600\n"
"v_mfma_f32_16x16x32_bf16 v[44:47], a[124:127], v[132:135], v[44:47]\n"
"ds_read_b128 v[124:127], v219 offset:26624\n"
"ds_read_b128 v[128:131], v219 offset:27648\n"
"v_mfma_f32_16x16x32_bf16 v[48:51], a[120:123], v[136:139], v[48:51]\n"
"buffer_load_dwordx4 v223, s[56:59], s39 offen offset:1024 lds\n"
"s_lshl2_add_u32 s37, s84, s31\n"
"v_mfma_f32_16x16x32_bf16 v[52:55], a[124:127], v[136:139], v[52:55]\n"
"s_lshl_b32 s37, s37, 2\n"
"s_add_u32 s79, s37, s30\n"
"v_mfma_f32_16x16x32_bf16 v[56:59], a[120:123], v[140:143], v[56:59]\n"
"s_mul_i32 s79, s79, 12\n"
"s_add_u32 s79, s79, s76\n"
"v_mfma_f32_16x16x32_bf16 v[60:63], a[124:127], v[140:143], v[60:63]\n"
"buffer_load_dwordx4 v223, s[56:59], s39 offen offset:2048 lds\n"
"s_mul_i32 s79, s79, 0x1800\n"
"v_mfma_f32_16x16x32_bf16 v[64:67], a[120:123], v[144:147], v[64:67]\n"
"s_add_u32 s36, s30, 1\n"
"s_and_b32 s36, s36, 3\n"
"v_mfma_f32_16x16x32_bf16 v[68:71], a[124:127], v[144:147], v[68:71]\n"
"s_add_u32 s36, s36, s37\n"
"s_mul_i32 s36, s36, 12\n"
"v_mfma_f32_16x16x32_bf16 v[72:75], a[120:123], v[148:151], v[72:75]\n"
"s_add_u32 s36, s36, s76\n"
"s_add_u32 s36, s36, 0\n"
"v_mfma_f32_16x16x32_bf16 v[76:79], a[124:127], v[148:151], v[76:79]\n"
"s_mul_i32 s36, s36, 0x1800\n"
"s_add_u32 s38, s30, 2\n"
"s_waitcnt lgkmcnt(0)\n"
"v_mfma_f32_16x16x32_bf16 v[80:83], a[120:123], v[152:155], v[80:83]\n"
"s_and_b32 s38, s38, 3\n"
"s_add_u32 s38, s38, s37\n"
"v_mfma_f32_16x16x32_bf16 v[84:87], a[124:127], v[152:155], v[84:87]\n"
"s_mul_i32 s38, s38, 12\n"
"s_add_u32 s38, s38, s76\n"
"v_mfma_f32_16x16x32_bf16 v[88:91], a[120:123], v[156:159], v[88:91]\n"
"s_add_u32 s38, s38, 4\n"
"s_mul_i32 s38, s38, 0x1800\n"
"v_mfma_f32_16x16x32_bf16 v[92:95], a[124:127], v[156:159], v[92:95]\n"
"s_waitcnt vmcnt(10)\n"
"s_barrier\n"
"v_mfma_f32_16x16x32_bf16 v[24:27], a[128:131], v[96:99], v[24:27]\n"
"ds_read_b64_tr_b16 a[160:161], v212 offset:16384\n"
"ds_read_b64_tr_b16 a[162:163], v212 offset:20480\n"
"v_mfma_f32_16x16x32_bf16 v[28:31], a[132:135], v[96:99], v[28:31]\n"
"ds_read_b128 a[176:179], v217 offset:16384\n"
"ds_read_b128 a[180:183], v217 offset:17408\n"
"v_mfma_f32_16x16x32_bf16 v[32:35], a[128:131], v[100:103], v[32:35]\n"
"ds_read_b128 a[184:187], v217 offset:18432\n"
"ds_read_b128 v[132:135], v217 offset:19456\n"
"v_mfma_f32_16x16x32_bf16 v[36:39], a[132:135], v[100:103], v[36:39]\n"
"ds_read_b128 a[188:191], v218 offset:0\n"
"ds_read_b128 a[192:195], v218 offset:1024\n"
"v_mfma_f32_16x16x32_bf16 v[40:43], a[128:131], v[104:107], v[40:43]\n"
"ds_read_b128 a[196:199], v218 offset:2048\n"
"ds_read_b64_tr_b16 a[164:165], v215 offset:16384\n"
"v_mfma_f32_16x16x32_bf16 v[44:47], a[132:135], v[104:107], v[44:47]\n"
"ds_read_b64_tr_b16 a[166:167], v215 offset:20480\n"
"ds_read_b128 v[136:139], v217 offset:20480\n"
"v_mfma_f32_16x16x32_bf16 v[48:51], a[128:131], v[108:111], v[48:51]\n"
"ds_read_b128 v[140:143], v217 offset:21504\n"
"ds_read_b128 v[144:147], v217 offset:22528\n"
"v_mfma_f32_16x16x32_bf16 v[52:55], a[132:135], v[108:111], v[52:55]\n"
"ds_read_b128 v[148:151], v217 offset:23552\n"
"ds_read_b128 v[152:155], v217 offset:24576\n"
"v_mfma_f32_16x16x32_bf16 v[56:59], a[128:131], v[112:115], v[56:59]\n"
"ds_read_b128 v[156:159], v217 offset:25600\n"
"ds_read_b128 v[96:99], v217 offset:26624\n"
"v_mfma_f32_16x16x32_bf16 v[60:63], a[132:135], v[112:115], v[60:63]\n"
"ds_read_b128 v[100:103], v217 offset:27648\n"
"ds_read_b64_tr_b16 a[168:169], v212 offset:24576\n"
"v_mfma_f32_16x16x32_bf16 v[64:67], a[128:131], v[116:119], v[64:67]\n"
"ds_read_b64_tr_b16 a[170:171], v212 offset:28672\n"
"ds_read_b128 v[104:107], v218 offset:3072\n"
"v_mfma_f32_16x16x32_bf16 v[68:71], a[132:135], v[116:119], v[68:71]\n"
"ds_read_b64_tr_b16 a[172:173], v215 offset:24576\n"
"ds_read_b64_tr_b16 a[174:175], v215 offset:28672\n"
"v_mfma_f32_16x16x32_bf16 v[72:75], a[128:131], v[120:123], v[72:75]\n"
"ds_read_b128 v[108:111], v218 offset:4096\n"
"ds_read_b128 v[112:115], v218 offset:5120\n"
"v_mfma_f32_16x16x32_bf16 v[76:79], a[132:135], v[120:123], v[76:79]\n"
"ds_read_b128 v[116:119], v218 offset:6144\n"
"ds_read_b128 v[120:123], v218 offset:7168\n"
"v_mfma_f32_16x16x32_bf16 v[80:83], a[128:131], v[124:127], v[80:83]\n"
"s_add_u32 s39, s30, 3\n"
"s_and_b32 s39, s39, 3\n"
"v_mfma_f32_16x16x32_bf16 v[84:87], a[132:135], v[124:127], v[84:87]\n"
"ds_read_b128 v[124:127], v218 offset:8192\n"
"s_add_u32 s39, s39, s37\n"
"v_mfma_f32_16x16x32_bf16 v[88:91], a[128:131], v[128:131], v[88:91]\n"
"s_mul_i32 s39, s39, 12\n"
"s_add_u32 s39, s39, s76\n"
"v_mfma_f32_16x16x32_bf16 v[92:95], a[132:135], v[128:131], v[92:95]\n"
"ds_read_b128 v[128:131], v218 offset:9216\n"
"s_add_u32 s39, s39, 8\n"
"s_waitcnt lgkmcnt(2)\n"
"v_mfma_f32_16x16x32_bf16 v[24:27], a[160:163], v[132:135], v[24:27]\n"
"s_mul_i32 s39, s39, 0x1800\n"
"s_lshl_b32 s81, s76, 6\n"
"v_mfma_f32_16x16x32_bf16 v[28:31], a[164:167], v[132:135], v[28:31]\n"
"ds_read_b128 v[132:135], v218 offset:10240\n"
"v_add_u32 v226, s81, v226\n"
"v_mfma_f32_16x16x32_bf16 v[32:35], a[160:163], v[136:139], v[32:35]\n"
"s_mov_b32 s81, 0x0\n"
"v_mfma_f32_16x16x32_bf16 v[36:39], a[164:167], v[136:139], v[36:39]\n"
"ds_read_b128 v[136:139], v218 offset:11264\n"
"v_mfma_f32_16x16x32_bf16 v[40:43], a[160:163], v[140:143], v[40:43]\n"
"v_mfma_f32_16x16x32_bf16 v[44:47], a[164:167], v[140:143], v[44:47]\n"
"v_mfma_f32_16x16x32_bf16 v[48:51], a[160:163], v[144:147], v[48:51]\n"
"v_mfma_f32_16x16x32_bf16 v[52:55], a[164:167], v[144:147], v[52:55]\n"
"v_mfma_f32_16x16x32_bf16 v[56:59], a[160:163], v[148:151], v[56:59]\n"
"v_mfma_f32_16x16x32_bf16 v[60:63], a[164:167], v[148:151], v[60:63]\n"
"v_mfma_f32_16x16x32_bf16 v[64:67], a[160:163], v[152:155], v[64:67]\n"
"v_mfma_f32_16x16x32_bf16 v[68:71], a[164:167], v[152:155], v[68:71]\n"
"v_mfma_f32_16x16x32_bf16 v[72:75], a[160:163], v[156:159], v[72:75]\n"
"v_mfma_f32_16x16x32_bf16 v[76:79], a[164:167], v[156:159], v[76:79]\n"
"v_mfma_f32_16x16x32_bf16 v[80:83], a[160:163], v[96:99], v[80:83]\n"
"v_mfma_f32_16x16x32_bf16 v[84:87], a[164:167], v[96:99], v[84:87]\n"
"v_mfma_f32_16x16x32_bf16 v[88:91], a[160:163], v[100:103], v[88:91]\n"
"v_mfma_f32_16x16x32_bf16 v[92:95], a[164:167], v[100:103], v[92:95]\n"
"s_waitcnt vmcnt(0) lgkmcnt(0)\n"
"s_barrier\n"
"v_mfma_f32_16x16x32_bf16 v[24:27], a[168:171], v[104:107], v[24:27]\n"
"ds_read_b64_tr_b16 a[200:201], v212 offset:32768\n"
"ds_read_b64_tr_b16 a[202:203], v212 offset:36864\n"
"v_mfma_f32_16x16x32_bf16 v[28:31], a[172:175], v[104:107], v[28:31]\n"
"ds_read_b128 a[216:219], v217 offset:0\n"
"ds_read_b128 a[220:223], v217 offset:1024\n"
"v_mfma_f32_16x16x32_bf16 v[32:35], a[168:171], v[108:111], v[32:35]\n"
"ds_read_b128 a[224:227], v217 offset:2048\n"
"ds_read_b128 v[140:143], v217 offset:3072\n"
"v_mfma_f32_16x16x32_bf16 v[36:39], a[172:175], v[108:111], v[36:39]\n"
"ds_read_b128 a[228:231], v217 offset:49152\n"
"ds_read_b128 a[232:235], v217 offset:50176\n"
"v_mfma_f32_16x16x32_bf16 v[40:43], a[168:171], v[112:115], v[40:43]\n"
"ds_read_b128 a[236:239], v217 offset:51200\n"
"ds_read_b64_tr_b16 a[204:205], v215 offset:32768\n"
"v_mfma_f32_16x16x32_bf16 v[44:47], a[172:175], v[112:115], v[44:47]\n"
"ds_read_b64_tr_b16 a[206:207], v215 offset:36864\n"
"ds_read_b128 v[144:147], v217 offset:4096\n"
"v_mfma_f32_16x16x32_bf16 v[48:51], a[168:171], v[116:119], v[48:51]\n"
"ds_read_b128 v[148:151], v217 offset:5120\n"
"ds_read_b128 v[152:155], v217 offset:6144\n"
"v_mfma_f32_16x16x32_bf16 v[52:55], a[172:175], v[116:119], v[52:55]\n"
"ds_read_b128 v[156:159], v217 offset:7168\n"
"ds_read_b128 v[96:99], v217 offset:8192\n"
"v_mfma_f32_16x16x32_bf16 v[56:59], a[168:171], v[120:123], v[56:59]\n"
"ds_read_b128 v[100:103], v217 offset:9216\n"
"ds_read_b128 v[104:107], v217 offset:10240\n"
"v_mfma_f32_16x16x32_bf16 v[60:63], a[172:175], v[120:123], v[60:63]\n"
"ds_read_b128 v[108:111], v217 offset:11264\n"
"ds_read_b64_tr_b16 a[208:209], v212 offset:40960\n"
"v_mfma_f32_16x16x32_bf16 v[64:67], a[168:171], v[124:127], v[64:67]\n"
"ds_read_b64_tr_b16 a[210:211], v212 offset:45056\n"
"ds_read_b128 v[112:115], v217 offset:52224\n"
"v_mfma_f32_16x16x32_bf16 v[68:71], a[172:175], v[124:127], v[68:71]\n"
"ds_read_b64_tr_b16 a[212:213], v215 offset:40960\n"
"ds_read_b64_tr_b16 a[214:215], v215 offset:45056\n"
"v_mfma_f32_16x16x32_bf16 v[72:75], a[168:171], v[128:131], v[72:75]\n"
"ds_read_b128 v[116:119], v217 offset:53248\n"
"ds_read_b128 v[120:123], v217 offset:54272\n"
"v_mfma_f32_16x16x32_bf16 v[76:79], a[172:175], v[128:131], v[76:79]\n"
"ds_read_b128 v[124:127], v217 offset:55296\n"
"ds_read_b128 v[128:131], v217 offset:56320\n"
"v_mfma_f32_16x16x32_bf16 v[80:83], a[168:171], v[132:135], v[80:83]\n"
"v_mfma_f32_16x16x32_bf16 v[84:87], a[172:175], v[132:135], v[84:87]\n"
"ds_read_b128 v[132:135], v217 offset:57344\n"
"v_mfma_f32_16x16x32_bf16 v[88:91], a[168:171], v[136:139], v[88:91]\n"
"v_mfma_f32_16x16x32_bf16 v[92:95], a[172:175], v[136:139], v[92:95]\n"
"ds_read_b128 v[136:139], v217 offset:58368\n"
"s_waitcnt lgkmcnt(2)\n"
"v_mfma_f32_16x16x32_bf16 v[24:27], a[200:203], v[140:143], v[24:27]\n"
"v_mfma_f32_16x16x32_bf16 v[28:31], a[204:207], v[140:143], v[28:31]\n"
"ds_read_b128 v[140:143], v217 offset:59392\n"
"v_mfma_f32_16x16x32_bf16 v[32:35], a[200:203], v[144:147], v[32:35]\n"
"v_mfma_f32_16x16x32_bf16 v[36:39], a[204:207], v[144:147], v[36:39]\n"
"ds_read_b128 v[144:147], v217 offset:60416\n"
"v_mfma_f32_16x16x32_bf16 v[40:43], a[200:203], v[148:151], v[40:43]\n"
"v_mfma_f32_16x16x32_bf16 v[44:47], a[204:207], v[148:151], v[44:47]\n"
"v_mfma_f32_16x16x32_bf16 v[48:51], a[200:203], v[152:155], v[48:51]\n"
"v_mfma_f32_16x16x32_bf16 v[52:55], a[204:207], v[152:155], v[52:55]\n"
"v_mfma_f32_16x16x32_bf16 v[56:59], a[200:203], v[156:159], v[56:59]\n"
"v_mfma_f32_16x16x32_bf16 v[60:63], a[204:207], v[156:159], v[60:63]\n"
"v_mfma_f32_16x16x32_bf16 v[64:67], a[200:203], v[96:99], v[64:67]\n"
"v_mfma_f32_16x16x32_bf16 v[68:71], a[204:207], v[96:99], v[68:71]\n"
"v_mfma_f32_16x16x32_bf16 v[72:75], a[200:203], v[100:103], v[72:75]\n"
"v_mfma_f32_16x16x32_bf16 v[76:79], a[204:207], v[100:103], v[76:79]\n"
"v_mfma_f32_16x16x32_bf16 v[80:83], a[200:203], v[104:107], v[80:83]\n"
"v_mfma_f32_16x16x32_bf16 v[84:87], a[204:207], v[104:107], v[84:87]\n"
"v_mfma_f32_16x16x32_bf16 v[88:91], a[200:203], v[108:111], v[88:91]\n"
"v_mfma_f32_16x16x32_bf16 v[92:95], a[204:207], v[108:111], v[92:95]\n"
"v_mfma_f32_16x16x32_bf16 v[24:27], a[208:211], v[112:115], v[24:27]\n"
"v_mfma_f32_16x16x32_bf16 v[28:31], a[212:215], v[112:115], v[28:31]\n"
"v_mfma_f32_16x16x32_bf16 v[32:35], a[208:211], v[116:119], v[32:35]\n"
"v_mfma_f32_16x16x32_bf16 v[36:39], a[212:215], v[116:119], v[36:39]\n"
"v_mfma_f32_16x16x32_bf16 v[40:43], a[208:211], v[120:123], v[40:43]\n"
"v_mfma_f32_16x16x32_bf16 v[44:47], a[212:215], v[120:123], v[44:47]\n"
"s_nop 2\n"
"v_cvt_pk_bf16_f32 v160, v24, v25\n"
"v_cvt_pk_bf16_f32 v161, v26, v27\n"
"v_mfma_f32_16x16x32_bf16 v[48:51], a[208:211], v[124:127], v[48:51]\n"
"v_cvt_pk_bf16_f32 v162, v28, v29\n"
"v_cvt_pk_bf16_f32 v163, v30, v31\n"
"buffer_store_dwordx4 v[160:163], v223, s[60:63], s36 offen sc1\n"
"v_mfma_f32_16x16x32_bf16 v[52:55], a[212:215], v[124:127], v[52:55]\n"
"v_cvt_pk_bf16_f32 v164, v32, v33\n"
"v_cvt_pk_bf16_f32 v165, v34, v35\n"
"v_mfma_f32_16x16x32_bf16 v[56:59], a[208:211], v[128:131], v[56:59]\n"
"v_cvt_pk_bf16_f32 v166, v36, v37\n"
"v_cvt_pk_bf16_f32 v167, v38, v39\n"
"buffer_store_dwordx4 v[164:167], v223, s[60:63], s36 offen offset:1024 sc1\n"
"v_mfma_f32_16x16x32_bf16 v[60:63], a[212:215], v[128:131], v[60:63]\n"
"v_cvt_pk_bf16_f32 v168, v40, v41\n"
"v_cvt_pk_bf16_f32 v169, v42, v43\n"
"s_waitcnt lgkmcnt(0)\n"
"v_mfma_f32_16x16x32_bf16 v[64:67], a[208:211], v[132:135], v[64:67]\n"
"v_cvt_pk_bf16_f32 v170, v44, v45\n"
"v_cvt_pk_bf16_f32 v171, v46, v47\n"
"buffer_store_dwordx4 v[168:171], v223, s[60:63], s36 offen offset:2048 sc1\n"
"v_mfma_f32_16x16x32_bf16 v[68:71], a[212:215], v[132:135], v[68:71]\n"
"v_cvt_pk_bf16_f32 v172, v48, v49\n"
"v_cvt_pk_bf16_f32 v173, v50, v51\n"
"v_mfma_f32_16x16x32_bf16 v[72:75], a[208:211], v[136:139], v[72:75]\n"
"v_cvt_pk_bf16_f32 v174, v52, v53\n"
"v_cvt_pk_bf16_f32 v175, v54, v55\n"
"buffer_store_dwordx4 v[172:175], v223, s[60:63], s38 offen sc1\n"
"v_mfma_f32_16x16x32_bf16 v[76:79], a[212:215], v[136:139], v[76:79]\n"
"v_cvt_pk_bf16_f32 v176, v56, v57\n"
"v_cvt_pk_bf16_f32 v177, v58, v59\n"
"v_mfma_f32_16x16x32_bf16 v[80:83], a[208:211], v[140:143], v[80:83]\n"
"v_cvt_pk_bf16_f32 v178, v60, v61\n"
"v_cvt_pk_bf16_f32 v179, v62, v63\n"
"buffer_store_dwordx4 v[176:179], v223, s[60:63], s38 offen offset:1024 sc1\n"
"v_mfma_f32_16x16x32_bf16 v[84:87], a[212:215], v[140:143], v[84:87]\n"
"v_cvt_pk_bf16_f32 v180, v64, v65\n"
"v_cvt_pk_bf16_f32 v181, v66, v67\n"
"v_mfma_f32_16x16x32_bf16 v[88:91], a[208:211], v[144:147], v[88:91]\n"
"v_cvt_pk_bf16_f32 v182, v68, v69\n"
"v_cvt_pk_bf16_f32 v183, v70, v71\n"
"buffer_store_dwordx4 v[180:183], v223, s[60:63], s38 offen offset:2048 sc1\n"
"v_mfma_f32_16x16x32_bf16 v[92:95], a[212:215], v[144:147], v[92:95]\n"
"v_cvt_pk_bf16_f32 v184, v72, v73\n"
"v_cvt_pk_bf16_f32 v185, v74, v75\n"
"v_mfma_f32_16x16x32_bf16 v[0:3], a[0:3], a[16:19], v[0:3]\n"
"v_cvt_pk_bf16_f32 v186, v76, v77\n"
"v_cvt_pk_bf16_f32 v187, v78, v79\n"
"buffer_store_dwordx4 v[184:187], v223, s[60:63], s39 offen sc1\n"
"v_mfma_f32_16x16x32_bf16 v[4:7], a[4:7], a[16:19], v[4:7]\n"
"v_cvt_pk_bf16_f32 v188, v80, v81\n"
"v_cvt_pk_bf16_f32 v189, v82, v83\n"
"v_mfma_f32_16x16x32_bf16 v[8:11], a[0:3], a[20:23], v[8:11]\n"
"v_cvt_pk_bf16_f32 v190, v84, v85\n"
"v_cvt_pk_bf16_f32 v191, v86, v87\n"
"buffer_store_dwordx4 v[188:191], v223, s[60:63], s39 offen offset:1024 sc1\n"
"v_mfma_f32_16x16x32_bf16 v[12:15], a[4:7], a[20:23], v[12:15]\n"
"v_cvt_pk_bf16_f32 v160, v88, v89\n"
"v_cvt_pk_bf16_f32 v161, v90, v91\n"
"v_mfma_f32_16x16x32_bf16 v[16:19], a[0:3], a[24:27], v[16:19]\n"
"v_cvt_pk_bf16_f32 v162, v92, v93\n"
"v_cvt_pk_bf16_f32 v163, v94, v95\n"
"buffer_store_dwordx4 v[160:163], v223, s[60:63], s39 offen offset:2048 sc1\n"
"v_mfma_f32_16x16x32_bf16 v[20:23], a[4:7], a[24:27], v[20:23]\n"
"v_mfma_f32_16x16x32_bf16 v[0:3], a[8:11], a[28:31], v[0:3]\n"
"v_mfma_f32_16x16x32_bf16 v[4:7], a[12:15], a[28:31], v[4:7]\n"
"v_mfma_f32_16x16x32_bf16 v[8:11], a[8:11], a[32:35], v[8:11]\n"
"v_mfma_f32_16x16x32_bf16 v[12:15], a[12:15], a[32:35], v[12:15]\n"
"v_mfma_f32_16x16x32_bf16 v[16:19], a[8:11], a[36:39], v[16:19]\n"
"v_mfma_f32_16x16x32_bf16 v[20:23], a[12:15], a[36:39], v[20:23]\n"
"v_mfma_f32_16x16x32_bf16 v[0:3], a[40:43], a[56:59], v[0:3]\n"
"v_mfma_f32_16x16x32_bf16 v[4:7], a[44:47], a[56:59], v[4:7]\n"
"v_mfma_f32_16x16x32_bf16 v[8:11], a[40:43], a[60:63], v[8:11]\n"
"v_mfma_f32_16x16x32_bf16 v[12:15], a[44:47], a[60:63], v[12:15]\n"
"v_mfma_f32_16x16x32_bf16 v[16:19], a[40:43], a[64:67], v[16:19]\n"
"v_mfma_f32_16x16x32_bf16 v[20:23], a[44:47], a[64:67], v[20:23]\n"
"v_mfma_f32_16x16x32_bf16 v[0:3], a[48:51], a[68:71], v[0:3]\n"
"v_mfma_f32_16x16x32_bf16 v[4:7], a[52:55], a[68:71], v[4:7]\n"
"v_mfma_f32_16x16x32_bf16 v[8:11], a[48:51], a[72:75], v[8:11]\n"
"v_mfma_f32_16x16x32_bf16 v[12:15], a[52:55], a[72:75], v[12:15]\n"
"v_mfma_f32_16x16x32_bf16 v[16:19], a[48:51], a[76:79], v[16:19]\n"
"v_mfma_f32_16x16x32_bf16 v[20:23], a[52:55], a[76:79], v[20:23]\n"
"v_mfma_f32_16x16x32_bf16 v[0:3], a[80:83], a[96:99], v[0:3]\n"
"v_mfma_f32_16x16x32_bf16 v[4:7], a[84:87], a[96:99], v[4:7]\n"
"v_mfma_f32_16x16x32_bf16 v[8:11], a[80:83], a[100:103], v[8:11]\n"
"v_mfma_f32_16x16x32_bf16 v[12:15], a[84:87], a[100:103], v[12:15]\n"
"v_mfma_f32_16x16x32_bf16 v[16:19], a[80:83], a[104:107], v[16:19]\n"
"v_mfma_f32_16x16x32_bf16 v[20:23], a[84:87], a[104:107], v[20:23]\n"
"v_mfma_f32_16x16x32_bf16 v[0:3], a[88:91], a[108:111], v[0:3]\n"
"v_mfma_f32_16x16x32_bf16 v[4:7], a[92:95], a[108:111], v[4:7]\n"
"v_mfma_f32_16x16x32_bf16 v[8:11], a[88:91], a[112:115], v[8:11]\n"
"v_mfma_f32_16x16x32_bf16 v[12:15], a[92:95], a[112:115], v[12:15]\n"
"v_mfma_f32_16x16x32_bf16 v[16:19], a[88:91], a[116:119], v[16:19]\n"
"v_mfma_f32_16x16x32_bf16 v[20:23], a[92:95], a[116:119], v[20:23]\n"
"v_mfma_f32_16x16x32_bf16 v[0:3], a[120:123], a[136:139], v[0:3]\n"
"v_mfma_f32_16x16x32_bf16 v[4:7], a[124:127], a[136:139], v[4:7]\n"
"v_mfma_f32_16x16x32_bf16 v[8:11], a[120:123], a[140:143], v[8:11]\n"
"v_mfma_f32_16x16x32_bf16 v[12:15], a[124:127], a[140:143], v[12:15]\n"
"v_mfma_f32_16x16x32_bf16 v[16:19], a[120:123], a[144:147], v[16:19]\n"
"v_mfma_f32_16x16x32_bf16 v[20:23], a[124:127], a[144:147], v[20:23]\n"
"v_mfma_f32_16x16x32_bf16 v[0:3], a[128:131], a[148:151], v[0:3]\n"
"v_mfma_f32_16x16x32_bf16 v[4:7], a[132:135], a[148:151], v[4:7]\n"
"v_mfma_f32_16x16x32_bf16 v[8:11], a[128:131], a[152:155], v[8:11]\n"
"v_mfma_f32_16x16x32_bf16 v[12:15], a[132:135], a[152:155], v[12:15]\n"
"v_mfma_f32_16x16x32_bf16 v[16:19], a[128:131], a[156:159], v[16:19]\n"
"v_mfma_f32_16x16x32_bf16 v[20:23], a[132:135], a[156:159], v[20:23]\n"
"v_mfma_f32_16x16x32_bf16 v[0:3], a[160:163], a[176:179], v[0:3]\n"
"v_mfma_f32_16x16x32_bf16 v[4:7], a[164:167], a[176:179], v[4:7]\n"
"v_mfma_f32_16x16x32_bf16 v[8:11], a[160:163], a[180:183], v[8:11]\n"
"v_mfma_f32_16x16x32_bf16 v[12:15], a[164:167], a[180:183], v[12:15]\n"
"v_mfma_f32_16x16x32_bf16 v[16:19], a[160:163], a[184:187], v[16:19]\n"
"v_mfma_f32_16x16x32_bf16 v[20:23], a[164:167], a[184:187], v[20:23]\n"
"v_mfma_f32_16x16x32_bf16 v[0:3], a[168:171], a[188:191], v[0:3]\n"
"v_mfma_f32_16x16x32_bf16 v[4:7], a[172:175], a[188:191], v[4:7]\n"
"v_mfma_f32_16x16x32_bf16 v[8:11], a[168:171], a[192:195], v[8:11]\n"
"v_mfma_f32_16x16x32_bf16 v[12:15], a[172:175], a[192:195], v[12:15]\n"
"v_mfma_f32_16x16x32_bf16 v[16:19], a[168:171], a[196:199], v[16:19]\n"
"v_mfma_f32_16x16x32_bf16 v[20:23], a[172:175], a[196:199], v[20:23]\n"
"s_waitcnt vmcnt(0)\n"
"s_barrier\n"
"v_mov_b32 v246, s90\n"
"v_mfma_f32_16x16x32_bf16 v[0:3], a[200:203], a[216:219], v[0:3]\n"
"v_mov_b32 v247, s91\n"
"buffer_store_dwordx2 v[246:247], v223, s[64:67], 0 offen sc1\n"
"v_mfma_f32_16x16x32_bf16 v[4:7], a[204:207], a[216:219], v[4:7]\n"
"s_add_u32 s64, s64, 0x800\n"
"s_addc_u32 s65, s65, 0\n"
"v_mfma_f32_16x16x32_bf16 v[8:11], a[200:203], a[220:223], v[8:11]\n"
"v_mfma_f32_16x16x32_bf16 v[12:15], a[204:207], a[220:223], v[12:15]\n"
"v_mfma_f32_16x16x32_bf16 v[16:19], a[200:203], a[224:227], v[16:19]\n"
"v_mfma_f32_16x16x32_bf16 v[20:23], a[204:207], a[224:227], v[20:23]\n"
"v_mfma_f32_16x16x32_bf16 v[0:3], a[208:211], a[228:231], v[0:3]\n"
"v_mfma_f32_16x16x32_bf16 v[4:7], a[212:215], a[228:231], v[4:7]\n"
"v_mfma_f32_16x16x32_bf16 v[8:11], a[208:211], a[232:235], v[8:11]\n"
"v_mfma_f32_16x16x32_bf16 v[12:15], a[212:215], a[232:235], v[12:15]\n"
"v_mfma_f32_16x16x32_bf16 v[16:19], a[208:211], a[236:239], v[16:19]\n"
"v_mfma_f32_16x16x32_bf16 v[20:23], a[212:215], a[236:239], v[20:23]\n"
"s_cmp_eq_u32 s76, 0\n"
"s_cbranch_scc1 LspinX_%=\n"
"LretX_%=:\n"
"s_barrier\n"
"buffer_load_dwordx4 v[96:99], v223, s[60:63], s79 offen sc1\n"
"buffer_load_dwordx4 v[100:103], v223, s[60:63], s79 offen offset:1024 sc1\n"
"buffer_load_dwordx4 v[104:107], v223, s[60:63], s79 offen offset:2048 sc1\n"
"s_add_u32 s79, s79, 0x6000\n"
"buffer_load_dwordx4 v[108:111], v223, s[60:63], s79 offen sc1\n"
"buffer_load_dwordx4 v[112:115], v223, s[60:63], s79 offen offset:1024 sc1\n"
"buffer_load_dwordx4 v[116:119], v223, s[60:63], s79 offen offset:2048 sc1\n"
"s_add_u32 s79, s79, 0x6000\n"
"buffer_load_dwordx4 v[120:123], v223, s[60:63], s79 offen sc1\n"
"buffer_load_dwordx4 v[124:127], v223, s[60:63], s79 offen offset:1024 sc1\n"
"buffer_load_dwordx4 v[128:131], v223, s[60:63], s79 offen offset:2048 sc1\n"
"s_waitcnt vmcnt(8)\n"
"v_dot2_f32_bf16 v0, v96, s22, v0\n"
"v_dot2_f32_bf16 v1, v96, s23, v1\n"
"v_dot2_f32_bf16 v2, v97, s22, v2\n"
"v_dot2_f32_bf16 v3, v97, s23, v3\n"
"s_waitcnt vmcnt(5)\n"
"v_dot2_f32_bf16 v0, v108, s22, v0\n"
"v_dot2_f32_bf16 v1, v108, s23, v1\n"
"v_dot2_f32_bf16 v2, v109, s22, v2\n"
"v_dot2_f32_bf16 v3, v109, s23, v3\n"
"s_waitcnt vmcnt(2)\n"
"v_dot2_f32_bf16 v0, v120, s22, v0\n"
"v_dot2_f32_bf16 v1, v120, s23, v1\n"
"v_dot2_f32_bf16 v2, v121, s22, v2\n"
"v_dot2_f32_bf16 v3, v121, s23, v3\n"
"s_nop 0\n"
"v_cvt_pk_bf16_f32 v0, v0, v1\n"
"s_nop 0\n"
"v_cvt_pk_bf16_f32 v1, v2, v3\n"
"buffer_store_dwordx2 v[0:1], v226, s[72:75], s81 offen\n"
"v_dot2_f32_bf16 v4, v98, s22, v4\n"
"v_dot2_f32_bf16 v5, v98, s23, v5\n"
"v_dot2_f32_bf16 v6, v99, s22, v6\n"
"v_dot2_f32_bf16 v7, v99, s23, v7\n"
"v_dot2_f32_bf16 v4, v110, s22, v4\n"
"v_dot2_f32_bf16 v5, v110, s23, v5\n"
"v_dot2_f32_bf16 v6, v111, s22, v6\n"
"v_dot2_f32_bf16 v7, v111, s23, v7\n"
"v_dot2_f32_bf16 v4, v122, s22, v4\n"
"v_dot2_f32_bf16 v5, v122, s23, v5\n"
"v_dot2_f32_bf16 v6, v123, s22, v6\n"
"v_dot2_f32_bf16 v7, v123, s23, v7\n"
"s_nop 0\n"
"v_cvt_pk_bf16_f32 v4, v4, v5\n"
"s_nop 0\n"
"v_cvt_pk_bf16_f32 v5, v6, v7\n"
"buffer_store_dwordx2 v[4:5], v226, s[72:75], s81 offen offset:32\n"
"v_dot2_f32_bf16 v8, v100, s22, v8\n"
"v_dot2_f32_bf16 v9, v100, s23, v9\n"
"v_dot2_f32_bf16 v10, v101, s22, v10\n"
"v_dot2_f32_bf16 v11, v101, s23, v11\n"
"v_dot2_f32_bf16 v8, v112, s22, v8\n"
"v_dot2_f32_bf16 v9, v112, s23, v9\n"
"v_dot2_f32_bf16 v10, v113, s22, v10\n"
"v_dot2_f32_bf16 v11, v113, s23, v11\n"
"s_waitcnt vmcnt(3)\n"
"v_dot2_f32_bf16 v8, v124, s22, v8\n"
"v_dot2_f32_bf16 v9, v124, s23, v9\n"
"v_dot2_f32_bf16 v10, v125, s22, v10\n"
"v_dot2_f32_bf16 v11, v125, s23, v11\n"
"s_nop 0\n"
"v_cvt_pk_bf16_f32 v8, v8, v9\n"
"s_nop 0\n"
"v_cvt_pk_bf16_f32 v9, v10, v11\n"
"s_mov_b32 s81, 0x4000\n"
"buffer_store_dwordx2 v[8:9], v226, s[72:75], s81 offen\n"
"v_dot2_f32_bf16 v12, v102, s22, v12\n"
"v_dot2_f32_bf16 v13, v102, s23, v13\n"
"v_dot2_f32_bf16 v14, v103, s22, v14\n"
"v_dot2_f32_bf16 v15, v103, s23, v15\n"
"v_dot2_f32_bf16 v12, v114, s22, v12\n"
"v_dot2_f32_bf16 v13, v114, s23, v13\n"
"v_dot2_f32_bf16 v14, v115, s22, v14\n"
"v_dot2_f32_bf16 v15, v115, s23, v15\n"
"v_dot2_f32_bf16 v12, v126, s22, v12\n"
"v_dot2_f32_bf16 v13, v126, s23, v13\n"
"v_dot2_f32_bf16 v14, v127, s22, v14\n"
"v_dot2_f32_bf16 v15, v127, s23, v15\n"
"s_nop 0\n"
"v_cvt_pk_bf16_f32 v12, v12, v13\n"
"s_nop 0\n"
"v_cvt_pk_bf16_f32 v13, v14, v15\n"
"buffer_store_dwordx2 v[12:13], v226, s[72:75], s81 offen offset:32\n"
"v_dot2_f32_bf16 v16, v104, s22, v16\n"
"v_dot2_f32_bf16 v17, v104, s23, v17\n"
"v_dot2_f32_bf16 v18, v105, s22, v18\n"
"v_dot2_f32_bf16 v19, v105, s23, v19\n"
"v_dot2_f32_bf16 v16, v116, s22, v16\n"
"v_dot2_f32_bf16 v17, v116, s23, v17\n"
"v_dot2_f32_bf16 v18, v117, s22, v18\n"
"v_dot2_f32_bf16 v19, v117, s23, v19\n"
"s_waitcnt vmcnt(4)\n"
"v_dot2_f32_bf16 v16, v128, s22, v16\n"
"v_dot2_f32_bf16 v17, v128, s23, v17\n"
"v_dot2_f32_bf16 v18, v129, s22, v18\n"
"v_dot2_f32_bf16 v19, v129, s23, v19\n"
"s_nop 0\n"
"v_cvt_pk_bf16_f32 v16, v16, v17\n"
"s_nop 0\n"
"v_cvt_pk_bf16_f32 v17, v18, v19\n"
"s_mov_b32 s81, 0x8000\n"
"buffer_store_dwordx2 v[16:17], v226, s[72:75], s81 offen\n"
"v_dot2_f32_bf16 v20, v106, s22, v20\n"
"v_dot2_f32_bf16 v21, v106, s23, v21\n"
"v_dot2_f32_bf16 v22, v107, s22, v22\n"
"v_dot2_f32_bf16 v23, v107, s23, v23\n"
"v_dot2_f32_bf16 v20, v118, s22, v20\n"
"v_dot2_f32_bf16 v21, v118, s23, v21\n"
"v_dot2_f32_bf16 v22, v119, s22, v22\n"
"v_dot2_f32_bf16 v23, v119, s23, v23\n"
"v_dot2_f32_bf16 v20, v130, s22, v20\n"
"v_dot2_f32_bf16 v21, v130, s23, v21\n"
"v_dot2_f32_bf16 v22, v131, s22, v22\n"
"v_dot2_f32_bf16 v23, v131, s23, v23\n"
"s_nop 0\n"
"v_cvt_pk_bf16_f32 v20, v20, v21\n"
"s_nop 0\n"
"v_cvt_pk_bf16_f32 v21, v22, v23\n"
"buffer_store_dwordx2 v[20:21], v226, s[72:75], s81 offen offset:32\n"
"s_or_b32 s21, s20, s76\n"
"s_cbranch_scc0 Lepo_%=\n"
"Lepr_%=:\n"
"s_branch Lend_%=\n"
"Lslow0_%=:\n"
"s_nop 7\n"
"s_nop 7\n"
"v_lshrrev_b32 v250, 4, v223\n"
"v_and_b32 v243, 15, v250\n"
"v_lshrrev_b32 v244, 4, v250\n"
"v_lshlrev_b32 v242, 8, v243\n"
"v_lshl_add_u32 v242, v244, 4, v242\n"
"v_lshlrev_b32 v243, 1, v243\n"
"v_lshl_add_u32 v243, v244, 10, v243\n"
"v_add_u32 v244, 0x1000, v243\n"
"v_mov_b32 v245, 0xff800000\n"
"v_mov_b32 v246, 0xff800000\n"
"v_mov_b32 v247, 0xff800000\n"
"s_mov_b32 s21, 0x0\n"
"Lp10_%=:\n"
"s_add_u32 s22, s21, 0\n"
"buffer_load_dwordx4 v[226:229], v242, s[44:47], s22 offen offset:0\n"
"buffer_load_dwordx4 v[230:233], v242, s[44:47], s22 offen offset:64\n"
"buffer_load_dwordx4 v[234:237], v242, s[44:47], s22 offen offset:128\n"
"buffer_load_dwordx4 v[238:241], v242, s[44:47], s22 offen offset:192\n"
"s_waitcnt vmcnt(0)\n"
"v_mfma_f32_16x16x32_bf16 v[144:147], v[226:229], v[96:99], 0\n"
"v_mfma_f32_16x16x32_bf16 v[144:147], v[230:233], v[100:103], v[144:147]\n"
"v_mfma_f32_16x16x32_bf16 v[144:147], v[234:237], v[104:107], v[144:147]\n"
"v_mfma_f32_16x16x32_bf16 v[144:147], v[238:241], v[108:111], v[144:147]\n"
"v_mfma_f32_16x16x32_bf16 v[152:155], v[226:229], v[112:115], 0\n"
"v_mfma_f32_16x16x32_bf16 v[152:155], v[230:233], v[116:119], v[152:155]\n"
"v_mfma_f32_16x16x32_bf16 v[152:155], v[234:237], v[120:123], v[152:155]\n"
"v_mfma_f32_16x16x32_bf16 v[152:155], v[238:241], v[124:127], v[152:155]\n"
"v_mfma_f32_16x16x32_bf16 v[160:163], v[226:229], v[128:131], 0\n"
"v_mfma_f32_16x16x32_bf16 v[160:163], v[230:233], v[132:135], v[160:163]\n"
"v_mfma_f32_16x16x32_bf16 v[160:163], v[234:237], v[136:139], v[160:163]\n"
"v_mfma_f32_16x16x32_bf16 v[160:163], v[238:241], v[140:143], v[160:163]\n"
"s_nop 7\n"
"s_nop 7\n"
"s_add_u32 s22, s21, 4096\n"
"buffer_load_dwordx4 v[226:229], v242, s[44:47], s22 offen offset:0\n"
"buffer_load_dwordx4 v[230:233], v242, s[44:47], s22 offen offset:64\n"
"buffer_load_dwordx4 v[234:237], v242, s[44:47], s22 offen offset:128\n"
"buffer_load_dwordx4 v[238:241], v242, s[44:47], s22 offen offset:192\n"
"s_waitcnt vmcnt(0)\n"
"v_mfma_f32_16x16x32_bf16 v[148:151], v[226:229], v[96:99], 0\n"
"v_mfma_f32_16x16x32_bf16 v[148:151], v[230:233], v[100:103], v[148:151]\n"
"v_mfma_f32_16x16x32_bf16 v[148:151], v[234:237], v[104:107], v[148:151]\n"
"v_mfma_f32_16x16x32_bf16 v[148:151], v[238:241], v[108:111], v[148:151]\n"
"v_mfma_f32_16x16x32_bf16 v[156:159], v[226:229], v[112:115], 0\n"
"v_mfma_f32_16x16x32_bf16 v[156:159], v[230:233], v[116:119], v[156:159]\n"
"v_mfma_f32_16x16x32_bf16 v[156:159], v[234:237], v[120:123], v[156:159]\n"
"v_mfma_f32_16x16x32_bf16 v[156:159], v[238:241], v[124:127], v[156:159]\n"
"v_mfma_f32_16x16x32_bf16 v[164:167], v[226:229], v[128:131], 0\n"
"v_mfma_f32_16x16x32_bf16 v[164:167], v[230:233], v[132:135], v[164:167]\n"
"v_mfma_f32_16x16x32_bf16 v[164:167], v[234:237], v[136:139], v[164:167]\n"
"v_mfma_f32_16x16x32_bf16 v[164:167], v[238:241], v[140:143], v[164:167]\n"
"s_nop 7\n"
"s_nop 7\n"
"v_max3_f32 v245, v245, v144, v145\n"
"v_max3_f32 v245, v245, v146, v147\n"
"v_max3_f32 v245, v245, v148, v149\n"
"v_max3_f32 v245, v245, v150, v151\n"
"v_max3_f32 v246, v246, v152, v153\n"
"v_max3_f32 v246, v246, v154, v155\n"
"v_max3_f32 v246, v246, v156, v157\n"
"v_max3_f32 v246, v246, v158, v159\n"
"v_max3_f32 v247, v247, v160, v161\n"
"v_max3_f32 v247, v247, v162, v163\n"
"v_max3_f32 v247, v247, v164, v165\n"
"v_max3_f32 v247, v247, v166, v167\n"
"s_add_u32 s21, s21, 0x2000\n"
"s_cmp_lt_u32 s21, 0x20000\n"
"s_cbranch_scc1 Lp10_%=\n"
"v_mov_b32 v248, v245\n"
"s_nop 1\n"
"v_permlane32_swap_b32 v245, v248\n"
"v_max_f32 v245, v245, v248\n"
"v_mov_b32 v248, v245\n"
"s_nop 1\n"
"v_permlane16_swap_b32 v245, v248\n"
"v_min_f32_e64 v180, -v245, -v248\n"
"v_mov_b32 v181, v180\n"
"v_mov_b32 v182, v180\n"
"v_mov_b32 v183, v180\n"
"v_mov_b32 v248, v246\n"
"s_nop 1\n"
"v_permlane32_swap_b32 v246, v248\n"
"v_max_f32 v246, v246, v248\n"
"v_mov_b32 v248, v246\n"
"s_nop 1\n"
"v_permlane16_swap_b32 v246, v248\n"
"v_min_f32_e64 v184, -v246, -v248\n"
"v_mov_b32 v185, v184\n"
"v_mov_b32 v186, v184\n"
"v_mov_b32 v187, v184\n"
"v_mov_b32 v248, v247\n"
"s_nop 1\n"
"v_permlane32_swap_b32 v247, v248\n"
"v_max_f32 v247, v247, v248\n"
"v_mov_b32 v248, v247\n"
"s_nop 1\n"
"v_permlane16_swap_b32 v247, v248\n"
"v_min_f32_e64 v188, -v247, -v248\n"
"v_mov_b32 v189, v188\n"
"v_mov_b32 v190, v188\n"
"v_mov_b32 v191, v188\n"
"v_mov_b32 v0, 0\n"
"v_mov_b32 v1, 0\n"
"v_mov_b32 v2, 0\n"
"v_mov_b32 v3, 0\n"
"v_mov_b32 v4, 0\n"
"v_mov_b32 v5, 0\n"
"v_mov_b32 v6, 0\n"
"v_mov_b32 v7, 0\n"
"v_mov_b32 v8, 0\n"
"v_mov_b32 v9, 0\n"
"v_mov_b32 v10, 0\n"
"v_mov_b32 v11, 0\n"
"v_mov_b32 v12, 0\n"
"v_mov_b32 v13, 0\n"
"v_mov_b32 v14, 0\n"
"v_mov_b32 v15, 0\n"
"v_mov_b32 v16, 0\n"
"v_mov_b32 v17, 0\n"
"v_mov_b32 v18, 0\n"
"v_mov_b32 v19, 0\n"
"v_mov_b32 v20, 0\n"
"v_mov_b32 v21, 0\n"
"v_mov_b32 v22, 0\n"
"v_mov_b32 v23, 0\n"
"v_mov_b32 v24, 0\n"
"v_mov_b32 v25, 0\n"
"v_mov_b32 v26, 0\n"
"v_mov_b32 v27, 0\n"
"v_mov_b32 v28, 0\n"
"v_mov_b32 v29, 0\n"
"v_mov_b32 v30, 0\n"
"v_mov_b32 v31, 0\n"
"v_mov_b32 v32, 0\n"
"v_mov_b32 v33, 0\n"
"v_mov_b32 v34, 0\n"
"v_mov_b32 v35, 0\n"
"v_mov_b32 v36, 0\n"
"v_mov_b32 v37, 0\n"
"v_mov_b32 v38, 0\n"
"v_mov_b32 v39, 0\n"
"v_mov_b32 v40, 0\n"
"v_mov_b32 v41, 0\n"
"v_mov_b32 v42, 0\n"
"v_mov_b32 v43, 0\n"
"v_mov_b32 v44, 0\n"
"v_mov_b32 v45, 0\n"
"v_mov_b32 v46, 0\n"
"v_mov_b32 v47, 0\n"
"v_mov_b32 v48, 0\n"
"v_mov_b32 v49, 0\n"
"v_mov_b32 v50, 0\n"
"v_mov_b32 v51, 0\n"
"v_mov_b32 v52, 0\n"
"v_mov_b32 v53, 0\n"
"v_mov_b32 v54, 0\n"
"v_mov_b32 v55, 0\n"
"v_mov_b32 v56, 0\n"
"v_mov_b32 v57, 0\n"
"v_mov_b32 v58, 0\n"
"v_mov_b32 v59, 0\n"
"v_mov_b32 v60, 0\n"
"v_mov_b32 v61, 0\n"
"v_mov_b32 v62, 0\n"
"v_mov_b32 v63, 0\n"
"v_mov_b32 v64, 0\n"
"v_mov_b32 v65, 0\n"
"v_mov_b32 v66, 0\n"
"v_mov_b32 v67, 0\n"
"v_mov_b32 v68, 0\n"
"v_mov_b32 v69, 0\n"
"v_mov_b32 v70, 0\n"
"v_mov_b32 v71, 0\n"
"v_mov_b32 v72, 0\n"
"v_mov_b32 v73, 0\n"
"v_mov_b32 v74, 0\n"
"v_mov_b32 v75, 0\n"
"v_mov_b32 v76, 0\n"
"v_mov_b32 v77, 0\n"
"v_mov_b32 v78, 0\n"
"v_mov_b32 v79, 0\n"
"v_mov_b32 v80, 0\n"
"v_mov_b32 v81, 0\n"
"v_mov_b32 v82, 0\n"
"v_mov_b32 v83, 0\n"
"v_mov_b32 v84, 0\n"
"v_mov_b32 v85, 0\n"
"v_mov_b32 v86, 0\n"
"v_mov_b32 v87, 0\n"
"v_mov_b32 v88, 0\n"
"v_mov_b32 v89, 0\n"
"v_mov_b32 v90, 0\n"
"v_mov_b32 v91, 0\n"
"v_mov_b32 v92, 0\n"
"v_mov_b32 v93, 0\n"
"v_mov_b32 v94, 0\n"
"v_mov_b32 v95, 0\n"
"v_mov_b32 v192, 0\n"
"v_mov_b32 v193, 0\n"
"v_mov_b32 v194, 0\n"
"v_mov_b32 v195, 0\n"
"v_mov_b32 v196, 0\n"
"v_mov_b32 v197, 0\n"
"v_mov_b32 v198, 0\n"
"v_mov_b32 v199, 0\n"
"v_mov_b32 v200, 0\n"
"v_mov_b32 v201, 0\n"
"v_mov_b32 v202, 0\n"
"v_mov_b32 v203, 0\n"
"s_mov_b32 s21, 0x0\n"
"Lp20_%=:\n"
"s_add_u32 s22, s21, 0\n"
"buffer_load_dwordx4 v[226:229], v242, s[44:47], s22 offen offset:0\n"
"buffer_load_dwordx4 v[230:233], v242, s[44:47], s22 offen offset:64\n"
"buffer_load_dwordx4 v[234:237], v242, s[44:47], s22 offen offset:128\n"
"buffer_load_dwordx4 v[238:241], v242, s[44:47], s22 offen offset:192\n"
"s_waitcnt vmcnt(0)\n"
"v_mfma_f32_16x16x32_bf16 v[144:147], v[226:229], v[96:99], v[180:183]\n"
"v_mfma_f32_16x16x32_bf16 v[144:147], v[230:233], v[100:103], v[144:147]\n"
"v_mfma_f32_16x16x32_bf16 v[144:147], v[234:237], v[104:107], v[144:147]\n"
"v_mfma_f32_16x16x32_bf16 v[144:147], v[238:241], v[108:111], v[144:147]\n"
"v_mfma_f32_16x16x32_bf16 v[152:155], v[226:229], v[112:115], v[184:187]\n"
"v_mfma_f32_16x16x32_bf16 v[152:155], v[230:233], v[116:119], v[152:155]\n"
"v_mfma_f32_16x16x32_bf16 v[152:155], v[234:237], v[120:123], v[152:155]\n"
"v_mfma_f32_16x16x32_bf16 v[152:155], v[238:241], v[124:127], v[152:155]\n"
"v_mfma_f32_16x16x32_bf16 v[160:163], v[226:229], v[128:131], v[188:191]\n"
"v_mfma_f32_16x16x32_bf16 v[160:163], v[230:233], v[132:135], v[160:163]\n"
"v_mfma_f32_16x16x32_bf16 v[160:163], v[234:237], v[136:139], v[160:163]\n"
"v_mfma_f32_16x16x32_bf16 v[160:163], v[238:241], v[140:143], v[160:163]\n"
"s_nop 7\n"
"s_nop 7\n"
"s_add_u32 s22, s21, 4096\n"
"buffer_load_dwordx4 v[226:229], v242, s[44:47], s22 offen offset:0\n"
"buffer_load_dwordx4 v[230:233], v242, s[44:47], s22 offen offset:64\n"
"buffer_load_dwordx4 v[234:237], v242, s[44:47], s22 offen offset:128\n"
"buffer_load_dwordx4 v[238:241], v242, s[44:47], s22 offen offset:192\n"
"s_waitcnt vmcnt(0)\n"
"v_mfma_f32_16x16x32_bf16 v[148:151], v[226:229], v[96:99], v[180:183]\n"
"v_mfma_f32_16x16x32_bf16 v[148:151], v[230:233], v[100:103], v[148:151]\n"
"v_mfma_f32_16x16x32_bf16 v[148:151], v[234:237], v[104:107], v[148:151]\n"
"v_mfma_f32_16x16x32_bf16 v[148:151], v[238:241], v[108:111], v[148:151]\n"
"v_mfma_f32_16x16x32_bf16 v[156:159], v[226:229], v[112:115], v[184:187]\n"
"v_mfma_f32_16x16x32_bf16 v[156:159], v[230:233], v[116:119], v[156:159]\n"
"v_mfma_f32_16x16x32_bf16 v[156:159], v[234:237], v[120:123], v[156:159]\n"
"v_mfma_f32_16x16x32_bf16 v[156:159], v[238:241], v[124:127], v[156:159]\n"
"v_mfma_f32_16x16x32_bf16 v[164:167], v[226:229], v[128:131], v[188:191]\n"
"v_mfma_f32_16x16x32_bf16 v[164:167], v[230:233], v[132:135], v[164:167]\n"
"v_mfma_f32_16x16x32_bf16 v[164:167], v[234:237], v[136:139], v[164:167]\n"
"v_mfma_f32_16x16x32_bf16 v[164:167], v[238:241], v[140:143], v[164:167]\n"
"s_nop 7\n"
"s_nop 7\n"
"v_exp_f32 v144, v144\n"
"v_exp_f32 v145, v145\n"
"v_exp_f32 v146, v146\n"
"v_exp_f32 v147, v147\n"
"v_exp_f32 v148, v148\n"
"v_exp_f32 v149, v149\n"
"v_exp_f32 v150, v150\n"
"v_exp_f32 v151, v151\n"
"v_exp_f32 v152, v152\n"
"v_exp_f32 v153, v153\n"
"v_exp_f32 v154, v154\n"
"v_exp_f32 v155, v155\n"
"v_exp_f32 v156, v156\n"
"v_exp_f32 v157, v157\n"
"v_exp_f32 v158, v158\n"
"v_exp_f32 v159, v159\n"
"v_exp_f32 v160, v160\n"
"v_exp_f32 v161, v161\n"
"v_exp_f32 v162, v162\n"
"v_exp_f32 v163, v163\n"
"v_exp_f32 v164, v164\n"
"v_exp_f32 v165, v165\n"
"v_exp_f32 v166, v166\n"
"v_exp_f32 v167, v167\n"
"s_nop 1\n"
"v_cvt_pk_bf16_f32 v168, v144, v145\n"
"v_cvt_pk_bf16_f32 v169, v146, v147\n"
"v_cvt_pk_bf16_f32 v170, v148, v149\n"
"v_cvt_pk_bf16_f32 v171, v150, v151\n"
"v_cvt_pk_bf16_f32 v172, v152, v153\n"
"v_cvt_pk_bf16_f32 v173, v154, v155\n"
"v_cvt_pk_bf16_f32 v174, v156, v157\n"
"v_cvt_pk_bf16_f32 v175, v158, v159\n"
"v_cvt_pk_bf16_f32 v176, v160, v161\n"
"v_cvt_pk_bf16_f32 v177, v162, v163\n"
"v_cvt_pk_bf16_f32 v178, v164, v165\n"
"v_cvt_pk_bf16_f32 v179, v166, v167\n"
"s_nop 2\n"
"v_mfma_f32_16x16x32_bf16 v[192:195], v[204:207], v[168:171], v[192:195]\n"
"v_mfma_f32_16x16x32_bf16 v[196:199], v[204:207], v[172:175], v[196:199]\n"
"v_mfma_f32_16x16x32_bf16 v[200:203], v[204:207], v[176:179], v[200:203]\n"
"buffer_load_ushort v226, v243, s[48:51], s21 offen offset:0\n"
"buffer_load_ushort v227, v243, s[48:51], s21 offen offset:256\n"
"buffer_load_ushort v228, v243, s[48:51], s21 offen offset:512\n"
"buffer_load_ushort v229, v243, s[48:51], s21 offen offset:768\n"
"buffer_load_ushort v230, v244, s[48:51], s21 offen offset:0\n"
"buffer_load_ushort v231, v244, s[48:51], s21 offen offset:256\n"
"buffer_load_ushort v232, v244, s[48:51], s21 offen offset:512\n"
"buffer_load_ushort v233, v244, s[48:51], s21 offen offset:768\n"
"s_waitcnt vmcnt(0)\n"
"v_lshl_or_b32 v234, v227, 16, v226\n"
"v_lshl_or_b32 v235, v229, 16, v228\n"
"v_lshl_or_b32 v236, v231, 16, v230\n"
"v_lshl_or_b32 v237, v233, 16, v232\n"
"s_nop 1\n"
"v_mfma_f32_16x16x32_bf16 v[0:3], v[234:237], v[168:171], v[0:3]\n"
"v_mfma_f32_16x16x32_bf16 v[4:7], v[234:237], v[172:175], v[4:7]\n"
"v_mfma_f32_16x16x32_bf16 v[8:11], v[234:237], v[176:179], v[8:11]\n"
"buffer_load_ushort v226, v243, s[48:51], s21 offen offset:32\n"
"buffer_load_ushort v227, v243, s[48:51], s21 offen offset:288\n"
"buffer_load_ushort v228, v243, s[48:51], s21 offen offset:544\n"
"buffer_load_ushort v229, v243, s[48:51], s21 offen offset:800\n"
"buffer_load_ushort v230, v244, s[48:51], s21 offen offset:32\n"
"buffer_load_ushort v231, v244, s[48:51], s21 offen offset:288\n"
"buffer_load_ushort v232, v244, s[48:51], s21 offen offset:544\n"
"buffer_load_ushort v233, v244, s[48:51], s21 offen offset:800\n"
"s_waitcnt vmcnt(0)\n"
"v_lshl_or_b32 v238, v227, 16, v226\n"
"v_lshl_or_b32 v239, v229, 16, v228\n"
"v_lshl_or_b32 v240, v231, 16, v230\n"
"v_lshl_or_b32 v241, v233, 16, v232\n"
"s_nop 1\n"
"v_mfma_f32_16x16x32_bf16 v[12:15], v[238:241], v[168:171], v[12:15]\n"
"v_mfma_f32_16x16x32_bf16 v[16:19], v[238:241], v[172:175], v[16:19]\n"
"v_mfma_f32_16x16x32_bf16 v[20:23], v[238:241], v[176:179], v[20:23]\n"
"buffer_load_ushort v226, v243, s[48:51], s21 offen offset:64\n"
"buffer_load_ushort v227, v243, s[48:51], s21 offen offset:320\n"
"buffer_load_ushort v228, v243, s[48:51], s21 offen offset:576\n"
"buffer_load_ushort v229, v243, s[48:51], s21 offen offset:832\n"
"buffer_load_ushort v230, v244, s[48:51], s21 offen offset:64\n"
"buffer_load_ushort v231, v244, s[48:51], s21 offen offset:320\n"
"buffer_load_ushort v232, v244, s[48:51], s21 offen offset:576\n"
"buffer_load_ushort v233, v244, s[48:51], s21 offen offset:832\n"
"s_waitcnt vmcnt(0)\n"
"v_lshl_or_b32 v234, v227, 16, v226\n"
"v_lshl_or_b32 v235, v229, 16, v228\n"
"v_lshl_or_b32 v236, v231, 16, v230\n"
"v_lshl_or_b32 v237, v233, 16, v232\n"
"s_nop 1\n"
"v_mfma_f32_16x16x32_bf16 v[24:27], v[234:237], v[168:171], v[24:27]\n"
"v_mfma_f32_16x16x32_bf16 v[28:31], v[234:237], v[172:175], v[28:31]\n"
"v_mfma_f32_16x16x32_bf16 v[32:35], v[234:237], v[176:179], v[32:35]\n"
"buffer_load_ushort v226, v243, s[48:51], s21 offen offset:96\n"
"buffer_load_ushort v227, v243, s[48:51], s21 offen offset:352\n"
"buffer_load_ushort v228, v243, s[48:51], s21 offen offset:608\n"
"buffer_load_ushort v229, v243, s[48:51], s21 offen offset:864\n"
"buffer_load_ushort v230, v244, s[48:51], s21 offen offset:96\n"
"buffer_load_ushort v231, v244, s[48:51], s21 offen offset:352\n"
"buffer_load_ushort v232, v244, s[48:51], s21 offen offset:608\n"
"buffer_load_ushort v233, v244, s[48:51], s21 offen offset:864\n"
"s_waitcnt vmcnt(0)\n"
"v_lshl_or_b32 v238, v227, 16, v226\n"
"v_lshl_or_b32 v239, v229, 16, v228\n"
"v_lshl_or_b32 v240, v231, 16, v230\n"
"v_lshl_or_b32 v241, v233, 16, v232\n"
"s_nop 1\n"
"v_mfma_f32_16x16x32_bf16 v[36:39], v[238:241], v[168:171], v[36:39]\n"
"v_mfma_f32_16x16x32_bf16 v[40:43], v[238:241], v[172:175], v[40:43]\n"
"v_mfma_f32_16x16x32_bf16 v[44:47], v[238:241], v[176:179], v[44:47]\n"
"buffer_load_ushort v226, v243, s[48:51], s21 offen offset:128\n"
"buffer_load_ushort v227, v243, s[48:51], s21 offen offset:384\n"
"buffer_load_ushort v228, v243, s[48:51], s21 offen offset:640\n"
"buffer_load_ushort v229, v243, s[48:51], s21 offen offset:896\n"
"buffer_load_ushort v230, v244, s[48:51], s21 offen offset:128\n"
"buffer_load_ushort v231, v244, s[48:51], s21 offen offset:384\n"
"buffer_load_ushort v232, v244, s[48:51], s21 offen offset:640\n"
"buffer_load_ushort v233, v244, s[48:51], s21 offen offset:896\n"
"s_waitcnt vmcnt(0)\n"
"v_lshl_or_b32 v234, v227, 16, v226\n"
"v_lshl_or_b32 v235, v229, 16, v228\n"
"v_lshl_or_b32 v236, v231, 16, v230\n"
"v_lshl_or_b32 v237, v233, 16, v232\n"
"s_nop 1\n"
"v_mfma_f32_16x16x32_bf16 v[48:51], v[234:237], v[168:171], v[48:51]\n"
"v_mfma_f32_16x16x32_bf16 v[52:55], v[234:237], v[172:175], v[52:55]\n"
"v_mfma_f32_16x16x32_bf16 v[56:59], v[234:237], v[176:179], v[56:59]\n"
"buffer_load_ushort v226, v243, s[48:51], s21 offen offset:160\n"
"buffer_load_ushort v227, v243, s[48:51], s21 offen offset:416\n"
"buffer_load_ushort v228, v243, s[48:51], s21 offen offset:672\n"
"buffer_load_ushort v229, v243, s[48:51], s21 offen offset:928\n"
"buffer_load_ushort v230, v244, s[48:51], s21 offen offset:160\n"
"buffer_load_ushort v231, v244, s[48:51], s21 offen offset:416\n"
"buffer_load_ushort v232, v244, s[48:51], s21 offen offset:672\n"
"buffer_load_ushort v233, v244, s[48:51], s21 offen offset:928\n"
"s_waitcnt vmcnt(0)\n"
"v_lshl_or_b32 v238, v227, 16, v226\n"
"v_lshl_or_b32 v239, v229, 16, v228\n"
"v_lshl_or_b32 v240, v231, 16, v230\n"
"v_lshl_or_b32 v241, v233, 16, v232\n"
"s_nop 1\n"
"v_mfma_f32_16x16x32_bf16 v[60:63], v[238:241], v[168:171], v[60:63]\n"
"v_mfma_f32_16x16x32_bf16 v[64:67], v[238:241], v[172:175], v[64:67]\n"
"v_mfma_f32_16x16x32_bf16 v[68:71], v[238:241], v[176:179], v[68:71]\n"
"buffer_load_ushort v226, v243, s[48:51], s21 offen offset:192\n"
"buffer_load_ushort v227, v243, s[48:51], s21 offen offset:448\n"
"buffer_load_ushort v228, v243, s[48:51], s21 offen offset:704\n"
"buffer_load_ushort v229, v243, s[48:51], s21 offen offset:960\n"
"buffer_load_ushort v230, v244, s[48:51], s21 offen offset:192\n"
"buffer_load_ushort v231, v244, s[48:51], s21 offen offset:448\n"
"buffer_load_ushort v232, v244, s[48:51], s21 offen offset:704\n"
"buffer_load_ushort v233, v244, s[48:51], s21 offen offset:960\n"
"s_waitcnt vmcnt(0)\n"
"v_lshl_or_b32 v234, v227, 16, v226\n"
"v_lshl_or_b32 v235, v229, 16, v228\n"
"v_lshl_or_b32 v236, v231, 16, v230\n"
"v_lshl_or_b32 v237, v233, 16, v232\n"
"s_nop 1\n"
"v_mfma_f32_16x16x32_bf16 v[72:75], v[234:237], v[168:171], v[72:75]\n"
"v_mfma_f32_16x16x32_bf16 v[76:79], v[234:237], v[172:175], v[76:79]\n"
"v_mfma_f32_16x16x32_bf16 v[80:83], v[234:237], v[176:179], v[80:83]\n"
"buffer_load_ushort v226, v243, s[48:51], s21 offen offset:224\n"
"buffer_load_ushort v227, v243, s[48:51], s21 offen offset:480\n"
"buffer_load_ushort v228, v243, s[48:51], s21 offen offset:736\n"
"buffer_load_ushort v229, v243, s[48:51], s21 offen offset:992\n"
"buffer_load_ushort v230, v244, s[48:51], s21 offen offset:224\n"
"buffer_load_ushort v231, v244, s[48:51], s21 offen offset:480\n"
"buffer_load_ushort v232, v244, s[48:51], s21 offen offset:736\n"
"buffer_load_ushort v233, v244, s[48:51], s21 offen offset:992\n"
"s_waitcnt vmcnt(0)\n"
"v_lshl_or_b32 v238, v227, 16, v226\n"
"v_lshl_or_b32 v239, v229, 16, v228\n"
"v_lshl_or_b32 v240, v231, 16, v230\n"
"v_lshl_or_b32 v241, v233, 16, v232\n"
"s_nop 1\n"
"v_mfma_f32_16x16x32_bf16 v[84:87], v[238:241], v[168:171], v[84:87]\n"
"v_mfma_f32_16x16x32_bf16 v[88:91], v[238:241], v[172:175], v[88:91]\n"
"v_mfma_f32_16x16x32_bf16 v[92:95], v[238:241], v[176:179], v[92:95]\n"
"s_nop 7\n"
"s_nop 7\n"
"s_add_u32 s21, s21, 0x2000\n"
"s_cmp_lt_u32 s21, 0x20000\n"
"s_cbranch_scc1 Lp20_%=\n"
"s_branch Lback0_%=\n"
"LspinA_%=:\n"
"s_mov_b32 s21, 0\n"
"LspA_%=:\n"
"buffer_load_dwordx2 v[232:233], v225, s[68:71], s92 offen sc1\n"
"s_waitcnt vmcnt(0)\n"
"v_cmp_ne_u64 vcc, s[90:91], v[232:233]\n"
"s_nop 4\n"
"s_cbranch_vccz LokA_%=\n"
"s_sleep 2\n"
"s_add_u32 s21, s21, 1\n"
"s_cmp_lt_u32 s21, 0x20000\n"
"s_cbranch_scc1 LspA_%=\n"
"LokA_%=:\n"
"buffer_inv sc1\n"
"s_branch LretA_%=\n"
"Lslow1_%=:\n"
"s_nop 7\n"
"s_nop 7\n"
"v_lshrrev_b32 v250, 4, v223\n"
"v_and_b32 v243, 15, v250\n"
"v_lshrrev_b32 v244, 4, v250\n"
"v_lshlrev_b32 v242, 8, v243\n"
"v_lshl_add_u32 v242, v244, 4, v242\n"
"v_lshlrev_b32 v243, 1, v243\n"
"v_lshl_add_u32 v243, v244, 10, v243\n"
"v_add_u32 v244, 0x1000, v243\n"
"v_mov_b32 v245, 0xff800000\n"
"v_mov_b32 v246, 0xff800000\n"
"v_mov_b32 v247, 0xff800000\n"
"s_mov_b32 s21, 0x200000\n"
"Lp11_%=:\n"
"s_add_u32 s22, s21, 0\n"
"buffer_load_dwordx4 v[226:229], v242, s[44:47], s22 offen offset:0\n"
"buffer_load_dwordx4 v[230:233], v242, s[44:47], s22 offen offset:64\n"
"buffer_load_dwordx4 v[234:237], v242, s[44:47], s22 offen offset:128\n"
"buffer_load_dwordx4 v[238:241], v242, s[44:47], s22 offen offset:192\n"
"s_waitcnt vmcnt(0)\n"
"v_mfma_f32_16x16x32_bf16 v[48:51], v[226:229], v[0:3], 0\n"
"v_mfma_f32_16x16x32_bf16 v[48:51], v[230:233], v[4:7], v[48:51]\n"
"v_mfma_f32_16x16x32_bf16 v[48:51], v[234:237], v[8:11], v[48:51]\n"
"v_mfma_f32_16x16x32_bf16 v[48:51], v[238:241], v[12:15], v[48:51]\n"
"v_mfma_f32_16x16x32_bf16 v[56:59], v[226:229], v[16:19], 0\n"
"v_mfma_f32_16x16x32_bf16 v[56:59], v[230:233], v[20:23], v[56:59]\n"
"v_mfma_f32_16x16x32_bf16 v[56:59], v[234:237], v[24:27], v[56:59]\n"
"v_mfma_f32_16x16x32_bf16 v[56:59], v[238:241], v[28:31], v[56:59]\n"
"v_mfma_f32_16x16x32_bf16 v[64:67], v[226:229], v[32:35], 0\n"
"v_mfma_f32_16x16x32_bf16 v[64:67], v[230:233], v[36:39], v[64:67]\n"
"v_mfma_f32_16x16x32_bf16 v[64:67], v[234:237], v[40:43], v[64:67]\n"
"v_mfma_f32_16x16x32_bf16 v[64:67], v[238:241], v[44:47], v[64:67]\n"
"s_nop 7\n"
"s_nop 7\n"
"s_add_u32 s22, s21, 4096\n"
"buffer_load_dwordx4 v[226:229], v242, s[44:47], s22 offen offset:0\n"
"buffer_load_dwordx4 v[230:233], v242, s[44:47], s22 offen offset:64\n"
"buffer_load_dwordx4 v[234:237], v242, s[44:47], s22 offen offset:128\n"
"buffer_load_dwordx4 v[238:241], v242, s[44:47], s22 offen offset:192\n"
"s_waitcnt vmcnt(0)\n"
"v_mfma_f32_16x16x32_bf16 v[52:55], v[226:229], v[0:3], 0\n"
"v_mfma_f32_16x16x32_bf16 v[52:55], v[230:233], v[4:7], v[52:55]\n"
"v_mfma_f32_16x16x32_bf16 v[52:55], v[234:237], v[8:11], v[52:55]\n"
"v_mfma_f32_16x16x32_bf16 v[52:55], v[238:241], v[12:15], v[52:55]\n"
"v_mfma_f32_16x16x32_bf16 v[60:63], v[226:229], v[16:19], 0\n"
"v_mfma_f32_16x16x32_bf16 v[60:63], v[230:233], v[20:23], v[60:63]\n"
"v_mfma_f32_16x16x32_bf16 v[60:63], v[234:237], v[24:27], v[60:63]\n"
"v_mfma_f32_16x16x32_bf16 v[60:63], v[238:241], v[28:31], v[60:63]\n"
"v_mfma_f32_16x16x32_bf16 v[68:71], v[226:229], v[32:35], 0\n"
"v_mfma_f32_16x16x32_bf16 v[68:71], v[230:233], v[36:39], v[68:71]\n"
"v_mfma_f32_16x16x32_bf16 v[68:71], v[234:237], v[40:43], v[68:71]\n"
"v_mfma_f32_16x16x32_bf16 v[68:71], v[238:241], v[44:47], v[68:71]\n"
"s_nop 7\n"
"s_nop 7\n"
"v_max3_f32 v245, v245, v48, v49\n"
"v_max3_f32 v245, v245, v50, v51\n"
"v_max3_f32 v245, v245, v52, v53\n"
"v_max3_f32 v245, v245, v54, v55\n"
"v_max3_f32 v246, v246, v56, v57\n"
"v_max3_f32 v246, v246, v58, v59\n"
"v_max3_f32 v246, v246, v60, v61\n"
"v_max3_f32 v246, v246, v62, v63\n"
"v_max3_f32 v247, v247, v64, v65\n"
"v_max3_f32 v247, v247, v66, v67\n"
"v_max3_f32 v247, v247, v68, v69\n"
"v_max3_f32 v247, v247, v70, v71\n"
"s_add_u32 s21, s21, 0x2000\n"
"s_cmp_lt_u32 s21, 0x220000\n"
"s_cbranch_scc1 Lp11_%=\n"
"v_mov_b32 v248, v245\n"
"s_nop 1\n"
"v_permlane32_swap_b32 v245, v248\n"
"v_max_f32 v245, v245, v248\n"
"v_mov_b32 v248, v245\n"
"s_nop 1\n"
"v_permlane16_swap_b32 v245, v248\n"
"v_min_f32_e64 v84, -v245, -v248\n"
"v_mov_b32 v85, v84\n"
"v_mov_b32 v86, v84\n"
"v_mov_b32 v87, v84\n"
"v_mov_b32 v248, v246\n"
"s_nop 1\n"
"v_permlane32_swap_b32 v246, v248\n"
"v_max_f32 v246, v246, v248\n"
"v_mov_b32 v248, v246\n"
"s_nop 1\n"
"v_permlane16_swap_b32 v246, v248\n"
"v_min_f32_e64 v88, -v246, -v248\n"
"v_mov_b32 v89, v88\n"
"v_mov_b32 v90, v88\n"
"v_mov_b32 v91, v88\n"
"v_mov_b32 v248, v247\n"
"s_nop 1\n"
"v_permlane32_swap_b32 v247, v248\n"
"v_max_f32 v247, v247, v248\n"
"v_mov_b32 v248, v247\n"
"s_nop 1\n"
"v_permlane16_swap_b32 v247, v248\n"
"v_min_f32_e64 v92, -v247, -v248\n"
"v_mov_b32 v93, v92\n"
"v_mov_b32 v94, v92\n"
"v_mov_b32 v95, v92\n"
"v_mov_b32 v96, 0\n"
"v_mov_b32 v97, 0\n"
"v_mov_b32 v98, 0\n"
"v_mov_b32 v99, 0\n"
"v_mov_b32 v100, 0\n"
"v_mov_b32 v101, 0\n"
"v_mov_b32 v102, 0\n"
"v_mov_b32 v103, 0\n"
"v_mov_b32 v104, 0\n"
"v_mov_b32 v105, 0\n"
"v_mov_b32 v106, 0\n"
"v_mov_b32 v107, 0\n"
"v_mov_b32 v108, 0\n"
"v_mov_b32 v109, 0\n"
"v_mov_b32 v110, 0\n"
"v_mov_b32 v111, 0\n"
"v_mov_b32 v112, 0\n"
"v_mov_b32 v113, 0\n"
"v_mov_b32 v114, 0\n"
"v_mov_b32 v115, 0\n"
"v_mov_b32 v116, 0\n"
"v_mov_b32 v117, 0\n"
"v_mov_b32 v118, 0\n"
"v_mov_b32 v119, 0\n"
"v_mov_b32 v120, 0\n"
"v_mov_b32 v121, 0\n"
"v_mov_b32 v122, 0\n"
"v_mov_b32 v123, 0\n"
"v_mov_b32 v124, 0\n"
"v_mov_b32 v125, 0\n"
"v_mov_b32 v126, 0\n"
"v_mov_b32 v127, 0\n"
"v_mov_b32 v128, 0\n"
"v_mov_b32 v129, 0\n"
"v_mov_b32 v130, 0\n"
"v_mov_b32 v131, 0\n"
"v_mov_b32 v132, 0\n"
"v_mov_b32 v133, 0\n"
"v_mov_b32 v134, 0\n"
"v_mov_b32 v135, 0\n"
"v_mov_b32 v136, 0\n"
"v_mov_b32 v137, 0\n"
"v_mov_b32 v138, 0\n"
"v_mov_b32 v139, 0\n"
"v_mov_b32 v140, 0\n"
"v_mov_b32 v141, 0\n"
"v_mov_b32 v142, 0\n"
"v_mov_b32 v143, 0\n"
"v_mov_b32 v144, 0\n"
"v_mov_b32 v145, 0\n"
"v_mov_b32 v146, 0\n"
"v_mov_b32 v147, 0\n"
"v_mov_b32 v148, 0\n"
"v_mov_b32 v149, 0\n"
"v_mov_b32 v150, 0\n"
"v_mov_b32 v151, 0\n"
"v_mov_b32 v152, 0\n"
"v_mov_b32 v153, 0\n"
"v_mov_b32 v154, 0\n"
"v_mov_b32 v155, 0\n"
"v_mov_b32 v156, 0\n"
"v_mov_b32 v157, 0\n"
"v_mov_b32 v158, 0\n"
"v_mov_b32 v159, 0\n"
"v_mov_b32 v160, 0\n"
"v_mov_b32 v161, 0\n"
"v_mov_b32 v162, 0\n"
"v_mov_b32 v163, 0\n"
"v_mov_b32 v164, 0\n"
"v_mov_b32 v165, 0\n"
"v_mov_b32 v166, 0\n"
"v_mov_b32 v167, 0\n"
"v_mov_b32 v168, 0\n"
"v_mov_b32 v169, 0\n"
"v_mov_b32 v170, 0\n"
"v_mov_b32 v171, 0\n"
"v_mov_b32 v172, 0\n"
"v_mov_b32 v173, 0\n"
"v_mov_b32 v174, 0\n"
"v_mov_b32 v175, 0\n"
"v_mov_b32 v176, 0\n"
"v_mov_b32 v177, 0\n"
"v_mov_b32 v178, 0\n"
"v_mov_b32 v179, 0\n"
"v_mov_b32 v180, 0\n"
"v_mov_b32 v181, 0\n"
"v_mov_b32 v182, 0\n"
"v_mov_b32 v183, 0\n"
"v_mov_b32 v184, 0\n"
"v_mov_b32 v185, 0\n"
"v_mov_b32 v186, 0\n"
"v_mov_b32 v187, 0\n"
"v_mov_b32 v188, 0\n"
"v_mov_b32 v189, 0\n"
"v_mov_b32 v190, 0\n"
"v_mov_b32 v191, 0\n"
"v_mov_b32 v192, 0\n"
"v_mov_b32 v193, 0\n"
"v_mov_b32 v194, 0\n"
"v_mov_b32 v195, 0\n"
"v_mov_b32 v196, 0\n"
"v_mov_b32 v197, 0\n"
"v_mov_b32 v198, 0\n"
"v_mov_b32 v199, 0\n"
"v_mov_b32 v200, 0\n"
"v_mov_b32 v201, 0\n"
"v_mov_b32 v202, 0\n"
"v_mov_b32 v203, 0\n"
"s_mov_b32 s21, 0x200000\n"
"Lp21_%=:\n"
"s_add_u32 s22, s21, 0\n"
"buffer_load_dwordx4 v[226:229], v242, s[44:47], s22 offen offset:0\n"
"buffer_load_dwordx4 v[230:233], v242, s[44:47], s22 offen offset:64\n"
"buffer_load_dwordx4 v[234:237], v242, s[44:47], s22 offen offset:128\n"
"buffer_load_dwordx4 v[238:241], v242, s[44:47], s22 offen offset:192\n"
"s_waitcnt vmcnt(0)\n"
"v_mfma_f32_16x16x32_bf16 v[48:51], v[226:229], v[0:3], v[84:87]\n"
"v_mfma_f32_16x16x32_bf16 v[48:51], v[230:233], v[4:7], v[48:51]\n"
"v_mfma_f32_16x16x32_bf16 v[48:51], v[234:237], v[8:11], v[48:51]\n"
"v_mfma_f32_16x16x32_bf16 v[48:51], v[238:241], v[12:15], v[48:51]\n"
"v_mfma_f32_16x16x32_bf16 v[56:59], v[226:229], v[16:19], v[88:91]\n"
"v_mfma_f32_16x16x32_bf16 v[56:59], v[230:233], v[20:23], v[56:59]\n"
"v_mfma_f32_16x16x32_bf16 v[56:59], v[234:237], v[24:27], v[56:59]\n"
"v_mfma_f32_16x16x32_bf16 v[56:59], v[238:241], v[28:31], v[56:59]\n"
"v_mfma_f32_16x16x32_bf16 v[64:67], v[226:229], v[32:35], v[92:95]\n"
"v_mfma_f32_16x16x32_bf16 v[64:67], v[230:233], v[36:39], v[64:67]\n"
"v_mfma_f32_16x16x32_bf16 v[64:67], v[234:237], v[40:43], v[64:67]\n"
"v_mfma_f32_16x16x32_bf16 v[64:67], v[238:241], v[44:47], v[64:67]\n"
"s_nop 7\n"
"s_nop 7\n"
"s_add_u32 s22, s21, 4096\n"
"buffer_load_dwordx4 v[226:229], v242, s[44:47], s22 offen offset:0\n"
"buffer_load_dwordx4 v[230:233], v242, s[44:47], s22 offen offset:64\n"
"buffer_load_dwordx4 v[234:237], v242, s[44:47], s22 offen offset:128\n"
"buffer_load_dwordx4 v[238:241], v242, s[44:47], s22 offen offset:192\n"
"s_waitcnt vmcnt(0)\n"
"v_mfma_f32_16x16x32_bf16 v[52:55], v[226:229], v[0:3], v[84:87]\n"
"v_mfma_f32_16x16x32_bf16 v[52:55], v[230:233], v[4:7], v[52:55]\n"
"v_mfma_f32_16x16x32_bf16 v[52:55], v[234:237], v[8:11], v[52:55]\n"
"v_mfma_f32_16x16x32_bf16 v[52:55], v[238:241], v[12:15], v[52:55]\n"
"v_mfma_f32_16x16x32_bf16 v[60:63], v[226:229], v[16:19], v[88:91]\n"
"v_mfma_f32_16x16x32_bf16 v[60:63], v[230:233], v[20:23], v[60:63]\n"
"v_mfma_f32_16x16x32_bf16 v[60:63], v[234:237], v[24:27], v[60:63]\n"
"v_mfma_f32_16x16x32_bf16 v[60:63], v[238:241], v[28:31], v[60:63]\n"
"v_mfma_f32_16x16x32_bf16 v[68:71], v[226:229], v[32:35], v[92:95]\n"
"v_mfma_f32_16x16x32_bf16 v[68:71], v[230:233], v[36:39], v[68:71]\n"
"v_mfma_f32_16x16x32_bf16 v[68:71], v[234:237], v[40:43], v[68:71]\n"
"v_mfma_f32_16x16x32_bf16 v[68:71], v[238:241], v[44:47], v[68:71]\n"
"s_nop 7\n"
"s_nop 7\n"
"v_exp_f32 v48, v48\n"
"v_exp_f32 v49, v49\n"
"v_exp_f32 v50, v50\n"
"v_exp_f32 v51, v51\n"
"v_exp_f32 v52, v52\n"
"v_exp_f32 v53, v53\n"
"v_exp_f32 v54, v54\n"
"v_exp_f32 v55, v55\n"
"v_exp_f32 v56, v56\n"
"v_exp_f32 v57, v57\n"
"v_exp_f32 v58, v58\n"
"v_exp_f32 v59, v59\n"
"v_exp_f32 v60, v60\n"
"v_exp_f32 v61, v61\n"
"v_exp_f32 v62, v62\n"
"v_exp_f32 v63, v63\n"
"v_exp_f32 v64, v64\n"
"v_exp_f32 v65, v65\n"
"v_exp_f32 v66, v66\n"
"v_exp_f32 v67, v67\n"
"v_exp_f32 v68, v68\n"
"v_exp_f32 v69, v69\n"
"v_exp_f32 v70, v70\n"
"v_exp_f32 v71, v71\n"
"s_nop 1\n"
"v_cvt_pk_bf16_f32 v72, v48, v49\n"
"v_cvt_pk_bf16_f32 v73, v50, v51\n"
"v_cvt_pk_bf16_f32 v74, v52, v53\n"
"v_cvt_pk_bf16_f32 v75, v54, v55\n"
"v_cvt_pk_bf16_f32 v76, v56, v57\n"
"v_cvt_pk_bf16_f32 v77, v58, v59\n"
"v_cvt_pk_bf16_f32 v78, v60, v61\n"
"v_cvt_pk_bf16_f32 v79, v62, v63\n"
"v_cvt_pk_bf16_f32 v80, v64, v65\n"
"v_cvt_pk_bf16_f32 v81, v66, v67\n"
"v_cvt_pk_bf16_f32 v82, v68, v69\n"
"v_cvt_pk_bf16_f32 v83, v70, v71\n"
"s_nop 2\n"
"v_mfma_f32_16x16x32_bf16 v[192:195], v[204:207], v[72:75], v[192:195]\n"
"v_mfma_f32_16x16x32_bf16 v[196:199], v[204:207], v[76:79], v[196:199]\n"
"v_mfma_f32_16x16x32_bf16 v[200:203], v[204:207], v[80:83], v[200:203]\n"
"buffer_load_ushort v226, v243, s[48:51], s21 offen offset:0\n"
"buffer_load_ushort v227, v243, s[48:51], s21 offen offset:256\n"
"buffer_load_ushort v228, v243, s[48:51], s21 offen offset:512\n"
"buffer_load_ushort v229, v243, s[48:51], s21 offen offset:768\n"
"buffer_load_ushort v230, v244, s[48:51], s21 offen offset:0\n"
"buffer_load_ushort v231, v244, s[48:51], s21 offen offset:256\n"
"buffer_load_ushort v232, v244, s[48:51], s21 offen offset:512\n"
"buffer_load_ushort v233, v244, s[48:51], s21 offen offset:768\n"
"s_waitcnt vmcnt(0)\n"
"v_lshl_or_b32 v234, v227, 16, v226\n"
"v_lshl_or_b32 v235, v229, 16, v228\n"
"v_lshl_or_b32 v236, v231, 16, v230\n"
"v_lshl_or_b32 v237, v233, 16, v232\n"
"s_nop 1\n"
"v_mfma_f32_16x16x32_bf16 v[96:99], v[234:237], v[72:75], v[96:99]\n"
"v_mfma_f32_16x16x32_bf16 v[100:103], v[234:237], v[76:79], v[100:103]\n"
"v_mfma_f32_16x16x32_bf16 v[104:107], v[234:237], v[80:83], v[104:107]\n"
"buffer_load_ushort v226, v243, s[48:51], s21 offen offset:32\n"
"buffer_load_ushort v227, v243, s[48:51], s21 offen offset:288\n"
"buffer_load_ushort v228, v243, s[48:51], s21 offen offset:544\n"
"buffer_load_ushort v229, v243, s[48:51], s21 offen offset:800\n"
"buffer_load_ushort v230, v244, s[48:51], s21 offen offset:32\n"
"buffer_load_ushort v231, v244, s[48:51], s21 offen offset:288\n"
"buffer_load_ushort v232, v244, s[48:51], s21 offen offset:544\n"
"buffer_load_ushort v233, v244, s[48:51], s21 offen offset:800\n"
"s_waitcnt vmcnt(0)\n"
"v_lshl_or_b32 v238, v227, 16, v226\n"
"v_lshl_or_b32 v239, v229, 16, v228\n"
"v_lshl_or_b32 v240, v231, 16, v230\n"
"v_lshl_or_b32 v241, v233, 16, v232\n"
"s_nop 1\n"
"v_mfma_f32_16x16x32_bf16 v[108:111], v[238:241], v[72:75], v[108:111]\n"
"v_mfma_f32_16x16x32_bf16 v[112:115], v[238:241], v[76:79], v[112:115]\n"
"v_mfma_f32_16x16x32_bf16 v[116:119], v[238:241], v[80:83], v[116:119]\n"
"buffer_load_ushort v226, v243, s[48:51], s21 offen offset:64\n"
"buffer_load_ushort v227, v243, s[48:51], s21 offen offset:320\n"
"buffer_load_ushort v228, v243, s[48:51], s21 offen offset:576\n"
"buffer_load_ushort v229, v243, s[48:51], s21 offen offset:832\n"
"buffer_load_ushort v230, v244, s[48:51], s21 offen offset:64\n"
"buffer_load_ushort v231, v244, s[48:51], s21 offen offset:320\n"
"buffer_load_ushort v232, v244, s[48:51], s21 offen offset:576\n"
"buffer_load_ushort v233, v244, s[48:51], s21 offen offset:832\n"
"s_waitcnt vmcnt(0)\n"
"v_lshl_or_b32 v234, v227, 16, v226\n"
"v_lshl_or_b32 v235, v229, 16, v228\n"
"v_lshl_or_b32 v236, v231, 16, v230\n"
"v_lshl_or_b32 v237, v233, 16, v232\n"
"s_nop 1\n"
"v_mfma_f32_16x16x32_bf16 v[120:123], v[234:237], v[72:75], v[120:123]\n"
"v_mfma_f32_16x16x32_bf16 v[124:127], v[234:237], v[76:79], v[124:127]\n"
"v_mfma_f32_16x16x32_bf16 v[128:131], v[234:237], v[80:83], v[128:131]\n"
"buffer_load_ushort v226, v243, s[48:51], s21 offen offset:96\n"
"buffer_load_ushort v227, v243, s[48:51], s21 offen offset:352\n"
"buffer_load_ushort v228, v243, s[48:51], s21 offen offset:608\n"
"buffer_load_ushort v229, v243, s[48:51], s21 offen offset:864\n"
"buffer_load_ushort v230, v244, s[48:51], s21 offen offset:96\n"
"buffer_load_ushort v231, v244, s[48:51], s21 offen offset:352\n"
"buffer_load_ushort v232, v244, s[48:51], s21 offen offset:608\n"
"buffer_load_ushort v233, v244, s[48:51], s21 offen offset:864\n"
"s_waitcnt vmcnt(0)\n"
"v_lshl_or_b32 v238, v227, 16, v226\n"
"v_lshl_or_b32 v239, v229, 16, v228\n"
"v_lshl_or_b32 v240, v231, 16, v230\n"
"v_lshl_or_b32 v241, v233, 16, v232\n"
"s_nop 1\n"
"v_mfma_f32_16x16x32_bf16 v[132:135], v[238:241], v[72:75], v[132:135]\n"
"v_mfma_f32_16x16x32_bf16 v[136:139], v[238:241], v[76:79], v[136:139]\n"
"v_mfma_f32_16x16x32_bf16 v[140:143], v[238:241], v[80:83], v[140:143]\n"
"buffer_load_ushort v226, v243, s[48:51], s21 offen offset:128\n"
"buffer_load_ushort v227, v243, s[48:51], s21 offen offset:384\n"
"buffer_load_ushort v228, v243, s[48:51], s21 offen offset:640\n"
"buffer_load_ushort v229, v243, s[48:51], s21 offen offset:896\n"
"buffer_load_ushort v230, v244, s[48:51], s21 offen offset:128\n"
"buffer_load_ushort v231, v244, s[48:51], s21 offen offset:384\n"
"buffer_load_ushort v232, v244, s[48:51], s21 offen offset:640\n"
"buffer_load_ushort v233, v244, s[48:51], s21 offen offset:896\n"
"s_waitcnt vmcnt(0)\n"
"v_lshl_or_b32 v234, v227, 16, v226\n"
"v_lshl_or_b32 v235, v229, 16, v228\n"
"v_lshl_or_b32 v236, v231, 16, v230\n"
"v_lshl_or_b32 v237, v233, 16, v232\n"
"s_nop 1\n"
"v_mfma_f32_16x16x32_bf16 v[144:147], v[234:237], v[72:75], v[144:147]\n"
"v_mfma_f32_16x16x32_bf16 v[148:151], v[234:237], v[76:79], v[148:151]\n"
"v_mfma_f32_16x16x32_bf16 v[152:155], v[234:237], v[80:83], v[152:155]\n"
"buffer_load_ushort v226, v243, s[48:51], s21 offen offset:160\n"
"buffer_load_ushort v227, v243, s[48:51], s21 offen offset:416\n"
"buffer_load_ushort v228, v243, s[48:51], s21 offen offset:672\n"
"buffer_load_ushort v229, v243, s[48:51], s21 offen offset:928\n"
"buffer_load_ushort v230, v244, s[48:51], s21 offen offset:160\n"
"buffer_load_ushort v231, v244, s[48:51], s21 offen offset:416\n"
"buffer_load_ushort v232, v244, s[48:51], s21 offen offset:672\n"
"buffer_load_ushort v233, v244, s[48:51], s21 offen offset:928\n"
"s_waitcnt vmcnt(0)\n"
"v_lshl_or_b32 v238, v227, 16, v226\n"
"v_lshl_or_b32 v239, v229, 16, v228\n"
"v_lshl_or_b32 v240, v231, 16, v230\n"
"v_lshl_or_b32 v241, v233, 16, v232\n"
"s_nop 1\n"
"v_mfma_f32_16x16x32_bf16 v[156:159], v[238:241], v[72:75], v[156:159]\n"
"v_mfma_f32_16x16x32_bf16 v[160:163], v[238:241], v[76:79], v[160:163]\n"
"v_mfma_f32_16x16x32_bf16 v[164:167], v[238:241], v[80:83], v[164:167]\n"
"buffer_load_ushort v226, v243, s[48:51], s21 offen offset:192\n"
"buffer_load_ushort v227, v243, s[48:51], s21 offen offset:448\n"
"buffer_load_ushort v228, v243, s[48:51], s21 offen offset:704\n"
"buffer_load_ushort v229, v243, s[48:51], s21 offen offset:960\n"
"buffer_load_ushort v230, v244, s[48:51], s21 offen offset:192\n"
"buffer_load_ushort v231, v244, s[48:51], s21 offen offset:448\n"
"buffer_load_ushort v232, v244, s[48:51], s21 offen offset:704\n"
"buffer_load_ushort v233, v244, s[48:51], s21 offen offset:960\n"
"s_waitcnt vmcnt(0)\n"
"v_lshl_or_b32 v234, v227, 16, v226\n"
"v_lshl_or_b32 v235, v229, 16, v228\n"
"v_lshl_or_b32 v236, v231, 16, v230\n"
"v_lshl_or_b32 v237, v233, 16, v232\n"
"s_nop 1\n"
"v_mfma_f32_16x16x32_bf16 v[168:171], v[234:237], v[72:75], v[168:171]\n"
"v_mfma_f32_16x16x32_bf16 v[172:175], v[234:237], v[76:79], v[172:175]\n"
"v_mfma_f32_16x16x32_bf16 v[176:179], v[234:237], v[80:83], v[176:179]\n"
"buffer_load_ushort v226, v243, s[48:51], s21 offen offset:224\n"
"buffer_load_ushort v227, v243, s[48:51], s21 offen offset:480\n"
"buffer_load_ushort v228, v243, s[48:51], s21 offen offset:736\n"
"buffer_load_ushort v229, v243, s[48:51], s21 offen offset:992\n"
"buffer_load_ushort v230, v244, s[48:51], s21 offen offset:224\n"
"buffer_load_ushort v231, v244, s[48:51], s21 offen offset:480\n"
"buffer_load_ushort v232, v244, s[48:51], s21 offen offset:736\n"
"buffer_load_ushort v233, v244, s[48:51], s21 offen offset:992\n"
"s_waitcnt vmcnt(0)\n"
"v_lshl_or_b32 v238, v227, 16, v226\n"
"v_lshl_or_b32 v239, v229, 16, v228\n"
"v_lshl_or_b32 v240, v231, 16, v230\n"
"v_lshl_or_b32 v241, v233, 16, v232\n"
"s_nop 1\n"
"v_mfma_f32_16x16x32_bf16 v[180:183], v[238:241], v[72:75], v[180:183]\n"
"v_mfma_f32_16x16x32_bf16 v[184:187], v[238:241], v[76:79], v[184:187]\n"
"v_mfma_f32_16x16x32_bf16 v[188:191], v[238:241], v[80:83], v[188:191]\n"
"s_nop 7\n"
"s_nop 7\n"
"s_add_u32 s21, s21, 0x2000\n"
"s_cmp_lt_u32 s21, 0x220000\n"
"s_cbranch_scc1 Lp21_%=\n"
"s_branch Lback1_%=\n"
"LspinB_%=:\n"
"s_mov_b32 s21, 0\n"
"LspB_%=:\n"
"buffer_load_dwordx2 v[232:233], v225, s[68:71], s92 offen offset:2048 sc1\n"
"s_waitcnt vmcnt(0)\n"
"v_cmp_ne_u64 vcc, s[90:91], v[232:233]\n"
"s_nop 4\n"
"s_cbranch_vccz LokB_%=\n"
"s_sleep 2\n"
"s_add_u32 s21, s21, 1\n"
"s_cmp_lt_u32 s21, 0x20000\n"
"s_cbranch_scc1 LspB_%=\n"
"LokB_%=:\n"
"buffer_inv sc1\n"
"s_branch LretB_%=\n"
"LspinX_%=:\n"
"s_mov_b32 s21, 0\n"
"LspX_%=:\n"
"buffer_load_dwordx2 v[232:233], v225, s[68:71], s93 offen sc1\n"
"s_waitcnt vmcnt(0)\n"
"v_cmp_ne_u64 vcc, s[90:91], v[232:233]\n"
"s_nop 4\n"
"s_cbranch_vccz LokX_%=\n"
"s_sleep 2\n"
"s_add_u32 s21, s21, 1\n"
"s_cmp_lt_u32 s21, 0x20000\n"
"s_cbranch_scc1 LspX_%=\n"
"LokX_%=:\n"
"s_nop 0\n"
"s_branch LretX_%=\n"
"Lepo_%=:\n"
"v_lshlrev_b32 v225, 1, v223\n"
"s_mov_b32 s21, 0\n"
"Lep1_%=:\n"
"buffer_load_dwordx4 v[232:235], v225, s[68:71], 0 offen offset:2048 sc1\n"
"buffer_load_dwordx4 v[236:239], v225, s[68:71], 0 offen offset:2064 sc1\n"
"s_waitcnt vmcnt(0)\n"
"v_cmp_ne_u64 s[22:23], s[90:91], v[232:233]\n"
"v_cmp_ne_u64 s[24:25], s[90:91], v[234:235]\n"
"v_cmp_ne_u64 s[26:27], s[90:91], v[236:237]\n"
"v_cmp_ne_u64 s[28:29], s[90:91], v[238:239]\n"
"s_nop 4\n"
"s_or_b64 s[22:23], s[22:23], s[24:25]\n"
"s_or_b64 s[26:27], s[26:27], s[28:29]\n"
"s_or_b64 s[22:23], s[22:23], s[26:27]\n"
"s_cbranch_scc0 Lep2_%=\n"
"s_sleep 2\n"
"s_add_u32 s21, s21, 1\n"
"s_cmp_lt_u32 s21, 0x20000\n"
"s_cbranch_scc1 Lep1_%=\n"
"Lep2_%=:\n"
"s_mov_b32 s70, 8\n"
"v_mov_b32 v232, s90\n"
"v_mov_b32 v233, s91\n"
"buffer_store_dwordx2 v[232:233], v223, s[68:71], 0 offen sc1\n"
"s_branch Lepr_%=\n"
"Lend_%=:\n"
  :
  : [tid] "v"(tid), [bid] "s"(bid), [ka] "s"(__builtin_amdgcn_kernarg_segment_ptr())
  : "s4","s5","s6","s7","s8","s9","s10","s11","s12","s13","s14","s15","s16","s17","v0","v1","v2","v3","v4","v5","v6","v7","v8","v9","v10","v11","v12","v13","v14","v15","v16","v17","v18","v19","v20","v21","v22","v23","v24","v25","v26","v27","v28","v29","v30","v31","v32","v33","v34","v35","v36","v37","v38","v39","v40","v41","v42","v43","v44","v45","v46","v47","v48","v49","v50","v51","v52","v53","v54","v55","v56","v57","v58","v59","v60","v61","v62","v63","v64","v65","v66","v67","v68","v69","v70","v71","v72","v73","v74","v75","v76","v77","v78","v79","v80","v81","v82","v83","v84","v85","v86","v87","v88","v89","v90","v91","v92","v93","v94","v95","v96","v97","v98","v99","v100","v101","v102","v103","v104","v105","v106","v107","v108","v109","v110","v111","v112","v113","v114","v115","v116","v117","v118","v119","v120","v121","v122","v123","v124","v125","v126","v127","v128","v129","v130","v131","v132","v133","v134","v135","v136","v137","v138","v139","v140","v141","v142","v143","v144","v145","v146","v147","v148","v149","v150","v151","v152","v153","v154","v155","v156","v157","v158","v159","v160","v161","v162","v163","v164","v165","v166","v167","v168","v169","v170","v171","v172","v173","v174","v175","v176","v177","v178","v179","v180","v181","v182","v183","v184","v185","v186","v187","v188","v189","v190","v191","v192","v193","v194","v195","v196","v197","v198","v199","v200","v201","v202","v203","v204","v205","v206","v207","v208","v209","v210","v211","v212","v213","v214","v215","v216","v217","v218","v219","v220","v221","v222","v223","v224","v225","v226","v227","v228","v229","v230","v231","v232","v233","v234","v235","v236","v237","v238","v239","v240","v241","v242","v243","v244","v245","v246","v247","v248","v249","v250","v251","v253","a0","a1","a2","a3","a4","a5","a6","a7","a8","a9","a10","a11","a12","a13","a14","a15","a16","a17","a18","a19","a20","a21","a22","a23","a24","a25","a26","a27","a28","a29","a30","a31","a32","a33","a34","a35","a36","a37","a38","a39","a40","a41","a42","a43","a44","a45","a46","a47","a48","a49","a50","a51","a52","a53","a54","a55","a56","a57","a58","a59","a60","a61","a62","a63","a64","a65","a66","a67","a68","a69","a70","a71","a72","a73","a74","a75","a76","a77","a78","a79","a80","a81","a82","a83","a84","a85","a86","a87","a88","a89","a90","a91","a92","a93","a94","a95","a96","a97","a98","a99","a100","a101","a102","a103","a104","a105","a106","a107","a108","a109","a110","a111","a112","a113","a114","a115","a116","a117","a118","a119","a120","a121","a122","a123","a124","a125","a126","a127","a128","a129","a130","a131","a132","a133","a134","a135","a136","a137","a138","a139","a140","a141","a142","a143","a144","a145","a146","a147","a148","a149","a150","a151","a152","a153","a154","a155","a156","a157","a158","a159","a160","a161","a162","a163","a164","a165","a166","a167","a168","a169","a170","a171","a172","a173","a174","a175","a176","a177","a178","a179","a180","a181","a182","a183","a184","a185","a186","a187","a188","a189","a190","a191","a192","a193","a194","a195","a196","a197","a198","a199","a200","a201","a202","a203","a204","a205","a206","a207","a208","a209","a210","a211","a212","a213","a214","a215","a216","a217","a218","a219","a220","a221","a222","a223","a224","a225","a226","a227","a228","a229","a230","a231","a232","a233","a234","a235","a236","a237","a238","a239","a240","a241","a242","a243","a244","a245","a246","a247","a248","a249","a250","a251","a252","a253","a254","a255","s20","s21","s22","s23","s24","s25","s26","s27","s28","s29","s30","s31","s36","s37","s38","s39","s40","s41","s42","s43","s44","s45","s46","s47","s48","s49","s50","s51","s52","s53","s54","s55","s56","s57","s58","s59","s60","s61","s62","s63","s64","s65","s66","s67","s68","s69","s70","s71","s72","s73","s74","s75","s76","s77","s78","s79","s80","s81","s82","s83","s84","s85","s86","s87","s88","s89","s90","s91","s92","s93","s94","s95","s96","s97","s98","s99","s100","vcc","scc","memory");
}

extern "C" void kernel_launch(void* const* d_in, const int* in_sizes, int n_in, void* d_out, int out_size, void* d_ws, size_t ws_size, hipStream_t stream) {
  (void)in_sizes; (void)n_in; (void)out_size; (void)ws_size;
  (void)hipFuncSetAttribute((const void*)gqa_block_kernel, hipFuncAttributeMaxDynamicSharedMemorySize, 163840);
  gqa_block_kernel<<<256, 256, 163840, stream>>>(d_in[0], d_in[1], d_in[2], d_in[3], d_in[4], d_ws, d_out);
}
```
